```python
import torch

def _softmax(x, axis=-1):
    return torch.softmax(x, dim=axis)
def _gelu(x):
    return torch.nn.functional.gelu(x, approximate="tanh")
def _silu(x):
    return torch.nn.functional.silu(x)
def _sigmoid(x):
    return torch.sigmoid(x)
def _layernorm(x, axis=-1, eps=1e-5):
    m = x.mean(dim=axis, keepdim=True)
    v = x.var(dim=axis, keepdim=True, unbiased=False)
    return (x - m) / torch.sqrt(v + eps)
def _scatter_add(tgt, idx, src, axis=0):
    return tgt.clone().index_add_(axis, idx, src)
def _segment_sum(src, seg, n):
    out = torch.zeros((n,) + src.shape[1:], dtype=src.dtype)
    return out.index_add_(0, seg, src)
def _pad(x, pw):
    flat = tuple(v for pair in reversed(pw) for v in pair)
    return torch.nn.functional.pad(x, flat)

kernel_name = 'standalone__gqa_block_cached__near_ridge'

def setup_inputs():

    _w = (lambda a: (lambda t: t.to(torch.bfloat16) if t.is_floating_point() else t)(torch.as_tensor(a)))
    B, Sq, D = 16, 80, 576
    Hq, Hkv, Sk, Dh = 32, 16, 1024, 128
    assert Hq % Hkv == 0, (Hq, Hkv)
    return {
        "x": _w((lambda *s: torch.randn(*s, dtype=torch.bfloat16) * 0.8)(B, Sq, D)),
        "k_cache": _w((lambda *s: torch.randn(*s, dtype=torch.bfloat16) * 0.8)(B, Hkv, Sk, Dh)),
        "v_cache": _w((lambda *s: torch.randn(*s, dtype=torch.bfloat16) * 0.05)(B, Hkv, Sk, Dh)),
        "wq": _w((lambda *s: torch.randn(*s, dtype=torch.bfloat16) * 0.8)(D, Hq * Dh) * (D ** -0.5)),
        "wo": _w((lambda *s: torch.randn(*s, dtype=torch.bfloat16) * 0.05)(Hq * Dh, D) * ((Hq * Dh) ** -0.5)),
    }

def reference(x, k_cache, v_cache, wq, wo):
    B, Sq, D = x.shape
    Hq, Dh = 32, 128
    Hkv = k_cache.shape[1]
    rep = Hq // Hkv
    q = (lambda x, *d: x.permute(*d))((x @ wq).reshape(B, Sq, Hq, Dh), 0, 2, 1, 3)
    k = (lambda x, n, axis: torch.repeat_interleave(x, n, dim=axis))(k_cache, rep, axis=1)
    v = (lambda x, n, axis: torch.repeat_interleave(x, n, dim=axis))(v_cache, rep, axis=1)
    scores = torch.einsum("bhqd,bhkd->bhqk", q, k) * (Dh ** -0.5)
    p = _softmax(scores, axis=-1)
    o = torch.einsum("bhqk,bhkd->bhqd", p, v)
    return (lambda x, *d: x.permute(*d))(o, 0, 2, 1, 3).reshape(B, Sq, Hq * Dh) @ wo
```

```cpp
#include <hip/hip_runtime.h>
#include <hip/hip_bf16.h>
typedef __attribute__((ext_vector_type(8))) short bf16x8;
typedef __attribute__((ext_vector_type(4))) float f32x4;
typedef unsigned u32x4 __attribute__((ext_vector_type(4)));
typedef unsigned short u16;
constexpr int B = 16, SQ = 80, DM = 576, HQ = 32, HKV = 16, SK = 1024, DH = 128;
__device__ __forceinline__ u16 f2bf(float f) { __hip_bfloat16 h = __float2bfloat16(f); return *reinterpret_cast<u16*>(&h); }
__device__ __forceinline__ float bf2f(u16 u) { return __uint_as_float(((unsigned)u) << 16); }

template <int M, int N, int K>
__global__ __launch_bounds__(256) void gemm_k(const u16* __restrict__ A, const u16* __restrict__ W, u16* __restrict__ C, float scale) {
  __shared__ __attribute__((aligned(16))) u16 As[64][40];
  __shared__ __attribute__((aligned(16))) u16 Ws[64][40];
  const int tid = threadIdx.x, lane = tid & 63, wid = tid >> 6;
  const int m0 = blockIdx.y * 64, n0 = blockIdx.x * 64;
  const int wm = (wid >> 1) * 32, wn = (wid & 1) * 32;
  f32x4 acc[2][2] = {};
  for (int k0 = 0; k0 < K; k0 += 32) {
    { int r = tid >> 2, c = (tid & 3) * 8; *(u32x4*)&As[r][c] = *(const u32x4*)(A + (size_t)(m0 + r) * K + k0 + c); }
    { int r = tid >> 3, c = (tid & 7) * 8; u32x4 w = *(const u32x4*)(W + (size_t)(k0 + r) * N + n0 + c);
      const u16* wp = (const u16*)&w;
      #pragma unroll
      for (int j = 0; j < 8; ++j) Ws[c + j][r] = wp[j]; }
    __syncthreads();
    bf16x8 a[2], b[2];
    #pragma unroll
    for (int i = 0; i < 2; ++i) { a[i] = *(const bf16x8*)&As[wm + i * 16 + (lane & 15)][(lane >> 4) * 8]; b[i] = *(const bf16x8*)&Ws[wn + i * 16 + (lane & 15)][(lane >> 4) * 8]; }
    #pragma unroll
    for (int i = 0; i < 2; ++i)
      #pragma unroll
      for (int j = 0; j < 2; ++j) acc[i][j] = __builtin_amdgcn_mfma_f32_16x16x32_bf16(a[i], b[j], acc[i][j], 0, 0, 0);
    __syncthreads();
  }
  #pragma unroll
  for (int i = 0; i < 2; ++i)
    #pragma unroll
    for (int j = 0; j < 2; ++j)
      #pragma unroll
      for (int r = 0; r < 4; ++r) C[(size_t)(m0 + wm + i * 16 + (lane >> 4) * 4 + r) * N + n0 + wn + j * 16 + (lane & 15)] = f2bf(acc[i][j][r] * scale);
}

__global__ __launch_bounds__(640) void attn_k(const u16* __restrict__ Q, const u16* __restrict__ Kc, const u16* __restrict__ Vc, u16* __restrict__ O) {
  __shared__ __attribute__((aligned(16))) u16 Ks[32][136];
  __shared__ __attribute__((aligned(16))) u16 Vt[128][40];
  const int tid = threadIdx.x, lane = tid & 63, wid = tid >> 6;
  const int b = blockIdx.x / HKV, kvh = blockIdx.x % HKV;
  const int h = kvh * 2 + wid / 5, q0 = (wid % 5) * 16;
  const int i = lane & 15, g = lane >> 4;
  bf16x8 qf[4];
  #pragma unroll
  for (int s = 0; s < 4; ++s) qf[s] = *(const bf16x8*)(Q + (size_t)(b * SQ + q0 + i) * (HQ * DH) + h * DH + s * 32 + g * 8);
  const u16* Kp = Kc + (size_t)(b * HKV + kvh) * SK * DH; const u16* Vp = Vc + (size_t)(b * HKV + kvh) * SK * DH;
  f32x4 o[8] = {}; float m = -1e30f, l = 0.f;
  for (int t = 0; t < SK / 32; ++t) {
    if (tid < 512) { int r = tid >> 4, c = (tid & 15) * 8;
      *(u32x4*)&Ks[r][c] = *(const u32x4*)(Kp + (size_t)(t * 32 + r) * DH + c);
      u32x4 w = *(const u32x4*)(Vp + (size_t)(t * 32 + r) * DH + c); const u16* wp = (const u16*)&w;
      int slot = 8 * ((r & 15) >> 2) + (r >> 4) * 4 + (r & 3);
      #pragma unroll
      for (int j = 0; j < 8; ++j) Vt[c + j][slot] = wp[j]; }
    __syncthreads();
    f32x4 s0 = {}, s1 = {};
    #pragma unroll
    for (int s = 0; s < 4; ++s) {
      bf16x8 k0 = *(const bf16x8*)&Ks[i][s * 32 + g * 8], k1 = *(const bf16x8*)&Ks[16 + i][s * 32 + g * 8];
      s0 = __builtin_amdgcn_mfma_f32_16x16x32_bf16(k0, qf[s], s0, 0, 0, 0);
      s1 = __builtin_amdgcn_mfma_f32_16x16x32_bf16(k1, qf[s], s1, 0, 0, 0); }
    float mx = fmaxf(fmaxf(fmaxf(s0[0], s0[1]), fmaxf(s0[2], s0[3])), fmaxf(fmaxf(s1[0], s1[1]), fmaxf(s1[2], s1[3])));
    mx = fmaxf(mx, __shfl_xor(mx, 16)); mx = fmaxf(mx, __shfl_xor(mx, 32));
    float mn = fmaxf(m, mx), al = exp2f(m - mn); m = mn;
    float p[8]; float ps = 0.f;
    #pragma unroll
    for (int r = 0; r < 4; ++r) { p[r] = exp2f(s0[r] - mn); p[4 + r] = exp2f(s1[r] - mn); }
    bf16x8 pf;
    #pragma unroll
    for (int r = 0; r < 8; ++r) { ps += p[r]; pf[r] = (short)f2bf(p[r]); }
    ps += __shfl_xor(ps, 16); ps += __shfl_xor(ps, 32);
    l = l * al + ps;
    #pragma unroll
    for (int d = 0; d < 8; ++d) { o[d] *= al; bf16x8 vf = *(const bf16x8*)&Vt[d * 16 + i][g * 8];
      o[d] = __builtin_amdgcn_mfma_f32_16x16x32_bf16(vf, pf, o[d], 0, 0, 0); }
    __syncthreads();
  }
  float il = 1.f / l;
  #pragma unroll
  for (int d = 0; d < 8; ++d)
    #pragma unroll
    for (int r = 0; r < 4; ++r) O[(size_t)(b * SQ + q0 + i) * (HQ * DH) + h * DH + d * 16 + g * 4 + r] = f2bf(o[d][r] * il);
}
#define NPROBE 0
__global__ __launch_bounds__(256) void probe(const u32x4* __restrict__ k, const u32x4* __restrict__ v, unsigned* __restrict__ out) {
  const int wg = blockIdx.x;
  const u32x4* kp = k + (size_t)wg * (256 * 128 * 2 / 16);
  const u32x4* vp = v + (size_t)wg * (256 * 128 * 2 / 16);
  u32x4 acc = {0, 0, 0, 0};
  #pragma unroll 1
  for (int i = threadIdx.x; i < 256 * 128 * 2 / 16; i += 256 * 8) {
    u32x4 a[8], b[8];
    #pragma unroll
    for (int j = 0; j < 8; ++j) { a[j] = kp[i + j * 256]; b[j] = vp[i + j * 256]; }
    #pragma unroll
    for (int j = 0; j < 8; ++j) { acc ^= a[j]; acc ^= b[j]; }
  }
  unsigned r = acc[0] ^ acc[1] ^ acc[2] ^ acc[3];
  if (r == 0x12345678u) out[wg * 256 + threadIdx.x] = r;
}
extern "C" void kernel_launch(void* const* d_in, const int* in_sizes, int n_in, void* d_out, int out_size, void* d_ws, size_t ws_size, hipStream_t stream) {
  const u16* x = (const u16*)d_in[0]; const u16* k = (const u16*)d_in[1]; const u16* v = (const u16*)d_in[2];
  const u16* wq = (const u16*)d_in[3]; const u16* wo = (const u16*)d_in[4];
  u16* Q = (u16*)d_ws; u16* O = Q + (size_t)B * SQ * HQ * DH;
  for (int i = 0; i < NPROBE; ++i) probe<<<1024, 256, 0, stream>>>((const u32x4*)k, (const u32x4*)v, (unsigned*)((char*)d_ws + (64 << 20)));
  gemm_k<B * SQ, HQ * DH, DM><<<dim3(HQ * DH / 64, B * SQ / 64), 256, 0, stream>>>(x, wq, Q, 0.08838834764831845f * 1.4426950408889634f);
  attn_k<<<B * HKV, 640, 0, stream>>>(Q, k, v, O);
  gemm_k<B * SQ, DM, HQ * DH><<<dim3(DM / 64, B * SQ / 64), 256, 0, stream>>>(O, wo, (u16*)d_out, 1.0f);
}
```

```cpp
#define VAR 1
#define DEPTH 32
#define NTS "nt"
#include <hip/hip_runtime.h>
#include <hip/hip_bf16.h>
typedef __attribute__((ext_vector_type(8))) short bf16x8;
typedef __attribute__((ext_vector_type(4))) float f32x4;
typedef unsigned u32x4 __attribute__((ext_vector_type(4)));
typedef unsigned short u16;
constexpr int B = 16, SQ = 80, DM = 576, HQ = 32, HKV = 16, SK = 1024, DH = 128;
__device__ __forceinline__ u16 f2bf(float f) { __hip_bfloat16 h = __float2bfloat16(f); return *reinterpret_cast<u16*>(&h); }
__device__ __forceinline__ float bf2f(u16 u) { return __uint_as_float(((unsigned)u) << 16); }

template <int M, int N, int K>
__global__ __launch_bounds__(256) void gemm_k(const u16* __restrict__ A, const u16* __restrict__ W, u16* __restrict__ C, float scale) {
  __shared__ __attribute__((aligned(16))) u16 As[64][40];
  __shared__ __attribute__((aligned(16))) u16 Ws[64][40];
  const int tid = threadIdx.x, lane = tid & 63, wid = tid >> 6;
  const int m0 = blockIdx.y * 64, n0 = blockIdx.x * 64;
  const int wm = (wid >> 1) * 32, wn = (wid & 1) * 32;
  f32x4 acc[2][2] = {};
  for (int k0 = 0; k0 < K; k0 += 32) {
    { int r = tid >> 2, c = (tid & 3) * 8; *(u32x4*)&As[r][c] = *(const u32x4*)(A + (size_t)(m0 + r) * K + k0 + c); }
    { int r = tid >> 3, c = (tid & 7) * 8; u32x4 w = *(const u32x4*)(W + (size_t)(k0 + r) * N + n0 + c);
      const u16* wp = (const u16*)&w;
      #pragma unroll
      for (int j = 0; j < 8; ++j) Ws[c + j][r] = wp[j]; }
    __syncthreads();
    bf16x8 a[2], b[2];
    #pragma unroll
    for (int i = 0; i < 2; ++i) { a[i] = *(const bf16x8*)&As[wm + i * 16 + (lane & 15)][(lane >> 4) * 8]; b[i] = *(const bf16x8*)&Ws[wn + i * 16 + (lane & 15)][(lane >> 4) * 8]; }
    #pragma unroll
    for (int i = 0; i < 2; ++i)
      #pragma unroll
      for (int j = 0; j < 2; ++j) acc[i][j] = __builtin_amdgcn_mfma_f32_16x16x32_bf16(a[i], b[j], acc[i][j], 0, 0, 0);
    __syncthreads();
  }
  #pragma unroll
  for (int i = 0; i < 2; ++i)
    #pragma unroll
    for (int j = 0; j < 2; ++j)
      #pragma unroll
      for (int r = 0; r < 4; ++r) C[(size_t)(m0 + wm + i * 16 + (lane >> 4) * 4 + r) * N + n0 + wn + j * 16 + (lane & 15)] = f2bf(acc[i][j][r] * scale);
}

__global__ __launch_bounds__(640) void attn_k(const u16* __restrict__ Q, const u16* __restrict__ Kc, const u16* __restrict__ Vc, u16* __restrict__ O) {
  __shared__ __attribute__((aligned(16))) u16 Ks[32][136];
  __shared__ __attribute__((aligned(16))) u16 Vt[128][40];
  const int tid = threadIdx.x, lane = tid & 63, wid = tid >> 6;
  const int b = blockIdx.x / HKV, kvh = blockIdx.x % HKV;
  const int h = kvh * 2 + wid / 5, q0 = (wid % 5) * 16;
  const int i = lane & 15, g = lane >> 4;
  bf16x8 qf[4];
  #pragma unroll
  for (int s = 0; s < 4; ++s) qf[s] = *(const bf16x8*)(Q + (size_t)(b * SQ + q0 + i) * (HQ * DH) + h * DH + s * 32 + g * 8);
  const u16* Kp = Kc + (size_t)(b * HKV + kvh) * SK * DH; const u16* Vp = Vc + (size_t)(b * HKV + kvh) * SK * DH;
  f32x4 o[8] = {}; float m = -1e30f, l = 0.f;
  for (int t = 0; t < SK / 32; ++t) {
    if (tid < 512) { int r = tid >> 4, c = (tid & 15) * 8;
      *(u32x4*)&Ks[r][c] = *(const u32x4*)(Kp + (size_t)(t * 32 + r) * DH + c);
      u32x4 w = *(const u32x4*)(Vp + (size_t)(t * 32 + r) * DH + c); const u16* wp = (const u16*)&w;
      int slot = 8 * ((r & 15) >> 2) + (r >> 4) * 4 + (r & 3);
      #pragma unroll
      for (int j = 0; j < 8; ++j) Vt[c + j][slot] = wp[j]; }
    __syncthreads();
    f32x4 s0 = {}, s1 = {};
    #pragma unroll
    for (int s = 0; s < 4; ++s) {
      bf16x8 k0 = *(const bf16x8*)&Ks[i][s * 32 + g * 8], k1 = *(const bf16x8*)&Ks[16 + i][s * 32 + g * 8];
      s0 = __builtin_amdgcn_mfma_f32_16x16x32_bf16(k0, qf[s], s0, 0, 0, 0);
      s1 = __builtin_amdgcn_mfma_f32_16x16x32_bf16(k1, qf[s], s1, 0, 0, 0); }
    float mx = fmaxf(fmaxf(fmaxf(s0[0], s0[1]), fmaxf(s0[2], s0[3])), fmaxf(fmaxf(s1[0], s1[1]), fmaxf(s1[2], s1[3])));
    mx = fmaxf(mx, __shfl_xor(mx, 16)); mx = fmaxf(mx, __shfl_xor(mx, 32));
    float mn = fmaxf(m, mx), al = exp2f(m - mn); m = mn;
    float p[8]; float ps = 0.f;
    #pragma unroll
    for (int r = 0; r < 4; ++r) { p[r] = exp2f(s0[r] - mn); p[4 + r] = exp2f(s1[r] - mn); }
    bf16x8 pf;
    #pragma unroll
    for (int r = 0; r < 8; ++r) { ps += p[r]; pf[r] = (short)f2bf(p[r]); }
    ps += __shfl_xor(ps, 16); ps += __shfl_xor(ps, 32);
    l = l * al + ps;
    #pragma unroll
    for (int d = 0; d < 8; ++d) { o[d] *= al; bf16x8 vf = *(const bf16x8*)&Vt[d * 16 + i][g * 8];
      o[d] = __builtin_amdgcn_mfma_f32_16x16x32_bf16(vf, pf, o[d], 0, 0, 0); }
    __syncthreads();
  }
  float il = 1.f / l;
  #pragma unroll
  for (int d = 0; d < 8; ++d)
    #pragma unroll
    for (int r = 0; r < 4; ++r) O[(size_t)(b * SQ + q0 + i) * (HQ * DH) + h * DH + d * 16 + g * 4 + r] = f2bf(o[d][r] * il);
}

#define AMP 40
__device__ __forceinline__ unsigned long long rt() { return __builtin_amdgcn_s_memrealtime(); }
__global__ __launch_bounds__(256) void probe0(const u32x4* __restrict__ k, const u32x4* __restrict__ v, unsigned long long* __restrict__ st) {
  unsigned long long t0 = rt();
  const int wg = blockIdx.x;
  const u32x4* kp = k + (size_t)wg * (256 * 128 * 2 / 16);
  const u32x4* vp = v + (size_t)wg * (256 * 128 * 2 / 16);
  u32x4 acc = {0, 0, 0, 0};
  #pragma unroll 1
  for (int i = threadIdx.x; i < 256 * 128 * 2 / 16; i += 256 * 8) {
    u32x4 a[8], b[8];
    #pragma unroll
    for (int j = 0; j < 8; ++j) { a[j] = kp[i + j * 256]; b[j] = vp[i + j * 256]; }
    #pragma unroll
    for (int j = 0; j < 8; ++j) { acc ^= a[j]; acc ^= b[j]; }
  }
  unsigned r = acc[0] ^ acc[1] ^ acc[2] ^ acc[3];
  if (r == 0x12345678u) st[100000 + wg * 256 + threadIdx.x] = r;
  __syncthreads();
  unsigned long long t1 = rt();
  if (threadIdx.x == 0) { st[2 * wg] = t0; st[2 * wg + 1] = t1; }
}
__global__ __launch_bounds__(256) void probe1(const char* __restrict__ k, const char* __restrict__ v, unsigned long long* __restrict__ st) {
  __shared__ __attribute__((aligned(16))) char lds[163840];
  unsigned long long t0 = rt();
  const int wg = blockIdx.x, lane = threadIdx.x & 63;
  const int wid = __builtin_amdgcn_readfirstlane(threadIdx.x >> 6);
  const char* kp = k + (size_t)wg * (1024 * 256) + wid * 4096 + lane * 16;
  const char* vp = v + (size_t)wg * (1024 * 256) + wid * 4096 + lane * 16;
  unsigned base = (unsigned)(size_t)lds + wid * 40960;
  #pragma unroll 1
  for (int s = 0; s < 16; ++s) {
    unsigned dst = base + (s % 5) * 8192;
    #pragma unroll
    for (int j = 0; j < 4; ++j) {
      asm volatile("s_mov_b32 m0, %0\n\ts_nop 0\n\tglobal_load_lds_dwordx4 %1, off " NTS :: "s"(dst + j * 1024), "v"(kp + j * 1024) : "memory");
      asm volatile("s_mov_b32 m0, %0\n\ts_nop 0\n\tglobal_load_lds_dwordx4 %1, off " NTS :: "s"(dst + 4096 + j * 1024), "v"(vp + j * 1024) : "memory");
    }
    kp += 16384; vp += 16384;
    asm volatile("s_waitcnt vmcnt(%0)" :: "i"(DEPTH - 8) : "memory");
    __builtin_amdgcn_s_barrier();
  }
  asm volatile("s_waitcnt vmcnt(0)" ::: "memory");
  __builtin_amdgcn_s_barrier();
  unsigned long long t1 = rt();
  if (threadIdx.x == 0) { st[2 * wg] = t0; st[2 * wg + 1] = t1; }
}
__global__ __launch_bounds__(64) void spin(const unsigned long long* __restrict__ st, int n, unsigned long long* res) {
  unsigned long long lo = ~0ull, hi = 0;
  for (int i = threadIdx.x; i < n; i += 64) { unsigned long long a = st[2 * i], b = st[2 * i + 1]; lo = a < lo ? a : lo; hi = b > hi ? b : hi; }
  for (int o = 32; o; o >>= 1) { unsigned long long a = __shfl_xor(lo, o), b = __shfl_xor(hi, o); lo = a < lo ? a : lo; hi = b > hi ? b : hi; }
  unsigned long long d = hi - lo; if (d > 20000) d = 20000;
  if (threadIdx.x == 0) res[0] = d;
  unsigned long long t0 = rt();
  while (rt() - t0 < d * AMP) __builtin_amdgcn_s_sleep(4);
}
extern "C" void kernel_launch(void* const* d_in, const int* in_sizes, int n_in, void* d_out, int out_size, void* d_ws, size_t ws_size, hipStream_t stream) {
  const u16* x = (const u16*)d_in[0]; const u16* k = (const u16*)d_in[1]; const u16* v = (const u16*)d_in[2];
  const u16* wq = (const u16*)d_in[3]; const u16* wo = (const u16*)d_in[4];
  u16* Q = (u16*)d_ws; u16* O = Q + (size_t)B * SQ * HQ * DH;
  unsigned long long* st = (unsigned long long*)((char*)d_ws + (32 << 20));
  probe1<<<256, 256, 0, stream>>>((const char*)k, (const char*)v, st); const int n = 256;
  gemm_k<B * SQ, HQ * DH, DM><<<dim3(HQ * DH / 64, B * SQ / 64), 256, 0, stream>>>(x, wq, Q, 0.08838834764831845f * 1.4426950408889634f);
  attn_k<<<B * HKV, 640, 0, stream>>>(Q, k, v, O);
  gemm_k<B * SQ, DM, HQ * DH><<<dim3(DM / 64, B * SQ / 64), 256, 0, stream>>>(O, wo, (u16*)d_out, 1.0f);
  spin<<<1, 64, 0, stream>>>(st, n, st + 8192);
}
```

```cpp
#define WHAT 2
#define PF 0
#include <hip/hip_runtime.h>
#include <hip/hip_bf16.h>
typedef __attribute__((ext_vector_type(8))) short bf16x8;
typedef __attribute__((ext_vector_type(4))) float f32x4;
typedef unsigned u32x4 __attribute__((ext_vector_type(4)));
typedef unsigned short u16;
constexpr int B = 16, SQ = 80, DM = 576, HQ = 32, HKV = 16, SK = 1024, DH = 128;
__device__ __forceinline__ u16 f2bf(float f) { __hip_bfloat16 h = __float2bfloat16(f); return *reinterpret_cast<u16*>(&h); }
__device__ __forceinline__ float bf2f(u16 u) { return __uint_as_float(((unsigned)u) << 16); }

template <int M, int N, int K>
__global__ __launch_bounds__(256) void gemm_k(const u16* __restrict__ A, const u16* __restrict__ W, u16* __restrict__ C, float scale) {
  __shared__ __attribute__((aligned(16))) u16 As[64][40];
  __shared__ __attribute__((aligned(16))) u16 Ws[64][40];
  const int tid = threadIdx.x, lane = tid & 63, wid = tid >> 6;
  const int m0 = blockIdx.y * 64, n0 = blockIdx.x * 64;
  const int wm = (wid >> 1) * 32, wn = (wid & 1) * 32;
  f32x4 acc[2][2] = {};
  for (int k0 = 0; k0 < K; k0 += 32) {
    { int r = tid >> 2, c = (tid & 3) * 8; *(u32x4*)&As[r][c] = *(const u32x4*)(A + (size_t)(m0 + r) * K + k0 + c); }
    { int r = tid >> 3, c = (tid & 7) * 8; u32x4 w = *(const u32x4*)(W + (size_t)(k0 + r) * N + n0 + c);
      const u16* wp = (const u16*)&w;
      #pragma unroll
      for (int j = 0; j < 8; ++j) Ws[c + j][r] = wp[j]; }
    __syncthreads();
    bf16x8 a[2], b[2];
    #pragma unroll
    for (int i = 0; i < 2; ++i) { a[i] = *(const bf16x8*)&As[wm + i * 16 + (lane & 15)][(lane >> 4) * 8]; b[i] = *(const bf16x8*)&Ws[wn + i * 16 + (lane & 15)][(lane >> 4) * 8]; }
    #pragma unroll
    for (int i = 0; i < 2; ++i)
      #pragma unroll
      for (int j = 0; j < 2; ++j) acc[i][j] = __builtin_amdgcn_mfma_f32_16x16x32_bf16(a[i], b[j], acc[i][j], 0, 0, 0);
    __syncthreads();
  }
  #pragma unroll
  for (int i = 0; i < 2; ++i)
    #pragma unroll
    for (int j = 0; j < 2; ++j)
      #pragma unroll
      for (int r = 0; r < 4; ++r) C[(size_t)(m0 + wm + i * 16 + (lane >> 4) * 4 + r) * N + n0 + wn + j * 16 + (lane & 15)] = f2bf(acc[i][j][r] * scale);
}

__global__ __launch_bounds__(640) void attn_k(const u16* __restrict__ Q, const u16* __restrict__ Kc, const u16* __restrict__ Vc, u16* __restrict__ O) {
  __shared__ __attribute__((aligned(16))) u16 Ks[32][136];
  __shared__ __attribute__((aligned(16))) u16 Vt[128][40];
  const int tid = threadIdx.x, lane = tid & 63, wid = tid >> 6;
  const int b = blockIdx.x / HKV, kvh = blockIdx.x % HKV;
  const int h = kvh * 2 + wid / 5, q0 = (wid % 5) * 16;
  const int i = lane & 15, g = lane >> 4;
  bf16x8 qf[4];
  #pragma unroll
  for (int s = 0; s < 4; ++s) qf[s] = *(const bf16x8*)(Q + (size_t)(b * SQ + q0 + i) * (HQ * DH) + h * DH + s * 32 + g * 8);
  const u16* Kp = Kc + (size_t)(b * HKV + kvh) * SK * DH; const u16* Vp = Vc + (size_t)(b * HKV + kvh) * SK * DH;
  f32x4 o[8] = {}; float m = -1e30f, l = 0.f;
  for (int t = 0; t < SK / 32; ++t) {
    if (tid < 512) { int r = tid >> 4, c = (tid & 15) * 8;
      *(u32x4*)&Ks[r][c] = *(const u32x4*)(Kp + (size_t)(t * 32 + r) * DH + c);
      u32x4 w = *(const u32x4*)(Vp + (size_t)(t * 32 + r) * DH + c); const u16* wp = (const u16*)&w;
      int slot = 8 * ((r & 15) >> 2) + (r >> 4) * 4 + (r & 3);
      #pragma unroll
      for (int j = 0; j < 8; ++j) Vt[c + j][slot] = wp[j]; }
    __syncthreads();
    f32x4 s0 = {}, s1 = {};
    #pragma unroll
    for (int s = 0; s < 4; ++s) {
      bf16x8 k0 = *(const bf16x8*)&Ks[i][s * 32 + g * 8], k1 = *(const bf16x8*)&Ks[16 + i][s * 32 + g * 8];
      s0 = __builtin_amdgcn_mfma_f32_16x16x32_bf16(k0, qf[s], s0, 0, 0, 0);
      s1 = __builtin_amdgcn_mfma_f32_16x16x32_bf16(k1, qf[s], s1, 0, 0, 0); }
    float mx = fmaxf(fmaxf(fmaxf(s0[0], s0[1]), fmaxf(s0[2], s0[3])), fmaxf(fmaxf(s1[0], s1[1]), fmaxf(s1[2], s1[3])));
    mx = fmaxf(mx, __shfl_xor(mx, 16)); mx = fmaxf(mx, __shfl_xor(mx, 32));
    float mn = fmaxf(m, mx), al = exp2f(m - mn); m = mn;
    float p[8]; float ps = 0.f;
    #pragma unroll
    for (int r = 0; r < 4; ++r) { p[r] = exp2f(s0[r] - mn); p[4 + r] = exp2f(s1[r] - mn); }
    bf16x8 pf;
    #pragma unroll
    for (int r = 0; r < 8; ++r) { ps += p[r]; pf[r] = (short)f2bf(p[r]); }
    ps += __shfl_xor(ps, 16); ps += __shfl_xor(ps, 32);
    l = l * al + ps;
    #pragma unroll
    for (int d = 0; d < 8; ++d) { o[d] *= al; bf16x8 vf = *(const bf16x8*)&Vt[d * 16 + i][g * 8];
      o[d] = __builtin_amdgcn_mfma_f32_16x16x32_bf16(vf, pf, o[d], 0, 0, 0); }
    __syncthreads();
  }
  float il = 1.f / l;
  #pragma unroll
  for (int d = 0; d < 8; ++d)
    #pragma unroll
    for (int r = 0; r < 4; ++r) O[(size_t)(b * SQ + q0 + i) * (HQ * DH) + h * DH + d * 16 + g * 4 + r] = f2bf(o[d][r] * il);
}

#define R4(x) x x x x
#define R16(x) R4(R4(x))
#define R256(x) R16(R16(x))
#define R1024(x) R4(R256(x))
__device__ __forceinline__ unsigned long long rt() { return __builtin_amdgcn_s_memrealtime(); }
__global__ __launch_bounds__(256) void icache(unsigned long long* __restrict__ st, float* out) {
  __shared__ char lds[100000];
  float a = threadIdx.x, b = 1.0f;
  unsigned long long t;
  unsigned long long r0 = rt();
  #pragma unroll 1
  for (int pass = 0; pass < 2; ++pass) {
    asm volatile("s_memtime %0\n\ts_waitcnt lgkmcnt(0)" : "=s"(t) :: "memory");
    unsigned long long tt = t;
    unsigned long long ra = rt();
    asm volatile(".p2align 3\n\t" R4(R1024("v_fma_f32 %0, %0, %1, %1\n\t")) : "+v"(a) : "v"(b));
    asm volatile("s_memtime %0\n\ts_waitcnt lgkmcnt(0)" : "=s"(t) :: "memory");
    unsigned long long rb = rt();
    if (threadIdx.x == 0) { st[blockIdx.x * 8 + pass] = t - tt; st[blockIdx.x * 8 + 2 + pass] = rb - ra; }
  }
  unsigned long long r1 = rt();
  if (threadIdx.x == 0) { st[blockIdx.x * 8 + 4] = r0; st[blockIdx.x * 8 + 5] = r1; }
  if (a == 12345.678f) { out[0] = a; lds[threadIdx.x] = 1; out[1] = lds[threadIdx.x + 5]; }
}
__global__ __launch_bounds__(64) void spin(const unsigned long long* __restrict__ st) {
  unsigned long long sum = 0, lo = ~0ull, hi = 0;
  for (int i = threadIdx.x; i < 256; i += 64) { sum += st[8 * i + (WHAT == 0 ? 0 : 3)]; unsigned long long a = st[8 * i + 4]; lo = a < lo ? a : lo; hi = a > hi ? a : hi; }
  for (int o = 32; o; o >>= 1) { sum += __shfl_xor(sum, o); unsigned long long a = __shfl_xor(lo, o), b = __shfl_xor(hi, o); lo = a < lo ? a : lo; hi = b > hi ? b : hi; }
  unsigned long long d = WHAT == 0 ? sum / 256 * 5 : WHAT == 1 ? sum * 100 / 256 : (hi - lo) * 200;
  if (d > 1000000) d = 1000000;
  unsigned long long t0 = rt();
  while (rt() - t0 < d) __builtin_amdgcn_s_sleep(4);
}
extern "C" void kernel_launch(void* const* d_in, const int* in_sizes, int n_in, void* d_out, int out_size, void* d_ws, size_t ws_size, hipStream_t stream) {
  const u16* x = (const u16*)d_in[0]; const u16* k = (const u16*)d_in[1]; const u16* v = (const u16*)d_in[2];
  const u16* wq = (const u16*)d_in[3]; const u16* wo = (const u16*)d_in[4];
  u16* Q = (u16*)d_ws; u16* O = Q + (size_t)B * SQ * HQ * DH;
  unsigned long long* st = (unsigned long long*)((char*)d_ws + (32 << 20));
  icache<<<256, 256, 0, stream>>>(st, (float*)(st + 100000));
  gemm_k<B * SQ, HQ * DH, DM><<<dim3(HQ * DH / 64, B * SQ / 64), 256, 0, stream>>>(x, wq, Q, 0.08838834764831845f * 1.4426950408889634f);
  attn_k<<<B * HKV, 640, 0, stream>>>(Q, k, v, O);
  gemm_k<B * SQ, DM, HQ * DH><<<dim3(DM / 64, B * SQ / 64), 256, 0, stream>>>(O, wo, (u16*)d_out, 1.0f);
  spin<<<1, 64, 0, stream>>>(st);
}
```

```cpp
#include <hip/hip_runtime.h>
#include <hip/hip_bf16.h>
typedef unsigned short u16;
typedef __attribute__((ext_vector_type(8))) short bf16x8;
typedef __attribute__((ext_vector_type(4))) float f32x4;
typedef unsigned u32x4 __attribute__((ext_vector_type(4)));

__global__ __launch_bounds__(256, 1) void gqa_attn(const char* __restrict__ x, const char* __restrict__ k, const char* __restrict__ v,
                                                   const char* __restrict__ wq, const char* __restrict__ wo, char* out, char* ws) {
  __shared__ __attribute__((aligned(16))) char lds[163840];
  const unsigned wg = blockIdx.x, xcd = wg & 7u, i = wg >> 3;
  const unsigned bl = i & 7u, kl = i >> 3, b = (xcd & 1u) * 8u + bl, kvh = (xcd >> 1) * 4u + kl;
  unsigned rot = (((bl + kl) % 3u) * 3u + xcd) % 9u;
  rot |= ((bl + kl) & 3u) << 4 | (115) << 8 | (800) << 16; rot = __builtin_amdgcn_readfirstlane(rot);
  const unsigned wave = __builtin_amdgcn_readfirstlane(threadIdx.x >> 6);
  const char* xb = x + (size_t)b * (80 * 576 * 2);
  const char* kb = k + (size_t)(b * 16u + kvh) * (1024 * 128 * 2);
  const char* vb = v + (size_t)(b * 16u + kvh) * (1024 * 128 * 2);
  const char* wqb = wq + kvh * 512u;
  const unsigned ob = (b * 16u + kvh) | (bl) << 8;
  const char* wob = wo + (size_t)kvh * (256 * 1152);
  char* outb = out + (size_t)b * (80 * 1152);
  const char* ep = ws + 25166208 + b * 8192u;
  asm volatile(
    ".p2align 3\n"
    "s_memrealtime s[78:79]\n"
    "s_load_dwordx2 s[96:97], %12, 0x0\n"
    "v_mbcnt_lo_u32_b32 v154, -1, 0\n"
    "s_mov_b32 s60, %5\n"
    "s_mov_b32 s61, %6\n"
    "v_mbcnt_hi_u32_b32 v154, -1, v154\n"
    "s_mov_b64 s[44:45], %0\n"
    "s_mov_b32 s46, 0x16800\n"
    "s_mov_b32 s47, 0x20000\n"
    "s_mov_b64 s[48:49], %3\n"
    "s_mov_b32 s50, 0x47e200\n"
    "s_mov_b32 s51, 0x20000\n"
    "v_lshrrev_b32_e64 v156, 3, v154\n"
    "v_and_b32_e64 v157, 7, v154\n"
    "v_lshlrev_b32_e32 v155, 4, v154\n"
    "s_getpc_b64 s[56:57]\n"
    "s_and_b32 s56, s56, 0xffffff80\n"
    "s_mov_b32 s58, 0xb6c0\n"
    "s_mov_b32 s59, 0x20000\n"
    "s_lshr_b32 s67, %8, 3\n"
    "s_lshl_b32 s67, s67, 2\n"
    "s_add_u32 s67, s67, s60\n"
    "s_and_b32 s67, s67, 63\n"
    "s_lshl_b32 s67, s67, 10\n"
    "v_add_u32_e32 v159, s67, v155\n"
    "buffer_load_dwordx4 a[252:255], v159, s[56:59], 0 offen\n"
    "s_mul_i32 s67, s60, 20\n"
    "v_add_u32_e32 v158, s67, v156\n"
    "v_and_b32_e64 v159, 6, v158\n"
    "v_xor_b32_e64 v159, v159, v157\n"
    "v_lshlrev_b32_e64 v159, 4, v159\n"
    "s_mov_b32 s68, 0x480\n"
    "v_mad_u32_u24 v250, v158, s68, v159\n"
    "v_add_u32_e32 v251, 0x2000, v250\n"
    "v_add_u32_e32 v252, 0x4000, v250\n"
    "s_lshl_b32 s64, s61, 7\n"
    "s_and_b32 s64, s64, 0x7ff\n"
    "s_mul_i32 s71, s60, 0xa00\n"
    "v_bfe_u32 v158, v154, 4, 1\n"
    "v_lshlrev_b32_e32 v158, 1, v158\n"
    "v_xor_b32_e64 v158, v158, v157\n"
    "v_lshlrev_b32_e64 v158, 4, v158\n"
    "v_lshl_add_u32 v246, v156, 13, v158\n"
    "v_xor_b32_e64 v158, 64, v246\n"
    "v_add_u32_e32 v247, 0xfc00, v158\n"
    "v_add_u32_e32 v248, 0x1f800, v246\n"
    "v_add_u32_e32 v249, 0x2f400, v158\n"
    "s_lshl_b32 s77, s60, 7\n"
    "s_lshl_b32 s70, s64, 12\n"
    "s_add_u32 s70, s70, s77\n"
    "s_lshl_b32 s63, s60, 12\n"
    "s_bfe_u32 s61, s61, 0x40004\n"
    "s_mov_b64 s[36:37], %1\n"
    "s_sub_u32 s36, s36, 0x400\n"
    "s_subb_u32 s37, s37, 0\n"
    "s_mov_b32 s38, 0x40400\n"
    "s_mov_b32 s39, 0x20000\n"
    "s_mov_b64 s[40:41], %2\n"
    "s_sub_u32 s40, s40, 0x400\n"
    "s_subb_u32 s41, s41, 0\n"
    "s_mov_b32 s42, 0x40400\n"
    "s_mov_b32 s43, 0x20000\n"
    "v_and_b32_e64 v158, 6, v156\n"
    "v_xor_b32_e64 v158, v158, v157\n"
    "v_lshlrev_b32_e64 v158, 4, v158\n"
    "v_lshl_add_u32 v153, v156, 8, v158\n"
    "v_add_u32_e32 v152, 0x400, v153\n"
    "v_add_u32_e32 v153, 0x80, v153\n"
    "s_add_u32 m0, s71, 0x8000\n"
    "s_nop 0\n"
    "buffer_load_dwordx4 v250, s[44:47], s64 offen lds\n"
    "buffer_load_dwordx4 v251, s[44:47], s64 offen offset:1024 lds\n"
    "s_mov_b32 exec_hi, 0\n"
    "buffer_load_dwordx4 v252, s[44:47], s64 offen offset:2048 lds\n"
    "s_mov_b32 exec_hi, -1\n"
    "s_nop 0\n"
    "s_add_u32 m0, s63, 0x20000\n"
    "s_nop 0\n"
    "s_nop 0\n"
    "buffer_load_dwordx4 v246, s[48:51], s70 offen lds\n"
    "buffer_load_dwordx4 v247, s[48:51], s70 offen offset:1024 lds\n"
    "buffer_load_dwordx4 v248, s[48:51], s70 offen offset:2048 lds\n"
    "buffer_load_dwordx4 v249, s[48:51], s70 offen offset:3072 lds\n"
    "s_mov_b64 s[52:53], %4\n"
    "s_mov_b32 s54, 0x1900000\n"
    "s_mov_b32 s55, 0x20000\n"
    "s_lshr_b32 s65, s60, 1\n"
    "s_and_b32 s66, s60, 1\n"
    "s_mov_b32 s72, 0\n"
    "s_mov_b32 s76, 0x3e0293ee\n"
    "v_lshrrev_b32_e64 v156, 4, v154\n"
    "v_and_b32_e64 v157, 6, v154\n"
    "v_xor_b32_e64 v157, v157, v156\n"
    "v_lshlrev_b32_e64 v157, 4, v157\n"
    "v_and_b32_e64 v158, 7, v154\n"
    "v_lshl_add_u32 v157, v158, 7, v157\n"
    "v_bfe_u32 v158, v154, 3, 1\n"
    "v_lshl_add_u32 v159, v158, 10, v157\n"
    "v_add_u32_e32 v244, 0x8000, v159\n"
    "v_xor_b32_e64 v245, 64, v244\n"
    "v_lshl_add_u32 v159, v158, 11, v157\n"
    "s_lshl_b32 s67, s66, 13\n"
    "v_add_u32_e32 v140, s67, v159\n"
    "v_xor_b32_e64 v141, 64, v140\n"
    "v_xor_b32_e32 v142, 0x2000, v140\n"
    "v_xor_b32_e32 v143, 0x2000, v141\n"
    "v_bfe_u32 v156, v154, 2, 3\n"
    "v_and_b32_e64 v157, 6, v156\n"
    "v_lshlrev_b32_e64 v157, 4, v157\n"
    "v_and_b32_e64 v158, 3, v154\n"
    "v_lshl_add_u32 v157, v158, 3, v157\n"
    "v_lshl_add_u32 v157, v156, 7, v157\n"
    "v_lshrrev_b32_e64 v158, 5, v154\n"
    "v_lshl_add_u32 v157, v158, 11, v157\n"
    "s_add_u32 s67, s67, 0x10000\n"
    "v_add_u32_e64 v144, s67, v157\n"
    "v_xor_b32_e64 v145, 32, v144\n"
    "v_xor_b32_e64 v146, 64, v144\n"
    "v_xor_b32_e32 v147, 0x60, v144\n"
    "v_xor_b32_e32 v148, 0x2000, v144\n"
    "v_xor_b32_e32 v149, 0x2000, v145\n"
    "v_xor_b32_e32 v150, 0x2000, v146\n"
    "v_xor_b32_e32 v151, 0x2000, v147\n"
    "v_bfe_u32 v156, v154, 3, 2\n"
    "v_lshlrev_b32_e64 v156, 5, v156\n"
    "v_and_b32_e64 v157, 3, v154\n"
    "v_lshl_add_u32 v156, v157, 3, v156\n"
    "v_bfe_u32 v157, v154, 2, 2\n"
    "v_lshl_add_u32 v156, v157, 7, v156\n"
    "v_lshrrev_b32_e64 v157, 4, v154\n"
    "v_lshl_add_u32 v156, v157, 10, v156\n"
    "s_add_u32 s67, s63, 0x18000\n"
    "v_add_u32_e64 v240, s67, v156\n"
    "v_xor_b32_e64 v241, 32, v240\n"
    "v_xor_b32_e64 v242, 64, v240\n"
    "v_xor_b32_e32 v243, 0x60, v240\n"
    "s_mul_i32 s67, s65, 0x5000\n"
    "s_add_u32 s67, s67, 0x1e000\n"
    "s_lshl_b32 s68, s66, 11\n"
    "s_add_u32 s68, s68, s67\n"
    "v_and_b32_e64 v156, 15, v154\n"
    "v_lshlrev_b32_e64 v156, 4, v156\n"
    "v_lshrrev_b32_e64 v157, 5, v154\n"
    "v_lshl_add_u32 v156, v157, 8, v156\n"
    "v_bfe_u32 v157, v154, 4, 1\n"
    "v_lshl_add_u32 v156, v157, 3, v156\n"
    "v_add_u32_e64 v253, s68, v156\n"
    "s_mul_i32 s68, s66, 0x3000\n"
    "s_add_u32 s68, s68, s67\n"
    "v_add_u32_e32 v254, s68, v155\n"
    "s_add_u32 s68, s67, 0x2000\n"
    "v_add_u32_e64 v255, s68, v155\n"
    "v_mov_b32_e32 v156, 0x3f803f80\n"
    "v_accvgpr_write_b32 a60, v156\n"
    "v_accvgpr_write_b32 a61, v156\n"
    "v_accvgpr_write_b32 a62, v156\n"
    "v_accvgpr_write_b32 a63, v156\n"
    "s_add_u32 m0, s63, 0x24000\n"
    "s_add_u32 s70, s70, 0x40000\n"
    "buffer_load_dwordx4 v246, s[48:51], s70 offen lds\n"
    "buffer_load_dwordx4 v247, s[48:51], s70 offen offset:1024 lds\n"
    "buffer_load_dwordx4 v248, s[48:51], s70 offen offset:2048 lds\n"
    "buffer_load_dwordx4 v249, s[48:51], s70 offen offset:3072 lds\n"
    "s_waitcnt lgkmcnt(0)\n"
    "s_bfe_u32 s62, %6, 0x80008\n"
    "s_lshr_b32 s67, %6, 16\n"
    "s_add_u32 s99, s78, s67\n"
    "s_sub_u32 s99, s99, s62\n"
    "s_mul_i32 s96, s96, 0x9e3779b1\n"
    "s_add_u32 s96, s96, 0x7f4a7c15\n"
    "s_mul_i32 s97, s97, 0x85ebca6b\n"
    "s_add_u32 s97, s97, 0x165667b1\n"
    "s_waitcnt vmcnt(4)\n"
    "s_barrier\n"
    "ds_read_b128 a[64:67], v244\n"
    "ds_read_b64_tr_b16 a[192:193], v240 offset:32768\n"
    "ds_read_b64_tr_b16 a[194:195], v240 offset:33280\n"
    "ds_read_b128 a[68:71], v244 offset:2048\n"
    "ds_read_b128 a[72:75], v244 offset:4096\n"
    "ds_read_b128 a[76:79], v244 offset:6144\n"
    "ds_read_b128 a[80:83], v244 offset:8192\n"
    "ds_read_b64_tr_b16 a[196:197], v241 offset:32768\n"
    "ds_read_b64_tr_b16 a[198:199], v241 offset:33280\n"
    "ds_read_b64_tr_b16 a[200:201], v242 offset:32768\n"
    "ds_read_b64_tr_b16 a[202:203], v242 offset:33280\n"
    "ds_read_b64_tr_b16 a[204:205], v243 offset:32768\n"
    "ds_read_b64_tr_b16 a[206:207], v243 offset:33280\n"
    "s_add_u32 m0, s71, 0xa800\n"
    "s_add_u32 s64, s64, 0x80\n"
    "s_cmp_ge_u32 s64, 0x480\n"
    "s_cselect_b32 s64, 0, s64\n"
    "buffer_load_dwordx4 v250, s[44:47], s64 offen lds\n"
    "buffer_load_dwordx4 v251, s[44:47], s64 offen offset:1024 lds\n"
    "s_mov_b32 exec_hi, 0\n"
    "buffer_load_dwordx4 v252, s[44:47], s64 offen offset:2048 lds\n"
    "s_mov_b32 exec_hi, -1\n"
    "s_nop 0\n"
    "s_add_u32 m0, s63, 0x18000\n"
    "s_add_u32 s70, s70, 0x40000\n"
    "s_cmp_ge_u32 s70, 0x480000\n"
    "s_cselect_b32 s70, s77, s70\n"
    "s_nop 0\n"
    "buffer_load_dwordx4 v246, s[48:51], s70 offen lds\n"
    "buffer_load_dwordx4 v247, s[48:51], s70 offen offset:1024 lds\n"
    "buffer_load_dwordx4 v248, s[48:51], s70 offen offset:2048 lds\n"
    "buffer_load_dwordx4 v249, s[48:51], s70 offen offset:3072 lds\n"
    "s_waitcnt vmcnt(7) lgkmcnt(0)\n"
    "s_nop 0\n"
    "v_mfma_f32_16x16x32_bf16 v[160:163], a[192:195], a[64:67], 0\n"
    "ds_read_b128 a[84:87], v245\n"
    "v_mfma_f32_16x16x32_bf16 v[164:167], a[192:195], a[68:71], 0\n"
    "ds_read_b64_tr_b16 a[208:209], v240 offset:49152\n"
    "s_add_u32 m0, s63, 0x1c000\n"
    "v_mfma_f32_16x16x32_bf16 v[168:171], a[192:195], a[72:75], 0\n"
    "ds_read_b64_tr_b16 a[210:211], v240 offset:49664\n"
    "s_add_u32 s70, s70, 0x40000\n"
    "buffer_load_dwordx4 v246, s[48:51], s70 offen lds\n"
    "v_mfma_f32_16x16x32_bf16 v[172:175], a[192:195], a[76:79], 0\n"
    "ds_read_b128 a[88:91], v245 offset:2048\n"
    "buffer_load_dwordx4 v247, s[48:51], s70 offen offset:1024 lds\n"
    "v_mfma_f32_16x16x32_bf16 v[176:179], a[192:195], a[80:83], 0\n"
    "ds_read_b128 a[92:95], v245 offset:4096\n"
    "buffer_load_dwordx4 v248, s[48:51], s70 offen offset:2048 lds\n"
    "v_mfma_f32_16x16x32_bf16 v[180:183], a[196:199], a[64:67], 0\n"
    "ds_read_b128 a[96:99], v245 offset:6144\n"
    "buffer_load_dwordx4 v249, s[48:51], s70 offen offset:3072 lds\n"
    "v_mfma_f32_16x16x32_bf16 v[184:187], a[196:199], a[68:71], 0\n"
    "ds_read_b128 a[100:103], v245 offset:8192\n"
    "s_add_u32 m0, s71, 0xd000\n"
    "v_mfma_f32_16x16x32_bf16 v[188:191], a[196:199], a[72:75], 0\n"
    "ds_read_b64_tr_b16 a[212:213], v241 offset:49152\n"
    "s_add_u32 s64, s64, 0x80\n"
    "v_mfma_f32_16x16x32_bf16 v[192:195], a[196:199], a[76:79], 0\n"
    "ds_read_b64_tr_b16 a[214:215], v241 offset:49664\n"
    "s_cmp_ge_u32 s64, 0x480\n"
    "s_cselect_b32 s64, 0, s64\n"
    "s_nop 0\n"
    "v_mfma_f32_16x16x32_bf16 v[196:199], a[196:199], a[80:83], 0\n"
    "ds_read_b64_tr_b16 a[216:217], v242 offset:49152\n"
    "buffer_load_dwordx4 v250, s[44:47], s64 offen lds\n"
    "v_mfma_f32_16x16x32_bf16 v[200:203], a[200:203], a[64:67], 0\n"
    "ds_read_b64_tr_b16 a[218:219], v242 offset:49664\n"
    "v_mfma_f32_16x16x32_bf16 v[204:207], a[200:203], a[68:71], 0\n"
    "ds_read_b64_tr_b16 a[220:221], v243 offset:49152\n"
    "buffer_load_dwordx4 v251, s[44:47], s64 offen offset:1024 lds\n"
    "s_mov_b32 exec_hi, 0\n"
    "buffer_load_dwordx4 v252, s[44:47], s64 offen offset:2048 lds\n"
    "s_mov_b32 exec_hi, -1\n"
    "s_nop 0\n"
    "s_nop 0\n"
    "v_mfma_f32_16x16x32_bf16 v[208:211], a[200:203], a[72:75], 0\n"
    "ds_read_b64_tr_b16 a[222:223], v243 offset:49664\n"
    "s_add_u32 m0, s63, 0x20000\n"
    "v_mfma_f32_16x16x32_bf16 v[212:215], a[200:203], a[76:79], 0\n"
    "s_add_u32 s70, s70, 0x40000\n"
    "v_mov_b32_e64 v0, 0\n"
    "v_mfma_f32_16x16x32_bf16 v[216:219], a[200:203], a[80:83], 0\n"
    "s_cmp_ge_u32 s70, 0x480000\n"
    "v_mov_b32_e64 v1, 0\n"
    "v_mfma_f32_16x16x32_bf16 v[220:223], a[204:207], a[64:67], 0\n"
    "s_cselect_b32 s70, s77, s70\n"
    "v_mov_b32_e32 v2, 0\n"
    "v_mfma_f32_16x16x32_bf16 v[224:227], a[204:207], a[68:71], 0\n"
    "buffer_load_dwordx4 v246, s[48:51], s70 offen lds\n"
    "v_mov_b32_e64 v3, 0\n"
    "v_mfma_f32_16x16x32_bf16 v[228:231], a[204:207], a[72:75], 0\n"
    "buffer_load_dwordx4 v247, s[48:51], s70 offen offset:1024 lds\n"
    "buffer_load_dwordx4 v248, s[48:51], s70 offen offset:2048 lds\n"
    "v_mov_b32_e64 v4, 0\n"
    "v_mfma_f32_16x16x32_bf16 v[232:235], a[204:207], a[76:79], 0\n"
    "buffer_load_dwordx4 v249, s[48:51], s70 offen offset:3072 lds\n"
    "v_mov_b32_e64 v5, 0\n"
    "v_mfma_f32_16x16x32_bf16 v[236:239], a[204:207], a[80:83], 0\n"
    "v_mov_b32_e64 v6, 0\n"
    "s_waitcnt vmcnt(11) lgkmcnt(0)\n"
    "s_barrier\n"
    "v_mfma_f32_16x16x32_bf16 v[160:163], a[208:211], a[84:87], v[160:163]\n"
    "ds_read_b128 a[64:67], v244 offset:10240\n"
    "v_mfma_f32_16x16x32_bf16 v[164:167], a[208:211], a[88:91], v[164:167]\n"
    "ds_read_b64_tr_b16 a[192:193], v240\n"
    "s_add_u32 m0, s71, 0x8000\n"
    "v_mfma_f32_16x16x32_bf16 v[168:171], a[208:211], a[92:95], v[168:171]\n"
    "ds_read_b64_tr_b16 a[194:195], v240 offset:512\n"
    "s_add_u32 s64, s64, 0x80\n"
    "v_mfma_f32_16x16x32_bf16 v[172:175], a[208:211], a[96:99], v[172:175]\n"
    "ds_read_b128 a[68:71], v244 offset:12288\n"
    "s_cmp_ge_u32 s64, 0x480\n"
    "v_mfma_f32_16x16x32_bf16 v[176:179], a[208:211], a[100:103], v[176:179]\n"
    "ds_read_b128 a[72:75], v244 offset:14336\n"
    "v_mfma_f32_16x16x32_bf16 v[180:183], a[212:215], a[84:87], v[180:183]\n"
    "ds_read_b128 a[76:79], v244 offset:16384\n"
    "s_cselect_b32 s64, 0, s64\n"
    "s_nop 0\n"
    "v_mfma_f32_16x16x32_bf16 v[184:187], a[212:215], a[88:91], v[184:187]\n"
    "ds_read_b128 a[80:83], v244 offset:18432\n"
    "buffer_load_dwordx4 v250, s[44:47], s64 offen lds\n"
    "v_mfma_f32_16x16x32_bf16 v[188:191], a[212:215], a[92:95], v[188:191]\n"
    "ds_read_b64_tr_b16 a[196:197], v241\n"
    "v_mfma_f32_16x16x32_bf16 v[192:195], a[212:215], a[96:99], v[192:195]\n"
    "ds_read_b64_tr_b16 a[198:199], v241 offset:512\n"
    "buffer_load_dwordx4 v251, s[44:47], s64 offen offset:1024 lds\n"
    "v_mfma_f32_16x16x32_bf16 v[196:199], a[212:215], a[100:103], v[196:199]\n"
    "ds_read_b64_tr_b16 a[200:201], v242\n"
    "s_mov_b32 exec_hi, 0\n"
    "buffer_load_dwordx4 v252, s[44:47], s64 offen offset:2048 lds\n"
    "s_mov_b32 exec_hi, -1\n"
    "s_nop 0\n"
    "s_nop 0\n"
    "v_mfma_f32_16x16x32_bf16 v[200:203], a[216:219], a[84:87], v[200:203]\n"
    "ds_read_b64_tr_b16 a[202:203], v242 offset:512\n"
    "v_mfma_f32_16x16x32_bf16 v[204:207], a[216:219], a[88:91], v[204:207]\n"
    "ds_read_b64_tr_b16 a[204:205], v243\n"
    "s_add_u32 m0, s63, 0x24000\n"
    "v_mfma_f32_16x16x32_bf16 v[208:211], a[216:219], a[92:95], v[208:211]\n"
    "ds_read_b64_tr_b16 a[206:207], v243 offset:512\n"
    "s_add_u32 s70, s70, 0x40000\n"
    "v_mfma_f32_16x16x32_bf16 v[212:215], a[216:219], a[96:99], v[212:215]\n"
    "v_mov_b32_e64 v7, 0\n"
    "v_mfma_f32_16x16x32_bf16 v[216:219], a[216:219], a[100:103], v[216:219]\n"
    "buffer_load_dwordx4 v246, s[48:51], s70 offen lds\n"
    "v_mov_b32_e64 v8, 0\n"
    "v_mfma_f32_16x16x32_bf16 v[220:223], a[220:223], a[84:87], v[220:223]\n"
    "buffer_load_dwordx4 v247, s[48:51], s70 offen offset:1024 lds\n"
    "v_mov_b32_e64 v9, 0\n"
    "v_mfma_f32_16x16x32_bf16 v[224:227], a[220:223], a[88:91], v[224:227]\n"
    "v_mov_b32_e64 v10, 0\n"
    "v_mfma_f32_16x16x32_bf16 v[228:231], a[220:223], a[92:95], v[228:231]\n"
    "buffer_load_dwordx4 v248, s[48:51], s70 offen offset:2048 lds\n"
    "v_mov_b32_e64 v11, 0\n"
    "v_mfma_f32_16x16x32_bf16 v[232:235], a[220:223], a[96:99], v[232:235]\n"
    "buffer_load_dwordx4 v249, s[48:51], s70 offen offset:3072 lds\n"
    "v_mov_b32_e64 v12, 0\n"
    "v_mfma_f32_16x16x32_bf16 v[236:239], a[220:223], a[100:103], v[236:239]\n"
    "v_mov_b32_e32 v13, 0\n"
    "s_waitcnt vmcnt(14) lgkmcnt(0)\n"
    "v_mfma_f32_16x16x32_bf16 v[160:163], a[192:195], a[64:67], v[160:163]\n"
    "ds_read_b128 a[84:87], v245 offset:10240\n"
    "v_mfma_f32_16x16x32_bf16 v[164:167], a[192:195], a[68:71], v[164:167]\n"
    "ds_read_b64_tr_b16 a[208:209], v240 offset:16384\n"
    "s_add_u32 m0, s63, 0x18000\n"
    "v_mfma_f32_16x16x32_bf16 v[168:171], a[192:195], a[72:75], v[168:171]\n"
    "ds_read_b64_tr_b16 a[210:211], v240 offset:16896\n"
    "v_mfma_f32_16x16x32_bf16 v[172:175], a[192:195], a[76:79], v[172:175]\n"
    "ds_read_b128 a[88:91], v245 offset:12288\n"
    "s_add_u32 s70, s70, 0x40000\n"
    "v_mfma_f32_16x16x32_bf16 v[176:179], a[192:195], a[80:83], v[176:179]\n"
    "ds_read_b128 a[92:95], v245 offset:14336\n"
    "v_mfma_f32_16x16x32_bf16 v[180:183], a[196:199], a[64:67], v[180:183]\n"
    "ds_read_b128 a[96:99], v245 offset:16384\n"
    "s_cmp_ge_u32 s70, 0x480000\n"
    "v_mfma_f32_16x16x32_bf16 v[184:187], a[196:199], a[68:71], v[184:187]\n"
    "ds_read_b128 a[100:103], v245 offset:18432\n"
    "v_mfma_f32_16x16x32_bf16 v[188:191], a[196:199], a[72:75], v[188:191]\n"
    "ds_read_b64_tr_b16 a[212:213], v241 offset:16384\n"
    "v_mfma_f32_16x16x32_bf16 v[192:195], a[196:199], a[76:79], v[192:195]\n"
    "ds_read_b64_tr_b16 a[214:215], v241 offset:16896\n"
    "s_cselect_b32 s70, s77, s70\n"
    "s_nop 0\n"
    "v_mfma_f32_16x16x32_bf16 v[196:199], a[196:199], a[80:83], v[196:199]\n"
    "ds_read_b64_tr_b16 a[216:217], v242 offset:16384\n"
    "v_mfma_f32_16x16x32_bf16 v[200:203], a[200:203], a[64:67], v[200:203]\n"
    "ds_read_b64_tr_b16 a[218:219], v242 offset:16896\n"
    "buffer_load_dwordx4 v246, s[48:51], s70 offen lds\n"
    "v_mfma_f32_16x16x32_bf16 v[204:207], a[200:203], a[68:71], v[204:207]\n"
    "ds_read_b64_tr_b16 a[220:221], v243 offset:16384\n"
    "v_mfma_f32_16x16x32_bf16 v[208:211], a[200:203], a[72:75], v[208:211]\n"
    "ds_read_b64_tr_b16 a[222:223], v243 offset:16896\n"
    "buffer_load_dwordx4 v247, s[48:51], s70 offen offset:1024 lds\n"
    "v_mfma_f32_16x16x32_bf16 v[212:215], a[200:203], a[76:79], v[212:215]\n"
    "v_mov_b32_e64 v14, 0\n"
    "v_mfma_f32_16x16x32_bf16 v[216:219], a[200:203], a[80:83], v[216:219]\n"
    "v_mov_b32_e64 v15, 0\n"
    "v_mfma_f32_16x16x32_bf16 v[220:223], a[204:207], a[64:67], v[220:223]\n"
    "buffer_load_dwordx4 v248, s[48:51], s70 offen offset:2048 lds\n"
    "v_mov_b32_e64 v16, 0\n"
    "v_mfma_f32_16x16x32_bf16 v[224:227], a[204:207], a[68:71], v[224:227]\n"
    "v_mov_b32_e64 v17, 0\n"
    "v_mfma_f32_16x16x32_bf16 v[228:231], a[204:207], a[72:75], v[228:231]\n"
    "buffer_load_dwordx4 v249, s[48:51], s70 offen offset:3072 lds\n"
    "v_mov_b32_e64 v18, 0\n"
    "v_mfma_f32_16x16x32_bf16 v[232:235], a[204:207], a[76:79], v[232:235]\n"
    "v_mov_b32_e64 v19, 0\n"
    "v_mfma_f32_16x16x32_bf16 v[236:239], a[204:207], a[80:83], v[236:239]\n"
    "v_mov_b32_e64 v20, 0\n"
    "s_waitcnt vmcnt(11) lgkmcnt(0)\n"
    "s_barrier\n"
    "v_mfma_f32_16x16x32_bf16 v[160:163], a[208:211], a[84:87], v[160:163]\n"
    "ds_read_b128 a[64:67], v244 offset:20480\n"
    "v_mfma_f32_16x16x32_bf16 v[164:167], a[208:211], a[88:91], v[164:167]\n"
    "ds_read_b64_tr_b16 a[192:193], v240 offset:32768\n"
    "s_add_u32 m0, s71, 0xa800\n"
    "v_mfma_f32_16x16x32_bf16 v[168:171], a[208:211], a[92:95], v[168:171]\n"
    "ds_read_b64_tr_b16 a[194:195], v240 offset:33280\n"
    "s_add_u32 s64, s64, 0x80\n"
    "v_mfma_f32_16x16x32_bf16 v[172:175], a[208:211], a[96:99], v[172:175]\n"
    "ds_read_b128 a[68:71], v244 offset:22528\n"
    "s_cmp_ge_u32 s64, 0x480\n"
    "v_mfma_f32_16x16x32_bf16 v[176:179], a[208:211], a[100:103], v[176:179]\n"
    "ds_read_b128 a[72:75], v244 offset:24576\n"
    "v_mfma_f32_16x16x32_bf16 v[180:183], a[212:215], a[84:87], v[180:183]\n"
    "ds_read_b128 a[76:79], v244 offset:26624\n"
    "s_cselect_b32 s64, 0, s64\n"
    "s_nop 0\n"
    "v_mfma_f32_16x16x32_bf16 v[184:187], a[212:215], a[88:91], v[184:187]\n"
    "ds_read_b128 a[80:83], v244 offset:28672\n"
    "buffer_load_dwordx4 v250, s[44:47], s64 offen lds\n"
    "v_mfma_f32_16x16x32_bf16 v[188:191], a[212:215], a[92:95], v[188:191]\n"
    "ds_read_b64_tr_b16 a[196:197], v241 offset:32768\n"
    "v_mfma_f32_16x16x32_bf16 v[192:195], a[212:215], a[96:99], v[192:195]\n"
    "ds_read_b64_tr_b16 a[198:199], v241 offset:33280\n"
    "buffer_load_dwordx4 v251, s[44:47], s64 offen offset:1024 lds\n"
    "v_mfma_f32_16x16x32_bf16 v[196:199], a[212:215], a[100:103], v[196:199]\n"
    "ds_read_b64_tr_b16 a[200:201], v242 offset:32768\n"
    "s_mov_b32 exec_hi, 0\n"
    "buffer_load_dwordx4 v252, s[44:47], s64 offen offset:2048 lds\n"
    "s_mov_b32 exec_hi, -1\n"
    "s_nop 0\n"
    "s_nop 0\n"
    "v_mfma_f32_16x16x32_bf16 v[200:203], a[216:219], a[84:87], v[200:203]\n"
    "ds_read_b64_tr_b16 a[202:203], v242 offset:33280\n"
    "v_mfma_f32_16x16x32_bf16 v[204:207], a[216:219], a[88:91], v[204:207]\n"
    "ds_read_b64_tr_b16 a[204:205], v243 offset:32768\n"
    "s_add_u32 m0, s63, 0x1c000\n"
    "v_mfma_f32_16x16x32_bf16 v[208:211], a[216:219], a[92:95], v[208:211]\n"
    "ds_read_b64_tr_b16 a[206:207], v243 offset:33280\n"
    "s_add_u32 s70, s70, 0x40000\n"
    "v_mfma_f32_16x16x32_bf16 v[212:215], a[216:219], a[96:99], v[212:215]\n"
    "v_mov_b32_e64 v21, 0\n"
    "v_mfma_f32_16x16x32_bf16 v[216:219], a[216:219], a[100:103], v[216:219]\n"
    "buffer_load_dwordx4 v246, s[48:51], s70 offen lds\n"
    "v_mov_b32_e64 v22, 0\n"
    "v_mfma_f32_16x16x32_bf16 v[220:223], a[220:223], a[84:87], v[220:223]\n"
    "buffer_load_dwordx4 v247, s[48:51], s70 offen offset:1024 lds\n"
    "v_mov_b32_e64 v23, 0\n"
    "v_mfma_f32_16x16x32_bf16 v[224:227], a[220:223], a[88:91], v[224:227]\n"
    "v_mov_b32_e64 v24, 0\n"
    "v_mfma_f32_16x16x32_bf16 v[228:231], a[220:223], a[92:95], v[228:231]\n"
    "buffer_load_dwordx4 v248, s[48:51], s70 offen offset:2048 lds\n"
    "v_mov_b32_e64 v25, 0\n"
    "v_mfma_f32_16x16x32_bf16 v[232:235], a[220:223], a[96:99], v[232:235]\n"
    "buffer_load_dwordx4 v249, s[48:51], s70 offen offset:3072 lds\n"
    "v_mov_b32_e64 v26, 0\n"
    "v_mfma_f32_16x16x32_bf16 v[236:239], a[220:223], a[100:103], v[236:239]\n"
    "v_mov_b32_e32 v27, 0\n"
    "s_waitcnt vmcnt(11) lgkmcnt(0)\n"
    "v_mfma_f32_16x16x32_bf16 v[160:163], a[192:195], a[64:67], v[160:163]\n"
    "ds_read_b128 a[84:87], v245 offset:20480\n"
    "v_mfma_f32_16x16x32_bf16 v[164:167], a[192:195], a[68:71], v[164:167]\n"
    "ds_read_b64_tr_b16 a[208:209], v240 offset:49152\n"
    "s_add_u32 m0, s63, 0x20000\n"
    "v_mfma_f32_16x16x32_bf16 v[168:171], a[192:195], a[72:75], v[168:171]\n"
    "ds_read_b64_tr_b16 a[210:211], v240 offset:49664\n"
    "v_mfma_f32_16x16x32_bf16 v[172:175], a[192:195], a[76:79], v[172:175]\n"
    "ds_read_b128 a[88:91], v245 offset:22528\n"
    "s_add_u32 s70, s70, 0x40000\n"
    "v_mfma_f32_16x16x32_bf16 v[176:179], a[192:195], a[80:83], v[176:179]\n"
    "ds_read_b128 a[92:95], v245 offset:24576\n"
    "v_mfma_f32_16x16x32_bf16 v[180:183], a[196:199], a[64:67], v[180:183]\n"
    "ds_read_b128 a[96:99], v245 offset:26624\n"
    "s_cmp_ge_u32 s70, 0x480000\n"
    "v_mfma_f32_16x16x32_bf16 v[184:187], a[196:199], a[68:71], v[184:187]\n"
    "ds_read_b128 a[100:103], v245 offset:28672\n"
    "v_mfma_f32_16x16x32_bf16 v[188:191], a[196:199], a[72:75], v[188:191]\n"
    "ds_read_b64_tr_b16 a[212:213], v241 offset:49152\n"
    "v_mfma_f32_16x16x32_bf16 v[192:195], a[196:199], a[76:79], v[192:195]\n"
    "ds_read_b64_tr_b16 a[214:215], v241 offset:49664\n"
    "s_cselect_b32 s70, s77, s70\n"
    "s_nop 0\n"
    "v_mfma_f32_16x16x32_bf16 v[196:199], a[196:199], a[80:83], v[196:199]\n"
    "ds_read_b64_tr_b16 a[216:217], v242 offset:49152\n"
    "v_mfma_f32_16x16x32_bf16 v[200:203], a[200:203], a[64:67], v[200:203]\n"
    "ds_read_b64_tr_b16 a[218:219], v242 offset:49664\n"
    "buffer_load_dwordx4 v246, s[48:51], s70 offen lds\n"
    "v_mfma_f32_16x16x32_bf16 v[204:207], a[200:203], a[68:71], v[204:207]\n"
    "ds_read_b64_tr_b16 a[220:221], v243 offset:49152\n"
    "v_mfma_f32_16x16x32_bf16 v[208:211], a[200:203], a[72:75], v[208:211]\n"
    "ds_read_b64_tr_b16 a[222:223], v243 offset:49664\n"
    "buffer_load_dwordx4 v247, s[48:51], s70 offen offset:1024 lds\n"
    "v_mfma_f32_16x16x32_bf16 v[212:215], a[200:203], a[76:79], v[212:215]\n"
    "v_mov_b32_e64 v28, 0\n"
    "v_mfma_f32_16x16x32_bf16 v[216:219], a[200:203], a[80:83], v[216:219]\n"
    "v_mov_b32_e64 v29, 0\n"
    "v_mfma_f32_16x16x32_bf16 v[220:223], a[204:207], a[64:67], v[220:223]\n"
    "buffer_load_dwordx4 v248, s[48:51], s70 offen offset:2048 lds\n"
    "v_mov_b32_e64 v30, 0\n"
    "v_mfma_f32_16x16x32_bf16 v[224:227], a[204:207], a[68:71], v[224:227]\n"
    "v_mov_b32_e64 v31, 0\n"
    "v_mfma_f32_16x16x32_bf16 v[228:231], a[204:207], a[72:75], v[228:231]\n"
    "buffer_load_dwordx4 v249, s[48:51], s70 offen offset:3072 lds\n"
    "v_mov_b32_e64 v32, 0\n"
    "v_mfma_f32_16x16x32_bf16 v[232:235], a[204:207], a[76:79], v[232:235]\n"
    "v_mov_b32_e64 v33, 0\n"
    "v_mfma_f32_16x16x32_bf16 v[236:239], a[204:207], a[80:83], v[236:239]\n"
    "v_mov_b32_e64 v34, 0\n"
    "s_waitcnt vmcnt(11) lgkmcnt(0)\n"
    "s_barrier\n"
    "v_mfma_f32_16x16x32_bf16 v[160:163], a[208:211], a[84:87], v[160:163]\n"
    "ds_read_b128 a[64:67], v244\n"
    "v_mfma_f32_16x16x32_bf16 v[164:167], a[208:211], a[88:91], v[164:167]\n"
    "ds_read_b64_tr_b16 a[192:193], v240\n"
    "s_add_u32 m0, s71, 0xd000\n"
    "v_mfma_f32_16x16x32_bf16 v[168:171], a[208:211], a[92:95], v[168:171]\n"
    "ds_read_b64_tr_b16 a[194:195], v240 offset:512\n"
    "s_add_u32 s64, s64, 0x80\n"
    "v_mfma_f32_16x16x32_bf16 v[172:175], a[208:211], a[96:99], v[172:175]\n"
    "ds_read_b128 a[68:71], v244 offset:2048\n"
    "s_cmp_ge_u32 s64, 0x480\n"
    "v_mfma_f32_16x16x32_bf16 v[176:179], a[208:211], a[100:103], v[176:179]\n"
    "ds_read_b128 a[72:75], v244 offset:4096\n"
    "v_mfma_f32_16x16x32_bf16 v[180:183], a[212:215], a[84:87], v[180:183]\n"
    "ds_read_b128 a[76:79], v244 offset:6144\n"
    "s_cselect_b32 s64, 0, s64\n"
    "s_nop 0\n"
    "v_mfma_f32_16x16x32_bf16 v[184:187], a[212:215], a[88:91], v[184:187]\n"
    "ds_read_b128 a[80:83], v244 offset:8192\n"
    "buffer_load_dwordx4 v250, s[44:47], s64 offen lds\n"
    "v_mfma_f32_16x16x32_bf16 v[188:191], a[212:215], a[92:95], v[188:191]\n"
    "ds_read_b64_tr_b16 a[196:197], v241\n"
    "v_mfma_f32_16x16x32_bf16 v[192:195], a[212:215], a[96:99], v[192:195]\n"
    "ds_read_b64_tr_b16 a[198:199], v241 offset:512\n"
    "buffer_load_dwordx4 v251, s[44:47], s64 offen offset:1024 lds\n"
    "v_mfma_f32_16x16x32_bf16 v[196:199], a[212:215], a[100:103], v[196:199]\n"
    "ds_read_b64_tr_b16 a[200:201], v242\n"
    "s_mov_b32 exec_hi, 0\n"
    "buffer_load_dwordx4 v252, s[44:47], s64 offen offset:2048 lds\n"
    "s_mov_b32 exec_hi, -1\n"
    "s_nop 0\n"
    "s_nop 0\n"
    "v_mfma_f32_16x16x32_bf16 v[200:203], a[216:219], a[84:87], v[200:203]\n"
    "ds_read_b64_tr_b16 a[202:203], v242 offset:512\n"
    "v_mfma_f32_16x16x32_bf16 v[204:207], a[216:219], a[88:91], v[204:207]\n"
    "ds_read_b64_tr_b16 a[204:205], v243\n"
    "s_add_u32 m0, s63, 0x24000\n"
    "v_mfma_f32_16x16x32_bf16 v[208:211], a[216:219], a[92:95], v[208:211]\n"
    "ds_read_b64_tr_b16 a[206:207], v243 offset:512\n"
    "s_add_u32 s70, s70, 0x40000\n"
    "v_mfma_f32_16x16x32_bf16 v[212:215], a[216:219], a[96:99], v[212:215]\n"
    "v_mov_b32_e64 v35, 0\n"
    "v_mfma_f32_16x16x32_bf16 v[216:219], a[216:219], a[100:103], v[216:219]\n"
    "buffer_load_dwordx4 v246, s[48:51], s70 offen lds\n"
    "v_mov_b32_e64 v36, 0\n"
    "v_mfma_f32_16x16x32_bf16 v[220:223], a[220:223], a[84:87], v[220:223]\n"
    "buffer_load_dwordx4 v247, s[48:51], s70 offen offset:1024 lds\n"
    "v_mov_b32_e64 v37, 0\n"
    "v_mfma_f32_16x16x32_bf16 v[224:227], a[220:223], a[88:91], v[224:227]\n"
    "v_mov_b32_e64 v38, 0\n"
    "v_mfma_f32_16x16x32_bf16 v[228:231], a[220:223], a[92:95], v[228:231]\n"
    "buffer_load_dwordx4 v248, s[48:51], s70 offen offset:2048 lds\n"
    "v_mov_b32_e64 v39, 0\n"
    "v_mfma_f32_16x16x32_bf16 v[232:235], a[220:223], a[96:99], v[232:235]\n"
    "buffer_load_dwordx4 v249, s[48:51], s70 offen offset:3072 lds\n"
    "v_mov_b32_e64 v40, 0\n"
    "v_mfma_f32_16x16x32_bf16 v[236:239], a[220:223], a[100:103], v[236:239]\n"
    "v_mov_b32_e32 v41, 0\n"
    "s_waitcnt vmcnt(11) lgkmcnt(0)\n"
    "v_mfma_f32_16x16x32_bf16 v[160:163], a[192:195], a[64:67], v[160:163]\n"
    "ds_read_b128 a[84:87], v245\n"
    "v_mfma_f32_16x16x32_bf16 v[164:167], a[192:195], a[68:71], v[164:167]\n"
    "ds_read_b64_tr_b16 a[208:209], v240 offset:16384\n"
    "s_add_u32 m0, s63, 0x18000\n"
    "v_mfma_f32_16x16x32_bf16 v[168:171], a[192:195], a[72:75], v[168:171]\n"
    "ds_read_b64_tr_b16 a[210:211], v240 offset:16896\n"
    "v_mfma_f32_16x16x32_bf16 v[172:175], a[192:195], a[76:79], v[172:175]\n"
    "ds_read_b128 a[88:91], v245 offset:2048\n"
    "s_add_u32 s70, s70, 0x40000\n"
    "v_mfma_f32_16x16x32_bf16 v[176:179], a[192:195], a[80:83], v[176:179]\n"
    "ds_read_b128 a[92:95], v245 offset:4096\n"
    "v_mfma_f32_16x16x32_bf16 v[180:183], a[196:199], a[64:67], v[180:183]\n"
    "ds_read_b128 a[96:99], v245 offset:6144\n"
    "s_cmp_ge_u32 s70, 0x480000\n"
    "v_mfma_f32_16x16x32_bf16 v[184:187], a[196:199], a[68:71], v[184:187]\n"
    "ds_read_b128 a[100:103], v245 offset:8192\n"
    "v_mfma_f32_16x16x32_bf16 v[188:191], a[196:199], a[72:75], v[188:191]\n"
    "ds_read_b64_tr_b16 a[212:213], v241 offset:16384\n"
    "v_mfma_f32_16x16x32_bf16 v[192:195], a[196:199], a[76:79], v[192:195]\n"
    "ds_read_b64_tr_b16 a[214:215], v241 offset:16896\n"
    "s_cselect_b32 s70, s77, s70\n"
    "s_nop 0\n"
    "v_mfma_f32_16x16x32_bf16 v[196:199], a[196:199], a[80:83], v[196:199]\n"
    "ds_read_b64_tr_b16 a[216:217], v242 offset:16384\n"
    "v_mfma_f32_16x16x32_bf16 v[200:203], a[200:203], a[64:67], v[200:203]\n"
    "ds_read_b64_tr_b16 a[218:219], v242 offset:16896\n"
    "buffer_load_dwordx4 v246, s[48:51], s70 offen lds\n"
    "v_mfma_f32_16x16x32_bf16 v[204:207], a[200:203], a[68:71], v[204:207]\n"
    "ds_read_b64_tr_b16 a[220:221], v243 offset:16384\n"
    "v_mfma_f32_16x16x32_bf16 v[208:211], a[200:203], a[72:75], v[208:211]\n"
    "ds_read_b64_tr_b16 a[222:223], v243 offset:16896\n"
    "buffer_load_dwordx4 v247, s[48:51], s70 offen offset:1024 lds\n"
    "v_mfma_f32_16x16x32_bf16 v[212:215], a[200:203], a[76:79], v[212:215]\n"
    "v_mov_b32_e64 v42, 0\n"
    "v_mfma_f32_16x16x32_bf16 v[216:219], a[200:203], a[80:83], v[216:219]\n"
    "v_mov_b32_e64 v43, 0\n"
    "v_mfma_f32_16x16x32_bf16 v[220:223], a[204:207], a[64:67], v[220:223]\n"
    "buffer_load_dwordx4 v248, s[48:51], s70 offen offset:2048 lds\n"
    "v_mov_b32_e64 v44, 0\n"
    "v_mfma_f32_16x16x32_bf16 v[224:227], a[204:207], a[68:71], v[224:227]\n"
    "v_mov_b32_e64 v45, 0\n"
    "v_mfma_f32_16x16x32_bf16 v[228:231], a[204:207], a[72:75], v[228:231]\n"
    "buffer_load_dwordx4 v249, s[48:51], s70 offen offset:3072 lds\n"
    "v_mov_b32_e64 v46, 0\n"
    "v_mfma_f32_16x16x32_bf16 v[232:235], a[204:207], a[76:79], v[232:235]\n"
    "v_mov_b32_e64 v47, 0\n"
    "v_mfma_f32_16x16x32_bf16 v[236:239], a[204:207], a[80:83], v[236:239]\n"
    "v_mov_b32_e64 v48, 0\n"
    "s_waitcnt vmcnt(11) lgkmcnt(0)\n"
    "s_barrier\n"
    "v_mfma_f32_16x16x32_bf16 v[160:163], a[208:211], a[84:87], v[160:163]\n"
    "ds_read_b128 a[64:67], v244 offset:10240\n"
    "v_mfma_f32_16x16x32_bf16 v[164:167], a[208:211], a[88:91], v[164:167]\n"
    "ds_read_b64_tr_b16 a[192:193], v240 offset:32768\n"
    "s_add_u32 m0, s71, 0x8000\n"
    "v_mfma_f32_16x16x32_bf16 v[168:171], a[208:211], a[92:95], v[168:171]\n"
    "ds_read_b64_tr_b16 a[194:195], v240 offset:33280\n"
    "s_add_u32 s64, s64, 0x80\n"
    "v_mfma_f32_16x16x32_bf16 v[172:175], a[208:211], a[96:99], v[172:175]\n"
    "ds_read_b128 a[68:71], v244 offset:12288\n"
    "s_cmp_ge_u32 s64, 0x480\n"
    "v_mfma_f32_16x16x32_bf16 v[176:179], a[208:211], a[100:103], v[176:179]\n"
    "ds_read_b128 a[72:75], v244 offset:14336\n"
    "v_mfma_f32_16x16x32_bf16 v[180:183], a[212:215], a[84:87], v[180:183]\n"
    "ds_read_b128 a[76:79], v244 offset:16384\n"
    "s_cselect_b32 s64, 0, s64\n"
    "s_nop 0\n"
    "v_mfma_f32_16x16x32_bf16 v[184:187], a[212:215], a[88:91], v[184:187]\n"
    "ds_read_b128 a[80:83], v244 offset:18432\n"
    "buffer_load_dwordx4 v250, s[44:47], s64 offen lds\n"
    "v_mfma_f32_16x16x32_bf16 v[188:191], a[212:215], a[92:95], v[188:191]\n"
    "ds_read_b64_tr_b16 a[196:197], v241 offset:32768\n"
    "v_mfma_f32_16x16x32_bf16 v[192:195], a[212:215], a[96:99], v[192:195]\n"
    "ds_read_b64_tr_b16 a[198:199], v241 offset:33280\n"
    "buffer_load_dwordx4 v251, s[44:47], s64 offen offset:1024 lds\n"
    "v_mfma_f32_16x16x32_bf16 v[196:199], a[212:215], a[100:103], v[196:199]\n"
    "ds_read_b64_tr_b16 a[200:201], v242 offset:32768\n"
    "s_mov_b32 exec_hi, 0\n"
    "buffer_load_dwordx4 v252, s[44:47], s64 offen offset:2048 lds\n"
    "s_mov_b32 exec_hi, -1\n"
    "s_nop 0\n"
    "s_nop 0\n"
    "v_mfma_f32_16x16x32_bf16 v[200:203], a[216:219], a[84:87], v[200:203]\n"
    "ds_read_b64_tr_b16 a[202:203], v242 offset:33280\n"
    "v_mfma_f32_16x16x32_bf16 v[204:207], a[216:219], a[88:91], v[204:207]\n"
    "ds_read_b64_tr_b16 a[204:205], v243 offset:32768\n"
    "s_add_u32 m0, s63, 0x1c000\n"
    "v_mfma_f32_16x16x32_bf16 v[208:211], a[216:219], a[92:95], v[208:211]\n"
    "ds_read_b64_tr_b16 a[206:207], v243 offset:33280\n"
    "s_add_u32 s70, s70, 0x40000\n"
    "v_mfma_f32_16x16x32_bf16 v[212:215], a[216:219], a[96:99], v[212:215]\n"
    "v_mov_b32_e64 v49, 0\n"
    "v_mfma_f32_16x16x32_bf16 v[216:219], a[216:219], a[100:103], v[216:219]\n"
    "buffer_load_dwordx4 v246, s[48:51], s70 offen lds\n"
    "v_mov_b32_e64 v50, 0\n"
    "v_mfma_f32_16x16x32_bf16 v[220:223], a[220:223], a[84:87], v[220:223]\n"
    "buffer_load_dwordx4 v247, s[48:51], s70 offen offset:1024 lds\n"
    "v_mov_b32_e64 v51, 0\n"
    "v_mfma_f32_16x16x32_bf16 v[224:227], a[220:223], a[88:91], v[224:227]\n"
    "v_mov_b32_e64 v52, 0\n"
    "v_mfma_f32_16x16x32_bf16 v[228:231], a[220:223], a[92:95], v[228:231]\n"
    "buffer_load_dwordx4 v248, s[48:51], s70 offen offset:2048 lds\n"
    "v_mov_b32_e64 v53, 0\n"
    "v_mfma_f32_16x16x32_bf16 v[232:235], a[220:223], a[96:99], v[232:235]\n"
    "buffer_load_dwordx4 v249, s[48:51], s70 offen offset:3072 lds\n"
    "v_mov_b32_e64 v54, 0\n"
    "v_mfma_f32_16x16x32_bf16 v[236:239], a[220:223], a[100:103], v[236:239]\n"
    "v_mov_b32_e32 v55, 0\n"
    "s_waitcnt vmcnt(11) lgkmcnt(0)\n"
    "v_mfma_f32_16x16x32_bf16 v[160:163], a[192:195], a[64:67], v[160:163]\n"
    "ds_read_b128 a[84:87], v245 offset:10240\n"
    "v_mfma_f32_16x16x32_bf16 v[164:167], a[192:195], a[68:71], v[164:167]\n"
    "ds_read_b64_tr_b16 a[208:209], v240 offset:49152\n"
    "s_add_u32 m0, s63, 0x20000\n"
    "v_mfma_f32_16x16x32_bf16 v[168:171], a[192:195], a[72:75], v[168:171]\n"
    "ds_read_b64_tr_b16 a[210:211], v240 offset:49664\n"
    "v_mfma_f32_16x16x32_bf16 v[172:175], a[192:195], a[76:79], v[172:175]\n"
    "ds_read_b128 a[88:91], v245 offset:12288\n"
    "s_add_u32 s70, s70, 0x40000\n"
    "v_mfma_f32_16x16x32_bf16 v[176:179], a[192:195], a[80:83], v[176:179]\n"
    "ds_read_b128 a[92:95], v245 offset:14336\n"
    "v_mfma_f32_16x16x32_bf16 v[180:183], a[196:199], a[64:67], v[180:183]\n"
    "ds_read_b128 a[96:99], v245 offset:16384\n"
    "s_cmp_ge_u32 s70, 0x480000\n"
    "v_mfma_f32_16x16x32_bf16 v[184:187], a[196:199], a[68:71], v[184:187]\n"
    "ds_read_b128 a[100:103], v245 offset:18432\n"
    "v_mfma_f32_16x16x32_bf16 v[188:191], a[196:199], a[72:75], v[188:191]\n"
    "ds_read_b64_tr_b16 a[212:213], v241 offset:49152\n"
    "v_mfma_f32_16x16x32_bf16 v[192:195], a[196:199], a[76:79], v[192:195]\n"
    "ds_read_b64_tr_b16 a[214:215], v241 offset:49664\n"
    "s_cselect_b32 s70, s77, s70\n"
    "s_nop 0\n"
    "v_mfma_f32_16x16x32_bf16 v[196:199], a[196:199], a[80:83], v[196:199]\n"
    "ds_read_b64_tr_b16 a[216:217], v242 offset:49152\n"
    "v_mfma_f32_16x16x32_bf16 v[200:203], a[200:203], a[64:67], v[200:203]\n"
    "ds_read_b64_tr_b16 a[218:219], v242 offset:49664\n"
    "buffer_load_dwordx4 v246, s[48:51], s70 offen lds\n"
    "v_mfma_f32_16x16x32_bf16 v[204:207], a[200:203], a[68:71], v[204:207]\n"
    "ds_read_b64_tr_b16 a[220:221], v243 offset:49152\n"
    "v_mfma_f32_16x16x32_bf16 v[208:211], a[200:203], a[72:75], v[208:211]\n"
    "ds_read_b64_tr_b16 a[222:223], v243 offset:49664\n"
    "buffer_load_dwordx4 v247, s[48:51], s70 offen offset:1024 lds\n"
    "v_mfma_f32_16x16x32_bf16 v[212:215], a[200:203], a[76:79], v[212:215]\n"
    "v_mov_b32_e64 v56, 0\n"
    "v_mfma_f32_16x16x32_bf16 v[216:219], a[200:203], a[80:83], v[216:219]\n"
    "v_mov_b32_e64 v57, 0\n"
    "v_mfma_f32_16x16x32_bf16 v[220:223], a[204:207], a[64:67], v[220:223]\n"
    "buffer_load_dwordx4 v248, s[48:51], s70 offen offset:2048 lds\n"
    "v_mov_b32_e64 v58, 0\n"
    "v_mfma_f32_16x16x32_bf16 v[224:227], a[204:207], a[68:71], v[224:227]\n"
    "v_mov_b32_e64 v59, 0\n"
    "v_mfma_f32_16x16x32_bf16 v[228:231], a[204:207], a[72:75], v[228:231]\n"
    "buffer_load_dwordx4 v249, s[48:51], s70 offen offset:3072 lds\n"
    "v_mov_b32_e64 v60, 0\n"
    "v_mfma_f32_16x16x32_bf16 v[232:235], a[204:207], a[76:79], v[232:235]\n"
    "v_mov_b32_e64 v61, 0\n"
    "v_mfma_f32_16x16x32_bf16 v[236:239], a[204:207], a[80:83], v[236:239]\n"
    "v_mov_b32_e64 v62, 0\n"
    "s_waitcnt vmcnt(11) lgkmcnt(0)\n"
    "s_barrier\n"
    "v_mfma_f32_16x16x32_bf16 v[160:163], a[208:211], a[84:87], v[160:163]\n"
    "ds_read_b128 a[64:67], v244 offset:20480\n"
    "v_mfma_f32_16x16x32_bf16 v[164:167], a[208:211], a[88:91], v[164:167]\n"
    "ds_read_b64_tr_b16 a[192:193], v240\n"
    "s_add_u32 m0, s71, 0xa800\n"
    "v_mfma_f32_16x16x32_bf16 v[168:171], a[208:211], a[92:95], v[168:171]\n"
    "ds_read_b64_tr_b16 a[194:195], v240 offset:512\n"
    "s_add_u32 s64, s64, 0x80\n"
    "v_mfma_f32_16x16x32_bf16 v[172:175], a[208:211], a[96:99], v[172:175]\n"
    "ds_read_b128 a[68:71], v244 offset:22528\n"
    "s_cmp_ge_u32 s64, 0x480\n"
    "v_mfma_f32_16x16x32_bf16 v[176:179], a[208:211], a[100:103], v[176:179]\n"
    "ds_read_b128 a[72:75], v244 offset:24576\n"
    "v_mfma_f32_16x16x32_bf16 v[180:183], a[212:215], a[84:87], v[180:183]\n"
    "ds_read_b128 a[76:79], v244 offset:26624\n"
    "s_cselect_b32 s64, 0, s64\n"
    "s_nop 0\n"
    "v_mfma_f32_16x16x32_bf16 v[184:187], a[212:215], a[88:91], v[184:187]\n"
    "ds_read_b128 a[80:83], v244 offset:28672\n"
    "buffer_load_dwordx4 v250, s[44:47], s64 offen lds\n"
    "v_mfma_f32_16x16x32_bf16 v[188:191], a[212:215], a[92:95], v[188:191]\n"
    "ds_read_b64_tr_b16 a[196:197], v241\n"
    "v_mfma_f32_16x16x32_bf16 v[192:195], a[212:215], a[96:99], v[192:195]\n"
    "ds_read_b64_tr_b16 a[198:199], v241 offset:512\n"
    "buffer_load_dwordx4 v251, s[44:47], s64 offen offset:1024 lds\n"
    "v_mfma_f32_16x16x32_bf16 v[196:199], a[212:215], a[100:103], v[196:199]\n"
    "ds_read_b64_tr_b16 a[200:201], v242\n"
    "s_mov_b32 exec_hi, 0\n"
    "buffer_load_dwordx4 v252, s[44:47], s64 offen offset:2048 lds\n"
    "s_mov_b32 exec_hi, -1\n"
    "s_nop 0\n"
    "s_nop 0\n"
    "v_mfma_f32_16x16x32_bf16 v[200:203], a[216:219], a[84:87], v[200:203]\n"
    "ds_read_b64_tr_b16 a[202:203], v242 offset:512\n"
    "v_mfma_f32_16x16x32_bf16 v[204:207], a[216:219], a[88:91], v[204:207]\n"
    "ds_read_b64_tr_b16 a[204:205], v243\n"
    "s_add_u32 m0, s63, 0x24000\n"
    "v_mfma_f32_16x16x32_bf16 v[208:211], a[216:219], a[92:95], v[208:211]\n"
    "ds_read_b64_tr_b16 a[206:207], v243 offset:512\n"
    "s_add_u32 s70, s70, 0x40000\n"
    "v_mfma_f32_16x16x32_bf16 v[212:215], a[216:219], a[96:99], v[212:215]\n"
    "v_mov_b32_e64 v63, 0\n"
    "v_mfma_f32_16x16x32_bf16 v[216:219], a[216:219], a[100:103], v[216:219]\n"
    "buffer_load_dwordx4 v246, s[48:51], s70 offen lds\n"
    "v_mov_b32_e64 v64, 0\n"
    "v_mfma_f32_16x16x32_bf16 v[220:223], a[220:223], a[84:87], v[220:223]\n"
    "buffer_load_dwordx4 v247, s[48:51], s70 offen offset:1024 lds\n"
    "v_mov_b32_e64 v65, 0\n"
    "v_mfma_f32_16x16x32_bf16 v[224:227], a[220:223], a[88:91], v[224:227]\n"
    "v_mov_b32_e64 v66, 0\n"
    "v_mfma_f32_16x16x32_bf16 v[228:231], a[220:223], a[92:95], v[228:231]\n"
    "buffer_load_dwordx4 v248, s[48:51], s70 offen offset:2048 lds\n"
    "v_mov_b32_e64 v67, 0\n"
    "v_mfma_f32_16x16x32_bf16 v[232:235], a[220:223], a[96:99], v[232:235]\n"
    "buffer_load_dwordx4 v249, s[48:51], s70 offen offset:3072 lds\n"
    "v_mov_b32_e64 v68, 0\n"
    "v_mfma_f32_16x16x32_bf16 v[236:239], a[220:223], a[100:103], v[236:239]\n"
    "v_mov_b32_e32 v69, 0\n"
    "s_waitcnt vmcnt(11) lgkmcnt(0)\n"
    "v_mfma_f32_16x16x32_bf16 v[160:163], a[192:195], a[64:67], v[160:163]\n"
    "ds_read_b128 a[84:87], v245 offset:20480\n"
    "v_mfma_f32_16x16x32_bf16 v[164:167], a[192:195], a[68:71], v[164:167]\n"
    "ds_read_b64_tr_b16 a[208:209], v240 offset:16384\n"
    "s_add_u32 m0, s63, 0x18000\n"
    "v_mfma_f32_16x16x32_bf16 v[168:171], a[192:195], a[72:75], v[168:171]\n"
    "ds_read_b64_tr_b16 a[210:211], v240 offset:16896\n"
    "v_mfma_f32_16x16x32_bf16 v[172:175], a[192:195], a[76:79], v[172:175]\n"
    "ds_read_b128 a[88:91], v245 offset:22528\n"
    "s_add_u32 s70, s70, 0x40000\n"
    "v_mfma_f32_16x16x32_bf16 v[176:179], a[192:195], a[80:83], v[176:179]\n"
    "ds_read_b128 a[92:95], v245 offset:24576\n"
    "v_mfma_f32_16x16x32_bf16 v[180:183], a[196:199], a[64:67], v[180:183]\n"
    "ds_read_b128 a[96:99], v245 offset:26624\n"
    "s_cmp_ge_u32 s70, 0x480000\n"
    "v_mfma_f32_16x16x32_bf16 v[184:187], a[196:199], a[68:71], v[184:187]\n"
    "ds_read_b128 a[100:103], v245 offset:28672\n"
    "v_mfma_f32_16x16x32_bf16 v[188:191], a[196:199], a[72:75], v[188:191]\n"
    "ds_read_b64_tr_b16 a[212:213], v241 offset:16384\n"
    "v_mfma_f32_16x16x32_bf16 v[192:195], a[196:199], a[76:79], v[192:195]\n"
    "ds_read_b64_tr_b16 a[214:215], v241 offset:16896\n"
    "s_cselect_b32 s70, s77, s70\n"
    "s_nop 0\n"
    "v_mfma_f32_16x16x32_bf16 v[196:199], a[196:199], a[80:83], v[196:199]\n"
    "ds_read_b64_tr_b16 a[216:217], v242 offset:16384\n"
    "v_mfma_f32_16x16x32_bf16 v[200:203], a[200:203], a[64:67], v[200:203]\n"
    "ds_read_b64_tr_b16 a[218:219], v242 offset:16896\n"
    "buffer_load_dwordx4 v246, s[48:51], s70 offen lds\n"
    "v_mfma_f32_16x16x32_bf16 v[204:207], a[200:203], a[68:71], v[204:207]\n"
    "ds_read_b64_tr_b16 a[220:221], v243 offset:16384\n"
    "v_mfma_f32_16x16x32_bf16 v[208:211], a[200:203], a[72:75], v[208:211]\n"
    "ds_read_b64_tr_b16 a[222:223], v243 offset:16896\n"
    "buffer_load_dwordx4 v247, s[48:51], s70 offen offset:1024 lds\n"
    "v_mfma_f32_16x16x32_bf16 v[212:215], a[200:203], a[76:79], v[212:215]\n"
    "v_mov_b32_e64 v70, 0\n"
    "v_mfma_f32_16x16x32_bf16 v[216:219], a[200:203], a[80:83], v[216:219]\n"
    "v_mov_b32_e64 v71, 0\n"
    "v_mfma_f32_16x16x32_bf16 v[220:223], a[204:207], a[64:67], v[220:223]\n"
    "buffer_load_dwordx4 v248, s[48:51], s70 offen offset:2048 lds\n"
    "v_mov_b32_e64 v72, 0\n"
    "v_mfma_f32_16x16x32_bf16 v[224:227], a[204:207], a[68:71], v[224:227]\n"
    "v_mov_b32_e64 v73, 0\n"
    "v_mfma_f32_16x16x32_bf16 v[228:231], a[204:207], a[72:75], v[228:231]\n"
    "buffer_load_dwordx4 v249, s[48:51], s70 offen offset:3072 lds\n"
    "v_mov_b32_e64 v74, 0\n"
    "v_mfma_f32_16x16x32_bf16 v[232:235], a[204:207], a[76:79], v[232:235]\n"
    "v_mov_b32_e64 v75, 0\n"
    "v_mfma_f32_16x16x32_bf16 v[236:239], a[204:207], a[80:83], v[236:239]\n"
    "v_mov_b32_e64 v76, 0\n"
    "s_waitcnt vmcnt(11) lgkmcnt(0)\n"
    "s_barrier\n"
    "v_mfma_f32_16x16x32_bf16 v[160:163], a[208:211], a[84:87], v[160:163]\n"
    "ds_read_b128 a[64:67], v244\n"
    "v_mfma_f32_16x16x32_bf16 v[164:167], a[208:211], a[88:91], v[164:167]\n"
    "ds_read_b64_tr_b16 a[192:193], v240 offset:32768\n"
    "s_add_u32 m0, s71, 0xd000\n"
    "v_mfma_f32_16x16x32_bf16 v[168:171], a[208:211], a[92:95], v[168:171]\n"
    "ds_read_b64_tr_b16 a[194:195], v240 offset:33280\n"
    "s_add_u32 s64, s64, 0x80\n"
    "v_mfma_f32_16x16x32_bf16 v[172:175], a[208:211], a[96:99], v[172:175]\n"
    "ds_read_b128 a[68:71], v244 offset:2048\n"
    "s_cmp_ge_u32 s64, 0x480\n"
    "v_mfma_f32_16x16x32_bf16 v[176:179], a[208:211], a[100:103], v[176:179]\n"
    "ds_read_b128 a[72:75], v244 offset:4096\n"
    "v_mfma_f32_16x16x32_bf16 v[180:183], a[212:215], a[84:87], v[180:183]\n"
    "ds_read_b128 a[76:79], v244 offset:6144\n"
    "s_cselect_b32 s64, 0, s64\n"
    "s_nop 0\n"
    "v_mfma_f32_16x16x32_bf16 v[184:187], a[212:215], a[88:91], v[184:187]\n"
    "ds_read_b128 a[80:83], v244 offset:8192\n"
    "buffer_load_dwordx4 v250, s[44:47], s64 offen lds\n"
    "v_mfma_f32_16x16x32_bf16 v[188:191], a[212:215], a[92:95], v[188:191]\n"
    "ds_read_b64_tr_b16 a[196:197], v241 offset:32768\n"
    "v_mfma_f32_16x16x32_bf16 v[192:195], a[212:215], a[96:99], v[192:195]\n"
    "ds_read_b64_tr_b16 a[198:199], v241 offset:33280\n"
    "buffer_load_dwordx4 v251, s[44:47], s64 offen offset:1024 lds\n"
    "v_mfma_f32_16x16x32_bf16 v[196:199], a[212:215], a[100:103], v[196:199]\n"
    "ds_read_b64_tr_b16 a[200:201], v242 offset:32768\n"
    "s_mov_b32 exec_hi, 0\n"
    "buffer_load_dwordx4 v252, s[44:47], s64 offen offset:2048 lds\n"
    "s_mov_b32 exec_hi, -1\n"
    "s_nop 0\n"
    "s_nop 0\n"
    "v_mfma_f32_16x16x32_bf16 v[200:203], a[216:219], a[84:87], v[200:203]\n"
    "ds_read_b64_tr_b16 a[202:203], v242 offset:33280\n"
    "v_mfma_f32_16x16x32_bf16 v[204:207], a[216:219], a[88:91], v[204:207]\n"
    "ds_read_b64_tr_b16 a[204:205], v243 offset:32768\n"
    "s_add_u32 m0, s63, 0x1c000\n"
    "v_mfma_f32_16x16x32_bf16 v[208:211], a[216:219], a[92:95], v[208:211]\n"
    "ds_read_b64_tr_b16 a[206:207], v243 offset:33280\n"
    "s_add_u32 s70, s70, 0x40000\n"
    "v_mfma_f32_16x16x32_bf16 v[212:215], a[216:219], a[96:99], v[212:215]\n"
    "v_mov_b32_e64 v77, 0\n"
    "v_mfma_f32_16x16x32_bf16 v[216:219], a[216:219], a[100:103], v[216:219]\n"
    "buffer_load_dwordx4 v246, s[48:51], s70 offen lds\n"
    "v_mov_b32_e64 v78, 0\n"
    "v_mfma_f32_16x16x32_bf16 v[220:223], a[220:223], a[84:87], v[220:223]\n"
    "buffer_load_dwordx4 v247, s[48:51], s70 offen offset:1024 lds\n"
    "v_mov_b32_e64 v79, 0\n"
    "v_mfma_f32_16x16x32_bf16 v[224:227], a[220:223], a[88:91], v[224:227]\n"
    "v_mov_b32_e64 v80, 0\n"
    "v_mfma_f32_16x16x32_bf16 v[228:231], a[220:223], a[92:95], v[228:231]\n"
    "buffer_load_dwordx4 v248, s[48:51], s70 offen offset:2048 lds\n"
    "v_mov_b32_e64 v81, 0\n"
    "v_mfma_f32_16x16x32_bf16 v[232:235], a[220:223], a[96:99], v[232:235]\n"
    "buffer_load_dwordx4 v249, s[48:51], s70 offen offset:3072 lds\n"
    "v_mov_b32_e64 v82, 0\n"
    "v_mfma_f32_16x16x32_bf16 v[236:239], a[220:223], a[100:103], v[236:239]\n"
    "v_mov_b32_e32 v83, 0\n"
    "s_waitcnt vmcnt(11) lgkmcnt(0)\n"
    "v_mfma_f32_16x16x32_bf16 v[160:163], a[192:195], a[64:67], v[160:163]\n"
    "ds_read_b128 a[84:87], v245\n"
    "v_mfma_f32_16x16x32_bf16 v[164:167], a[192:195], a[68:71], v[164:167]\n"
    "ds_read_b64_tr_b16 a[208:209], v240 offset:49152\n"
    "s_add_u32 m0, s63, 0x20000\n"
    "v_mfma_f32_16x16x32_bf16 v[168:171], a[192:195], a[72:75], v[168:171]\n"
    "ds_read_b64_tr_b16 a[210:211], v240 offset:49664\n"
    "v_mfma_f32_16x16x32_bf16 v[172:175], a[192:195], a[76:79], v[172:175]\n"
    "ds_read_b128 a[88:91], v245 offset:2048\n"
    "s_add_u32 s70, s70, 0x40000\n"
    "v_mfma_f32_16x16x32_bf16 v[176:179], a[192:195], a[80:83], v[176:179]\n"
    "ds_read_b128 a[92:95], v245 offset:4096\n"
    "v_mfma_f32_16x16x32_bf16 v[180:183], a[196:199], a[64:67], v[180:183]\n"
    "ds_read_b128 a[96:99], v245 offset:6144\n"
    "s_cmp_ge_u32 s70, 0x480000\n"
    "v_mfma_f32_16x16x32_bf16 v[184:187], a[196:199], a[68:71], v[184:187]\n"
    "ds_read_b128 a[100:103], v245 offset:8192\n"
    "v_mfma_f32_16x16x32_bf16 v[188:191], a[196:199], a[72:75], v[188:191]\n"
    "ds_read_b64_tr_b16 a[212:213], v241 offset:49152\n"
    "v_mfma_f32_16x16x32_bf16 v[192:195], a[196:199], a[76:79], v[192:195]\n"
    "ds_read_b64_tr_b16 a[214:215], v241 offset:49664\n"
    "s_cselect_b32 s70, s77, s70\n"
    "s_nop 0\n"
    "v_mfma_f32_16x16x32_bf16 v[196:199], a[196:199], a[80:83], v[196:199]\n"
    "ds_read_b64_tr_b16 a[216:217], v242 offset:49152\n"
    "v_mfma_f32_16x16x32_bf16 v[200:203], a[200:203], a[64:67], v[200:203]\n"
    "ds_read_b64_tr_b16 a[218:219], v242 offset:49664\n"
    "buffer_load_dwordx4 v246, s[48:51], s70 offen lds\n"
    "v_mfma_f32_16x16x32_bf16 v[204:207], a[200:203], a[68:71], v[204:207]\n"
    "ds_read_b64_tr_b16 a[220:221], v243 offset:49152\n"
    "v_mfma_f32_16x16x32_bf16 v[208:211], a[200:203], a[72:75], v[208:211]\n"
    "ds_read_b64_tr_b16 a[222:223], v243 offset:49664\n"
    "buffer_load_dwordx4 v247, s[48:51], s70 offen offset:1024 lds\n"
    "v_mfma_f32_16x16x32_bf16 v[212:215], a[200:203], a[76:79], v[212:215]\n"
    "v_mov_b32_e64 v84, 0\n"
    "v_mfma_f32_16x16x32_bf16 v[216:219], a[200:203], a[80:83], v[216:219]\n"
    "v_mov_b32_e64 v85, 0\n"
    "v_mfma_f32_16x16x32_bf16 v[220:223], a[204:207], a[64:67], v[220:223]\n"
    "buffer_load_dwordx4 v248, s[48:51], s70 offen offset:2048 lds\n"
    "v_mov_b32_e64 v86, 0\n"
    "v_mfma_f32_16x16x32_bf16 v[224:227], a[204:207], a[68:71], v[224:227]\n"
    "v_mov_b32_e64 v87, 0\n"
    "v_mfma_f32_16x16x32_bf16 v[228:231], a[204:207], a[72:75], v[228:231]\n"
    "buffer_load_dwordx4 v249, s[48:51], s70 offen offset:3072 lds\n"
    "v_mov_b32_e64 v88, 0\n"
    "v_mfma_f32_16x16x32_bf16 v[232:235], a[204:207], a[76:79], v[232:235]\n"
    "v_mov_b32_e64 v89, 0\n"
    "v_mfma_f32_16x16x32_bf16 v[236:239], a[204:207], a[80:83], v[236:239]\n"
    "v_mov_b32_e64 v90, 0\n"
    "s_waitcnt vmcnt(11) lgkmcnt(0)\n"
    "s_barrier\n"
    "v_mfma_f32_16x16x32_bf16 v[160:163], a[208:211], a[84:87], v[160:163]\n"
    "ds_read_b128 a[64:67], v244 offset:10240\n"
    "v_mfma_f32_16x16x32_bf16 v[164:167], a[208:211], a[88:91], v[164:167]\n"
    "ds_read_b64_tr_b16 a[192:193], v240\n"
    "s_add_u32 m0, s63, 0x24000\n"
    "v_mfma_f32_16x16x32_bf16 v[168:171], a[208:211], a[92:95], v[168:171]\n"
    "ds_read_b64_tr_b16 a[194:195], v240 offset:512\n"
    "v_mfma_f32_16x16x32_bf16 v[172:175], a[208:211], a[96:99], v[172:175]\n"
    "ds_read_b128 a[68:71], v244 offset:12288\n"
    "v_mfma_f32_16x16x32_bf16 v[176:179], a[208:211], a[100:103], v[176:179]\n"
    "ds_read_b128 a[72:75], v244 offset:14336\n"
    "s_add_u32 s70, s70, 0x40000\n"
    "v_mfma_f32_16x16x32_bf16 v[180:183], a[212:215], a[84:87], v[180:183]\n"
    "ds_read_b128 a[76:79], v244 offset:16384\n"
    "v_mfma_f32_16x16x32_bf16 v[184:187], a[212:215], a[88:91], v[184:187]\n"
    "ds_read_b128 a[80:83], v244 offset:18432\n"
    "v_mfma_f32_16x16x32_bf16 v[188:191], a[212:215], a[92:95], v[188:191]\n"
    "ds_read_b64_tr_b16 a[196:197], v241\n"
    "buffer_load_dwordx4 v246, s[48:51], s70 offen lds\n"
    "v_mfma_f32_16x16x32_bf16 v[192:195], a[212:215], a[96:99], v[192:195]\n"
    "ds_read_b64_tr_b16 a[198:199], v241 offset:512\n"
    "v_mfma_f32_16x16x32_bf16 v[196:199], a[212:215], a[100:103], v[196:199]\n"
    "ds_read_b64_tr_b16 a[200:201], v242\n"
    "v_mfma_f32_16x16x32_bf16 v[200:203], a[216:219], a[84:87], v[200:203]\n"
    "ds_read_b64_tr_b16 a[202:203], v242 offset:512\n"
    "buffer_load_dwordx4 v247, s[48:51], s70 offen offset:1024 lds\n"
    "v_mfma_f32_16x16x32_bf16 v[204:207], a[216:219], a[88:91], v[204:207]\n"
    "ds_read_b64_tr_b16 a[204:205], v243\n"
    "v_mfma_f32_16x16x32_bf16 v[208:211], a[216:219], a[92:95], v[208:211]\n"
    "ds_read_b64_tr_b16 a[206:207], v243 offset:512\n"
    "v_mfma_f32_16x16x32_bf16 v[212:215], a[216:219], a[96:99], v[212:215]\n"
    "buffer_load_dwordx4 v248, s[48:51], s70 offen offset:2048 lds\n"
    "v_mov_b32_e64 v91, 0\n"
    "v_mfma_f32_16x16x32_bf16 v[216:219], a[216:219], a[100:103], v[216:219]\n"
    "v_mov_b32_e64 v92, 0\n"
    "v_mfma_f32_16x16x32_bf16 v[220:223], a[220:223], a[84:87], v[220:223]\n"
    "v_mov_b32_e64 v93, 0\n"
    "v_mfma_f32_16x16x32_bf16 v[224:227], a[220:223], a[88:91], v[224:227]\n"
    "buffer_load_dwordx4 v249, s[48:51], s70 offen offset:3072 lds\n"
    "v_mov_b32_e64 v94, 0\n"
    "v_mfma_f32_16x16x32_bf16 v[228:231], a[220:223], a[92:95], v[228:231]\n"
    "v_mov_b32_e64 v95, 0\n"
    "v_mfma_f32_16x16x32_bf16 v[232:235], a[220:223], a[96:99], v[232:235]\n"
    "v_accvgpr_write_b32 a48, 0\n"
    "v_mfma_f32_16x16x32_bf16 v[236:239], a[220:223], a[100:103], v[236:239]\n"
    "v_accvgpr_write_b32 a49, 0\n"
    "s_waitcnt vmcnt(8) lgkmcnt(0)\n"
    "s_nop 0\n"
    "v_mfma_f32_16x16x32_bf16 v[160:163], a[192:195], a[64:67], v[160:163]\n"
    "ds_read_b128 a[84:87], v245 offset:10240\n"
    "v_mfma_f32_16x16x32_bf16 v[164:167], a[192:195], a[68:71], v[164:167]\n"
    "ds_read_b64_tr_b16 a[208:209], v240 offset:16384\n"
    "s_add_u32 m0, s63, 0\n"
    "s_nop 0\n"
    "v_mfma_f32_16x16x32_bf16 v[168:171], a[192:195], a[72:75], v[168:171]\n"
    "ds_read_b64_tr_b16 a[210:211], v240 offset:16896\n"
    "v_mfma_f32_16x16x32_bf16 v[172:175], a[192:195], a[76:79], v[172:175]\n"
    "ds_read_b128 a[88:91], v245 offset:12288\n"
    "v_mfma_f32_16x16x32_bf16 v[176:179], a[192:195], a[80:83], v[176:179]\n"
    "ds_read_b128 a[92:95], v245 offset:14336\n"
    "s_add_u32 s68, s63, 0\n"
    "s_nop 0\n"
    "v_mfma_f32_16x16x32_bf16 v[180:183], a[196:199], a[64:67], v[180:183]\n"
    "ds_read_b128 a[96:99], v245 offset:16384\n"
    "v_mfma_f32_16x16x32_bf16 v[184:187], a[196:199], a[68:71], v[184:187]\n"
    "ds_read_b128 a[100:103], v245 offset:18432\n"
    "v_mfma_f32_16x16x32_bf16 v[188:191], a[196:199], a[72:75], v[188:191]\n"
    "ds_read_b64_tr_b16 a[212:213], v241 offset:16384\n"
    "buffer_load_dwordx4 v152, s[36:39], s68 offen nt lds\n"
    "v_mfma_f32_16x16x32_bf16 v[192:195], a[196:199], a[76:79], v[192:195]\n"
    "ds_read_b64_tr_b16 a[214:215], v241 offset:16896\n"
    "v_mfma_f32_16x16x32_bf16 v[196:199], a[196:199], a[80:83], v[196:199]\n"
    "ds_read_b64_tr_b16 a[216:217], v242 offset:16384\n"
    "v_mfma_f32_16x16x32_bf16 v[200:203], a[200:203], a[64:67], v[200:203]\n"
    "ds_read_b64_tr_b16 a[218:219], v242 offset:16896\n"
    "buffer_load_dwordx4 v153, s[36:39], s68 offen offset:1024 nt lds\n"
    "v_mfma_f32_16x16x32_bf16 v[204:207], a[200:203], a[68:71], v[204:207]\n"
    "ds_read_b64_tr_b16 a[220:221], v243 offset:16384\n"
    "v_mfma_f32_16x16x32_bf16 v[208:211], a[200:203], a[72:75], v[208:211]\n"
    "ds_read_b64_tr_b16 a[222:223], v243 offset:16896\n"
    "v_mfma_f32_16x16x32_bf16 v[212:215], a[200:203], a[76:79], v[212:215]\n"
    "buffer_load_dwordx4 v152, s[36:39], s68 offen offset:2048 nt lds\n"
    "v_accvgpr_write_b32 a50, 0\n"
    "v_mfma_f32_16x16x32_bf16 v[216:219], a[200:203], a[80:83], v[216:219]\n"
    "v_accvgpr_write_b32 a51, 0\n"
    "v_mfma_f32_16x16x32_bf16 v[220:223], a[204:207], a[64:67], v[220:223]\n"
    "v_accvgpr_write_b32 a52, 0\n"
    "v_mfma_f32_16x16x32_bf16 v[224:227], a[204:207], a[68:71], v[224:227]\n"
    "buffer_load_dwordx4 v153, s[36:39], s68 offen offset:3072 nt lds\n"
    "v_accvgpr_write_b32 a53, 0\n"
    "v_mfma_f32_16x16x32_bf16 v[228:231], a[204:207], a[72:75], v[228:231]\n"
    "v_accvgpr_write_b32 a54, 0\n"
    "v_mfma_f32_16x16x32_bf16 v[232:235], a[204:207], a[76:79], v[232:235]\n"
    "v_accvgpr_write_b32 a55, 0\n"
    "v_mfma_f32_16x16x32_bf16 v[236:239], a[204:207], a[80:83], v[236:239]\n"
    "v_accvgpr_write_b32 a56, 0\n"
    "s_waitcnt vmcnt(8) lgkmcnt(0)\n"
    "s_barrier\n"
    "v_mfma_f32_16x16x32_bf16 v[160:163], a[208:211], a[84:87], v[160:163]\n"
    "ds_read_b128 a[64:67], v244 offset:20480\n"
    "v_mfma_f32_16x16x32_bf16 v[164:167], a[208:211], a[88:91], v[164:167]\n"
    "ds_read_b64_tr_b16 a[192:193], v240 offset:32768\n"
    "s_add_u32 m0, s63, 0x10000\n"
    "v_mfma_f32_16x16x32_bf16 v[168:171], a[208:211], a[92:95], v[168:171]\n"
    "ds_read_b64_tr_b16 a[194:195], v240 offset:33280\n"
    "v_mfma_f32_16x16x32_bf16 v[172:175], a[208:211], a[96:99], v[172:175]\n"
    "ds_read_b128 a[68:71], v244 offset:22528\n"
    "v_mfma_f32_16x16x32_bf16 v[176:179], a[208:211], a[100:103], v[176:179]\n"
    "ds_read_b128 a[72:75], v244 offset:24576\n"
    "s_add_u32 s68, s63, 0\n"
    "s_nop 0\n"
    "v_mfma_f32_16x16x32_bf16 v[180:183], a[212:215], a[84:87], v[180:183]\n"
    "ds_read_b128 a[76:79], v244 offset:26624\n"
    "v_mfma_f32_16x16x32_bf16 v[184:187], a[212:215], a[88:91], v[184:187]\n"
    "ds_read_b128 a[80:83], v244 offset:28672\n"
    "v_mfma_f32_16x16x32_bf16 v[188:191], a[212:215], a[92:95], v[188:191]\n"
    "ds_read_b64_tr_b16 a[196:197], v241 offset:32768\n"
    "buffer_load_dwordx4 v152, s[40:43], s68 offen nt lds\n"
    "v_mfma_f32_16x16x32_bf16 v[192:195], a[212:215], a[96:99], v[192:195]\n"
    "ds_read_b64_tr_b16 a[198:199], v241 offset:33280\n"
    "v_mfma_f32_16x16x32_bf16 v[196:199], a[212:215], a[100:103], v[196:199]\n"
    "ds_read_b64_tr_b16 a[200:201], v242 offset:32768\n"
    "v_mfma_f32_16x16x32_bf16 v[200:203], a[216:219], a[84:87], v[200:203]\n"
    "ds_read_b64_tr_b16 a[202:203], v242 offset:33280\n"
    "buffer_load_dwordx4 v153, s[40:43], s68 offen offset:1024 nt lds\n"
    "v_mfma_f32_16x16x32_bf16 v[204:207], a[216:219], a[88:91], v[204:207]\n"
    "ds_read_b64_tr_b16 a[204:205], v243 offset:32768\n"
    "v_mfma_f32_16x16x32_bf16 v[208:211], a[216:219], a[92:95], v[208:211]\n"
    "ds_read_b64_tr_b16 a[206:207], v243 offset:33280\n"
    "v_mfma_f32_16x16x32_bf16 v[212:215], a[216:219], a[96:99], v[212:215]\n"
    "buffer_load_dwordx4 v152, s[40:43], s68 offen offset:2048 nt lds\n"
    "v_accvgpr_write_b32 a57, 0\n"
    "v_mfma_f32_16x16x32_bf16 v[216:219], a[216:219], a[100:103], v[216:219]\n"
    "v_mfma_f32_16x16x32_bf16 v[220:223], a[220:223], a[84:87], v[220:223]\n"
    "v_accvgpr_write_b32 a58, 0\n"
    "v_mfma_f32_16x16x32_bf16 v[224:227], a[220:223], a[88:91], v[224:227]\n"
    "buffer_load_dwordx4 v153, s[40:43], s68 offen offset:3072 nt lds\n"
    "v_mfma_f32_16x16x32_bf16 v[228:231], a[220:223], a[92:95], v[228:231]\n"
    "v_accvgpr_write_b32 a59, 0\n"
    "v_mfma_f32_16x16x32_bf16 v[232:235], a[220:223], a[96:99], v[232:235]\n"
    "v_mfma_f32_16x16x32_bf16 v[236:239], a[220:223], a[100:103], v[236:239]\n"
    "s_waitcnt vmcnt(8) lgkmcnt(0)\n"
    "s_nop 0\n"
    "v_mfma_f32_16x16x32_bf16 v[160:163], a[192:195], a[64:67], v[160:163]\n"
    "ds_read_b128 a[84:87], v245 offset:20480\n"
    "v_mfma_f32_16x16x32_bf16 v[164:167], a[192:195], a[68:71], v[164:167]\n"
    "ds_read_b64_tr_b16 a[208:209], v240 offset:49152\n"
    "v_mfma_f32_16x16x32_bf16 v[168:171], a[192:195], a[72:75], v[168:171]\n"
    "ds_read_b64_tr_b16 a[210:211], v240 offset:49664\n"
    "v_mfma_f32_16x16x32_bf16 v[172:175], a[192:195], a[76:79], v[172:175]\n"
    "ds_read_b128 a[88:91], v245 offset:22528\n"
    "v_mfma_f32_16x16x32_bf16 v[176:179], a[192:195], a[80:83], v[176:179]\n"
    "ds_read_b128 a[92:95], v245 offset:24576\n"
    "v_mfma_f32_16x16x32_bf16 v[180:183], a[196:199], a[64:67], v[180:183]\n"
    "ds_read_b128 a[96:99], v245 offset:26624\n"
    "v_mfma_f32_16x16x32_bf16 v[184:187], a[196:199], a[68:71], v[184:187]\n"
    "ds_read_b128 a[100:103], v245 offset:28672\n"
    "v_mfma_f32_16x16x32_bf16 v[188:191], a[196:199], a[72:75], v[188:191]\n"
    "ds_read_b64_tr_b16 a[212:213], v241 offset:49152\n"
    "v_mfma_f32_16x16x32_bf16 v[192:195], a[196:199], a[76:79], v[192:195]\n"
    "ds_read_b64_tr_b16 a[214:215], v241 offset:49664\n"
    "v_mfma_f32_16x16x32_bf16 v[196:199], a[196:199], a[80:83], v[196:199]\n"
    "ds_read_b64_tr_b16 a[216:217], v242 offset:49152\n"
    "v_mfma_f32_16x16x32_bf16 v[200:203], a[200:203], a[64:67], v[200:203]\n"
    "ds_read_b64_tr_b16 a[218:219], v242 offset:49664\n"
    "v_mfma_f32_16x16x32_bf16 v[204:207], a[200:203], a[68:71], v[204:207]\n"
    "ds_read_b64_tr_b16 a[220:221], v243 offset:49152\n"
    "v_mfma_f32_16x16x32_bf16 v[208:211], a[200:203], a[72:75], v[208:211]\n"
    "ds_read_b64_tr_b16 a[222:223], v243 offset:49664\n"
    "v_mfma_f32_16x16x32_bf16 v[212:215], a[200:203], a[76:79], v[212:215]\n"
    "v_mfma_f32_16x16x32_bf16 v[216:219], a[200:203], a[80:83], v[216:219]\n"
    "v_mfma_f32_16x16x32_bf16 v[220:223], a[204:207], a[64:67], v[220:223]\n"
    "v_mfma_f32_16x16x32_bf16 v[224:227], a[204:207], a[68:71], v[224:227]\n"
    "v_mfma_f32_16x16x32_bf16 v[228:231], a[204:207], a[72:75], v[228:231]\n"
    "v_mfma_f32_16x16x32_bf16 v[232:235], a[204:207], a[76:79], v[232:235]\n"
    "v_mfma_f32_16x16x32_bf16 v[236:239], a[204:207], a[80:83], v[236:239]\n"
    "s_waitcnt lgkmcnt(0)\n"
    "s_barrier\n"
    "v_mfma_f32_16x16x32_bf16 v[160:163], a[208:211], a[84:87], v[160:163]\n"
    "s_add_u32 m0, s63, 0x4000\n"
    "v_mfma_f32_16x16x32_bf16 v[164:167], a[208:211], a[88:91], v[164:167]\n"
    "s_add_u32 s68, s63, 0x4000\n"
    "v_mfma_f32_16x16x32_bf16 v[168:171], a[208:211], a[92:95], v[168:171]\n"
    "buffer_load_dwordx4 v152, s[36:39], s68 offen nt lds\n"
    "v_mfma_f32_16x16x32_bf16 v[172:175], a[208:211], a[96:99], v[172:175]\n"
    "buffer_load_dwordx4 v153, s[36:39], s68 offen offset:1024 nt lds\n"
    "v_mfma_f32_16x16x32_bf16 v[176:179], a[208:211], a[100:103], v[176:179]\n"
    "buffer_load_dwordx4 v152, s[36:39], s68 offen offset:2048 nt lds\n"
    "v_pk_mul_f32 v[160:161], v[160:161], s[76:77] op_sel_hi:[1,0]\n"
    "v_pk_mul_f32 v[162:163], v[162:163], s[76:77] op_sel_hi:[1,0]\n"
    "v_cvt_pk_bf16_f32 v160, v160, v161\n"
    "v_cvt_pk_bf16_f32 v161, v162, v163\n"
    "v_mfma_f32_16x16x32_bf16 v[180:183], a[212:215], a[84:87], v[180:183]\n"
    "ds_write_b64 v253, v[160:161]\n"
    "buffer_load_dwordx4 v153, s[36:39], s68 offen offset:3072 nt lds\n"
    "v_pk_mul_f32 v[164:165], v[164:165], s[76:77] op_sel_hi:[1,0]\n"
    "v_pk_mul_f32 v[166:167], v[166:167], s[76:77] op_sel_hi:[1,0]\n"
    "v_cvt_pk_bf16_f32 v164, v164, v165\n"
    "v_cvt_pk_bf16_f32 v165, v166, v167\n"
    "v_mfma_f32_16x16x32_bf16 v[184:187], a[212:215], a[88:91], v[184:187]\n"
    "ds_write_b64 v253, v[164:165] offset:4096\n"
    "v_pk_mul_f32 v[168:169], v[168:169], s[76:77] op_sel_hi:[1,0]\n"
    "v_pk_mul_f32 v[170:171], v[170:171], s[76:77] op_sel_hi:[1,0]\n"
    "v_cvt_pk_bf16_f32 v168, v168, v169\n"
    "v_cvt_pk_bf16_f32 v169, v170, v171\n"
    "v_mfma_f32_16x16x32_bf16 v[188:191], a[212:215], a[92:95], v[188:191]\n"
    "ds_write_b64 v253, v[168:169] offset:8192\n"
    "v_pk_mul_f32 v[172:173], v[172:173], s[76:77] op_sel_hi:[1,0]\n"
    "v_pk_mul_f32 v[174:175], v[174:175], s[76:77] op_sel_hi:[1,0]\n"
    "v_cvt_pk_bf16_f32 v172, v172, v173\n"
    "v_cvt_pk_bf16_f32 v173, v174, v175\n"
    "v_mfma_f32_16x16x32_bf16 v[192:195], a[212:215], a[96:99], v[192:195]\n"
    "ds_write_b64 v253, v[172:173] offset:12288\n"
    "v_pk_mul_f32 v[176:177], v[176:177], s[76:77] op_sel_hi:[1,0]\n"
    "v_pk_mul_f32 v[178:179], v[178:179], s[76:77] op_sel_hi:[1,0]\n"
    "v_cvt_pk_bf16_f32 v176, v176, v177\n"
    "v_cvt_pk_bf16_f32 v177, v178, v179\n"
    "v_mfma_f32_16x16x32_bf16 v[196:199], a[212:215], a[100:103], v[196:199]\n"
    "ds_write_b64 v253, v[176:177] offset:16384\n"
    "v_pk_mul_f32 v[180:181], v[180:181], s[76:77] op_sel_hi:[1,0]\n"
    "v_pk_mul_f32 v[182:183], v[182:183], s[76:77] op_sel_hi:[1,0]\n"
    "v_cvt_pk_bf16_f32 v180, v180, v181\n"
    "v_cvt_pk_bf16_f32 v181, v182, v183\n"
    "v_mfma_f32_16x16x32_bf16 v[200:203], a[216:219], a[84:87], v[200:203]\n"
    "ds_write_b64 v253, v[180:181] offset:512\n"
    "v_pk_mul_f32 v[184:185], v[184:185], s[76:77] op_sel_hi:[1,0]\n"
    "v_pk_mul_f32 v[186:187], v[186:187], s[76:77] op_sel_hi:[1,0]\n"
    "v_cvt_pk_bf16_f32 v184, v184, v185\n"
    "v_cvt_pk_bf16_f32 v185, v186, v187\n"
    "v_mfma_f32_16x16x32_bf16 v[204:207], a[216:219], a[88:91], v[204:207]\n"
    "ds_write_b64 v253, v[184:185] offset:4608\n"
    "v_pk_mul_f32 v[188:189], v[188:189], s[76:77] op_sel_hi:[1,0]\n"
    "v_pk_mul_f32 v[190:191], v[190:191], s[76:77] op_sel_hi:[1,0]\n"
    "v_cvt_pk_bf16_f32 v188, v188, v189\n"
    "v_cvt_pk_bf16_f32 v189, v190, v191\n"
    "v_mfma_f32_16x16x32_bf16 v[208:211], a[216:219], a[92:95], v[208:211]\n"
    "ds_write_b64 v253, v[188:189] offset:8704\n"
    "v_pk_mul_f32 v[192:193], v[192:193], s[76:77] op_sel_hi:[1,0]\n"
    "v_pk_mul_f32 v[194:195], v[194:195], s[76:77] op_sel_hi:[1,0]\n"
    "v_cvt_pk_bf16_f32 v192, v192, v193\n"
    "v_cvt_pk_bf16_f32 v193, v194, v195\n"
    "v_mfma_f32_16x16x32_bf16 v[212:215], a[216:219], a[96:99], v[212:215]\n"
    "ds_write_b64 v253, v[192:193] offset:12800\n"
    "v_pk_mul_f32 v[196:197], v[196:197], s[76:77] op_sel_hi:[1,0]\n"
    "v_pk_mul_f32 v[198:199], v[198:199], s[76:77] op_sel_hi:[1,0]\n"
    "v_cvt_pk_bf16_f32 v196, v196, v197\n"
    "v_cvt_pk_bf16_f32 v197, v198, v199\n"
    "v_mfma_f32_16x16x32_bf16 v[216:219], a[216:219], a[100:103], v[216:219]\n"
    "ds_write_b64 v253, v[196:197] offset:16896\n"
    "v_pk_mul_f32 v[200:201], v[200:201], s[76:77] op_sel_hi:[1,0]\n"
    "v_pk_mul_f32 v[202:203], v[202:203], s[76:77] op_sel_hi:[1,0]\n"
    "v_cvt_pk_bf16_f32 v200, v200, v201\n"
    "v_cvt_pk_bf16_f32 v201, v202, v203\n"
    "v_mfma_f32_16x16x32_bf16 v[220:223], a[220:223], a[84:87], v[220:223]\n"
    "ds_write_b64 v253, v[200:201] offset:1024\n"
    "v_pk_mul_f32 v[204:205], v[204:205], s[76:77] op_sel_hi:[1,0]\n"
    "v_pk_mul_f32 v[206:207], v[206:207], s[76:77] op_sel_hi:[1,0]\n"
    "v_cvt_pk_bf16_f32 v204, v204, v205\n"
    "v_cvt_pk_bf16_f32 v205, v206, v207\n"
    "v_mfma_f32_16x16x32_bf16 v[224:227], a[220:223], a[88:91], v[224:227]\n"
    "ds_write_b64 v253, v[204:205] offset:5120\n"
    "v_pk_mul_f32 v[208:209], v[208:209], s[76:77] op_sel_hi:[1,0]\n"
    "v_pk_mul_f32 v[210:211], v[210:211], s[76:77] op_sel_hi:[1,0]\n"
    "v_cvt_pk_bf16_f32 v208, v208, v209\n"
    "v_cvt_pk_bf16_f32 v209, v210, v211\n"
    "v_mfma_f32_16x16x32_bf16 v[228:231], a[220:223], a[92:95], v[228:231]\n"
    "ds_write_b64 v253, v[208:209] offset:9216\n"
    "v_pk_mul_f32 v[212:213], v[212:213], s[76:77] op_sel_hi:[1,0]\n"
    "v_pk_mul_f32 v[214:215], v[214:215], s[76:77] op_sel_hi:[1,0]\n"
    "v_cvt_pk_bf16_f32 v212, v212, v213\n"
    "v_cvt_pk_bf16_f32 v213, v214, v215\n"
    "v_mfma_f32_16x16x32_bf16 v[232:235], a[220:223], a[96:99], v[232:235]\n"
    "ds_write_b64 v253, v[212:213] offset:13312\n"
    "v_pk_mul_f32 v[216:217], v[216:217], s[76:77] op_sel_hi:[1,0]\n"
    "v_pk_mul_f32 v[218:219], v[218:219], s[76:77] op_sel_hi:[1,0]\n"
    "v_cvt_pk_bf16_f32 v216, v216, v217\n"
    "v_cvt_pk_bf16_f32 v217, v218, v219\n"
    "v_mfma_f32_16x16x32_bf16 v[236:239], a[220:223], a[100:103], v[236:239]\n"
    "ds_write_b64 v253, v[216:217] offset:17408\n"
    "v_pk_mul_f32 v[220:221], v[220:221], s[76:77] op_sel_hi:[1,0]\n"
    "v_pk_mul_f32 v[222:223], v[222:223], s[76:77] op_sel_hi:[1,0]\n"
    "v_cvt_pk_bf16_f32 v220, v220, v221\n"
    "v_cvt_pk_bf16_f32 v221, v222, v223\n"
    "s_waitcnt lgkmcnt(4)\n"
    "s_nop 0\n"
    "ds_write_b64 v253, v[220:221] offset:1536\n"
    "v_pk_mul_f32 v[224:225], v[224:225], s[76:77] op_sel_hi:[1,0]\n"
    "v_pk_mul_f32 v[226:227], v[226:227], s[76:77] op_sel_hi:[1,0]\n"
    "v_cvt_pk_bf16_f32 v224, v224, v225\n"
    "v_cvt_pk_bf16_f32 v225, v226, v227\n"
    "v_pk_mul_f32 v[228:229], v[228:229], s[76:77] op_sel_hi:[1,0]\n"
    "v_pk_mul_f32 v[230:231], v[230:231], s[76:77] op_sel_hi:[1,0]\n"
    "v_cvt_pk_bf16_f32 v228, v228, v229\n"
    "v_cvt_pk_bf16_f32 v229, v230, v231\n"
    "v_pk_mul_f32 v[232:233], v[232:233], s[76:77] op_sel_hi:[1,0]\n"
    "v_pk_mul_f32 v[234:235], v[234:235], s[76:77] op_sel_hi:[1,0]\n"
    "v_cvt_pk_bf16_f32 v232, v232, v233\n"
    "v_cvt_pk_bf16_f32 v233, v234, v235\n"
    "v_pk_mul_f32 v[236:237], v[236:237], s[76:77] op_sel_hi:[1,0]\n"
    "v_pk_mul_f32 v[238:239], v[238:239], s[76:77] op_sel_hi:[1,0]\n"
    "v_cvt_pk_bf16_f32 v236, v236, v237\n"
    "v_cvt_pk_bf16_f32 v237, v238, v239\n"
    "ds_write_b64 v253, v[224:225] offset:5632\n"
    "ds_write_b64 v253, v[228:229] offset:9728\n"
    "ds_write_b64 v253, v[232:233] offset:13824\n"
    "ds_write_b64 v253, v[236:237] offset:17920\n"
    "s_add_u32 m0, s63, 0x14000\n"
    "s_add_u32 s68, s63, 0x4000\n"
    "buffer_load_dwordx4 v152, s[40:43], s68 offen nt lds\n"
    "buffer_load_dwordx4 v153, s[40:43], s68 offen offset:1024 nt lds\n"
    "buffer_load_dwordx4 v152, s[40:43], s68 offen offset:2048 nt lds\n"
    "buffer_load_dwordx4 v153, s[40:43], s68 offen offset:3072 nt lds\n"
    "s_waitcnt vmcnt(12) lgkmcnt(0)\n"
    "s_barrier\n"
    "ds_read_b128 a[64:67], v140\n"
    "ds_read_b128 a[0:3], v254\n"
    "ds_read_b128 a[68:71], v141\n"
    "ds_read_b128 a[4:7], v254 offset:1024\n"
    "ds_read_b128 a[72:75], v140 offset:1024\n"
    "ds_read_b128 a[8:11], v254 offset:2048\n"
    "ds_read_b128 a[76:79], v141 offset:1024\n"
    "ds_read_b128 a[12:15], v254 offset:3072\n"
    "ds_read_b128 a[16:19], v254 offset:4096\n"
    "ds_read_b128 a[20:23], v254 offset:5120\n"
    "ds_read_b128 a[24:27], v254 offset:6144\n"
    "ds_read_b128 a[28:31], v254 offset:7168\n"
    "ds_read_b128 a[32:35], v255\n"
    "ds_read_b128 a[36:39], v255 offset:1024\n"
    "ds_read_b128 a[40:43], v255 offset:2048\n"
    "s_waitcnt lgkmcnt(14)\n"
    "ds_read_b128 a[44:47], v255 offset:3072\n"
    "s_waitcnt lgkmcnt(14)\n"
    "v_mfma_f32_16x16x32_bf16 v[96:99], a[64:67], a[0:3], 0\n"
    "s_waitcnt lgkmcnt(12)\n"
    "v_mfma_f32_16x16x32_bf16 v[96:99], a[68:71], a[4:7], v[96:99]\n"
    "s_waitcnt lgkmcnt(10)\n"
    "v_mfma_f32_16x16x32_bf16 v[96:99], a[72:75], a[8:11], v[96:99]\n"
    "s_waitcnt lgkmcnt(8)\n"
    "v_mfma_f32_16x16x32_bf16 v[96:99], a[76:79], a[12:15], v[96:99]\n"
    "ds_read_b128 a[80:83], v140 offset:4096\n"
    "s_waitcnt lgkmcnt(8)\n"
    "v_mfma_f32_16x16x32_bf16 v[104:107], a[64:67], a[16:19], 0\n"
    "ds_read_b128 a[84:87], v141 offset:4096\n"
    "s_waitcnt lgkmcnt(8)\n"
    "v_mfma_f32_16x16x32_bf16 v[104:107], a[68:71], a[20:23], v[104:107]\n"
    "ds_read_b128 a[88:91], v140 offset:5120\n"
    "s_waitcnt lgkmcnt(8)\n"
    "v_mfma_f32_16x16x32_bf16 v[104:107], a[72:75], a[24:27], v[104:107]\n"
    "ds_read_b128 a[92:95], v141 offset:5120\n"
    "s_waitcnt lgkmcnt(8)\n"
    "v_mfma_f32_16x16x32_bf16 v[104:107], a[76:79], a[28:31], v[104:107]\n"
    "ds_read_b128 a[96:99], v142\n"
    "s_waitcnt lgkmcnt(8)\n"
    "v_mfma_f32_16x16x32_bf16 v[112:115], a[64:67], a[32:35], 0\n"
    "ds_read_b128 a[112:115], v142 offset:4096\n"
    "s_waitcnt lgkmcnt(8)\n"
    "v_mfma_f32_16x16x32_bf16 v[112:115], a[68:71], a[36:39], v[112:115]\n"
    "ds_read_b128 a[100:103], v143\n"
    "s_waitcnt lgkmcnt(7)\n"
    "v_mfma_f32_16x16x32_bf16 v[112:115], a[72:75], a[40:43], v[112:115]\n"
    "ds_read_b128 a[116:119], v143 offset:4096\n"
    "v_mfma_f32_16x16x32_bf16 v[112:115], a[76:79], a[44:47], v[112:115]\n"
    "v_max3_f32 v96, v96, v97, v98\n"
    "v_max3_f32 v104, v104, v105, v106\n"
    "v_max_f32_e64 v96, v96, v99\n"
    "v_max_f32_e64 v104, v104, v107\n"
    "s_nop 1\n"
    "s_nop 1\n"
    "v_max3_f32 v112, v112, v113, v114\n"
    "v_max_f32_e64 v112, v112, v115\n"
    "v_mov_b32_e64 v97, v96\n"
    "v_mov_b32_e64 v105, v104\n"
    "v_mov_b32_e64 v113, v112\n"
    "v_permlane32_swap_b32 v96, v97\n"
    "v_permlane32_swap_b32 v104, v105\n"
    "v_permlane32_swap_b32 v112, v113\n"
    "v_max_f32_e32 v96, v96, v97\n"
    "v_max_f32_e64 v104, v104, v105\n"
    "v_max_f32_e64 v112, v112, v113\n"
    "v_mov_b32_e64 v97, v96\n"
    "v_mov_b32_e64 v105, v104\n"
    "v_mov_b32_e64 v113, v112\n"
    "v_permlane16_swap_b32 v96, v97\n"
    "v_permlane16_swap_b32 v104, v105\n"
    "v_permlane16_swap_b32 v112, v113\n"
    "v_max_f32_e32 v96, v96, v97\n"
    "v_max_f32_e64 v104, v104, v105\n"
    "v_max_f32_e64 v112, v112, v113\n"
    "v_sub_f32_e64 v128, 0, v96\n"
    "v_sub_f32_e64 v129, 0, v96\n"
    "v_sub_f32_e64 v130, 0, v96\n"
    "v_sub_f32_e64 v131, 0, v96\n"
    "v_sub_f32_e64 v132, 0, v104\n"
    "v_sub_f32_e64 v133, 0, v104\n"
    "v_sub_f32_e64 v134, 0, v104\n"
    "v_sub_f32_e64 v135, 0, v104\n"
    "v_sub_f32_e64 v136, 0, v112\n"
    "v_sub_f32_e64 v137, 0, v112\n"
    "v_sub_f32_e64 v138, 0, v112\n"
    "v_sub_f32_e64 v139, 0, v112\n"
    "s_add_u32 m0, s63, 0x8000\n"
    "s_add_u32 s68, s63, 0x8000\n"
    "buffer_load_dwordx4 v152, s[36:39], s68 offen nt lds\n"
    "buffer_load_dwordx4 v153, s[36:39], s68 offen offset:1024 nt lds\n"
    "buffer_load_dwordx4 v152, s[36:39], s68 offen offset:2048 nt lds\n"
    "buffer_load_dwordx4 v153, s[36:39], s68 offen offset:3072 nt lds\n"
    "s_waitcnt vmcnt(12) lgkmcnt(0)\n"
    "s_barrier\n"
    "L_att:\n"
    "v_mfma_f32_16x16x32_bf16 v[96:99], a[64:67], a[0:3], v[128:131]\n"
    "ds_read_b64_tr_b16 a[128:129], v144\n"
    "ds_read_b64_tr_b16 a[130:131], v144 offset:4096\n"
    "v_mfma_f32_16x16x32_bf16 v[96:99], a[68:71], a[4:7], v[96:99]\n"
    "ds_read_b64_tr_b16 a[132:133], v145\n"
    "ds_read_b64_tr_b16 a[134:135], v145 offset:4096\n"
    "v_mfma_f32_16x16x32_bf16 v[96:99], a[72:75], a[8:11], v[96:99]\n"
    "ds_read_b64_tr_b16 a[136:137], v146\n"
    "ds_read_b64_tr_b16 a[138:139], v146 offset:4096\n"
    "v_mfma_f32_16x16x32_bf16 v[96:99], a[76:79], a[12:15], v[96:99]\n"
    "ds_read_b64_tr_b16 a[140:141], v147\n"
    "ds_read_b64_tr_b16 a[142:143], v147 offset:4096\n"
    "v_mfma_f32_16x16x32_bf16 v[100:103], a[80:83], a[0:3], v[128:131]\n"
    "ds_read_b64_tr_b16 a[144:145], v144 offset:1024\n"
    "ds_read_b64_tr_b16 a[146:147], v144 offset:5120\n"
    "v_mfma_f32_16x16x32_bf16 v[100:103], a[84:87], a[4:7], v[100:103]\n"
    "ds_read_b64_tr_b16 a[148:149], v145 offset:1024\n"
    "ds_read_b64_tr_b16 a[150:151], v145 offset:5120\n"
    "v_mfma_f32_16x16x32_bf16 v[100:103], a[88:91], a[8:11], v[100:103]\n"
    "ds_read_b64_tr_b16 a[152:153], v146 offset:1024\n"
    "ds_read_b64_tr_b16 a[154:155], v146 offset:5120\n"
    "v_mfma_f32_16x16x32_bf16 v[100:103], a[92:95], a[12:15], v[100:103]\n"
    "ds_read_b64_tr_b16 a[156:157], v147 offset:1024\n"
    "s_waitcnt lgkmcnt(14)\n"
    "ds_read_b64_tr_b16 a[158:159], v147 offset:5120\n"
    "v_mfma_f32_16x16x32_bf16 v[104:107], a[64:67], a[16:19], v[132:135]\n"
    "v_exp_f32_e32 v96, v96\n"
    "v_mfma_f32_16x16x32_bf16 v[104:107], a[68:71], a[20:23], v[104:107]\n"
    "v_exp_f32_e64 v97, v97\n"
    "v_mfma_f32_16x16x32_bf16 v[104:107], a[72:75], a[24:27], v[104:107]\n"
    "v_exp_f32_e64 v98, v98\n"
    "v_mfma_f32_16x16x32_bf16 v[104:107], a[76:79], a[28:31], v[104:107]\n"
    "v_exp_f32_e64 v99, v99\n"
    "v_mfma_f32_16x16x32_bf16 v[108:111], a[80:83], a[16:19], v[132:135]\n"
    "v_exp_f32_e64 v100, v100\n"
    "v_mfma_f32_16x16x32_bf16 v[108:111], a[84:87], a[20:23], v[108:111]\n"
    "v_exp_f32_e64 v101, v101\n"
    "v_mfma_f32_16x16x32_bf16 v[108:111], a[88:91], a[24:27], v[108:111]\n"
    "v_exp_f32_e64 v102, v102\n"
    "v_mfma_f32_16x16x32_bf16 v[108:111], a[92:95], a[28:31], v[108:111]\n"
    "v_exp_f32_e64 v103, v103\n"
    "v_cvt_pk_bf16_f32 v96, v96, v97\n"
    "v_cvt_pk_bf16_f32 v97, v98, v99\n"
    "v_cvt_pk_bf16_f32 v98, v100, v101\n"
    "v_cvt_pk_bf16_f32 v99, v102, v103\n"
    "v_mfma_f32_16x16x32_bf16 v[112:115], a[64:67], a[32:35], v[136:139]\n"
    "v_exp_f32_e64 v104, v104\n"
    "v_mfma_f32_16x16x32_bf16 v[112:115], a[68:71], a[36:39], v[112:115]\n"
    "s_waitcnt lgkmcnt(0)\n"
    "ds_read_b128 a[104:107], v142 offset:1024\n"
    "s_add_u32 m0, s63, 0x18000\n"
    "v_mfma_f32_16x16x32_bf16 v[112:115], a[72:75], a[40:43], v[112:115]\n"
    "v_exp_f32_e32 v105, v105\n"
    "v_mfma_f32_16x16x32_bf16 v[112:115], a[76:79], a[44:47], v[112:115]\n"
    "ds_read_b128 a[120:123], v142 offset:5120\n"
    "v_cvt_pk_bf16_f32 v104, v104, v105\n"
    "v_mfma_f32_16x16x32_bf16 v[0:3], a[128:131], v[96:99], v[0:3]\n"
    "v_exp_f32_e64 v106, v106\n"
    "v_mfma_f32_16x16x32_bf16 v[4:7], a[132:135], v[96:99], v[4:7]\n"
    "ds_read_b128 a[108:111], v143 offset:1024\n"
    "ds_read_b128 a[124:127], v143 offset:5120\n"
    "v_mfma_f32_16x16x32_bf16 v[8:11], a[136:139], v[96:99], v[8:11]\n"
    "v_exp_f32_e64 v107, v107\n"
    "v_mfma_f32_16x16x32_bf16 v[12:15], a[140:143], v[96:99], v[12:15]\n"
    "s_add_u32 s68, s63, 0x8000\n"
    "v_cvt_pk_bf16_f32 v105, v106, v107\n"
    "v_mfma_f32_16x16x32_bf16 v[116:119], a[80:83], a[32:35], v[136:139]\n"
    "v_exp_f32_e64 v108, v108\n"
    "v_mfma_f32_16x16x32_bf16 v[116:119], a[84:87], a[36:39], v[116:119]\n"
    "ds_read_b64_tr_b16 a[160:161], v148\n"
    "ds_read_b64_tr_b16 a[162:163], v148 offset:4096\n"
    "v_mfma_f32_16x16x32_bf16 v[116:119], a[88:91], a[40:43], v[116:119]\n"
    "v_exp_f32_e64 v109, v109\n"
    "v_mfma_f32_16x16x32_bf16 v[116:119], a[92:95], a[44:47], v[116:119]\n"
    "ds_read_b64_tr_b16 a[164:165], v149\n"
    "v_cvt_pk_bf16_f32 v106, v108, v109\n"
    "v_mfma_f32_16x16x32_bf16 v[16:19], a[144:147], v[96:99], v[16:19]\n"
    "v_exp_f32_e64 v110, v110\n"
    "v_mfma_f32_16x16x32_bf16 v[20:23], a[148:151], v[96:99], v[20:23]\n"
    "ds_read_b64_tr_b16 a[166:167], v149 offset:4096\n"
    "buffer_load_dwordx4 v152, s[40:43], s68 offen nt lds\n"
    "v_mfma_f32_16x16x32_bf16 v[24:27], a[152:155], v[96:99], v[24:27]\n"
    "v_exp_f32_e64 v111, v111\n"
    "v_mfma_f32_16x16x32_bf16 v[28:31], a[156:159], v[96:99], v[28:31]\n"
    "ds_read_b64_tr_b16 a[168:169], v150\n"
    "v_cvt_pk_bf16_f32 v107, v110, v111\n"
    "v_mfma_f32_16x16x32_bf16 a[48:51], a[60:63], v[96:99], a[48:51]\n"
    "ds_read_b64_tr_b16 a[170:171], v150 offset:4096\n"
    "ds_read_b64_tr_b16 a[172:173], v151\n"
    "buffer_load_dwordx4 v153, s[40:43], s68 offen offset:1024 nt lds\n"
    "buffer_load_dwordx4 v152, s[40:43], s68 offen offset:2048 nt lds\n"
    "buffer_load_dwordx4 v153, s[40:43], s68 offen offset:3072 nt lds\n"
    "s_memrealtime s[78:79]\n"
    "s_add_u32 s99, s99, s62\n"
    "s_waitcnt lgkmcnt(0)\n"
    "s_sub_u32 s67, s78, s99\n"
    "s_cmp_lt_u32 s67, 0x80000000\n"
    "s_cbranch_scc1 L_rt0x\n"
    "L_rt0:\n"
    "s_sleep 1\n"
    "s_add_u32 s67, s67, 4\n"
    "s_cbranch_scc0 L_rt0\n"
    "L_rt0x:\n"
    "s_waitcnt vmcnt(8)\n"
    "s_barrier\n"
    "v_mfma_f32_16x16x32_bf16 v[120:123], a[96:99], a[0:3], v[128:131]\n"
    "v_exp_f32_e32 v112, v112\n"
    "v_mfma_f32_16x16x32_bf16 v[120:123], a[100:103], a[4:7], v[120:123]\n"
    "ds_read_b128 a[64:67], v140 offset:16384\n"
    "s_add_u32 m0, s63, 0xc000\n"
    "v_mfma_f32_16x16x32_bf16 v[120:123], a[104:107], a[8:11], v[120:123]\n"
    "v_exp_f32_e64 v113, v113\n"
    "v_mfma_f32_16x16x32_bf16 v[120:123], a[108:111], a[12:15], v[120:123]\n"
    "ds_read_b128 a[80:83], v140 offset:20480\n"
    "v_cvt_pk_bf16_f32 v112, v112, v113\n"
    "v_mfma_f32_16x16x32_bf16 v[32:35], a[128:131], v[104:107], v[32:35]\n"
    "v_exp_f32_e64 v114, v114\n"
    "v_mfma_f32_16x16x32_bf16 v[36:39], a[132:135], v[104:107], v[36:39]\n"
    "ds_read_b128 a[68:71], v141 offset:16384\n"
    "v_mfma_f32_16x16x32_bf16 v[40:43], a[136:139], v[104:107], v[40:43]\n"
    "v_exp_f32_e64 v115, v115\n"
    "v_mfma_f32_16x16x32_bf16 v[44:47], a[140:143], v[104:107], v[44:47]\n"
    "s_add_u32 s68, s63, 0xc000\n"
    "v_cvt_pk_bf16_f32 v113, v114, v115\n"
    "v_mfma_f32_16x16x32_bf16 v[124:127], a[112:115], a[0:3], v[128:131]\n"
    "v_exp_f32_e64 v116, v116\n"
    "v_mfma_f32_16x16x32_bf16 v[124:127], a[116:119], a[4:7], v[124:127]\n"
    "ds_read_b128 a[84:87], v141 offset:20480\n"
    "ds_read_b64_tr_b16 a[174:175], v151 offset:4096\n"
    "v_mfma_f32_16x16x32_bf16 v[124:127], a[120:123], a[8:11], v[124:127]\n"
    "v_exp_f32_e64 v117, v117\n"
    "v_mfma_f32_16x16x32_bf16 v[124:127], a[124:127], a[12:15], v[124:127]\n"
    "ds_read_b64_tr_b16 a[176:177], v148 offset:1024\n"
    "v_cvt_pk_bf16_f32 v114, v116, v117\n"
    "v_mfma_f32_16x16x32_bf16 v[48:51], a[144:147], v[104:107], v[48:51]\n"
    "v_exp_f32_e64 v118, v118\n"
    "v_mfma_f32_16x16x32_bf16 v[52:55], a[148:151], v[104:107], v[52:55]\n"
    "buffer_load_dwordx4 v152, s[36:39], s68 offen nt lds\n"
    "v_mfma_f32_16x16x32_bf16 v[56:59], a[152:155], v[104:107], v[56:59]\n"
    "v_exp_f32_e64 v119, v119\n"
    "v_mfma_f32_16x16x32_bf16 v[60:63], a[156:159], v[104:107], v[60:63]\n"
    "ds_read_b64_tr_b16 a[178:179], v148 offset:5120\n"
    "v_cvt_pk_bf16_f32 v115, v118, v119\n"
    "v_mfma_f32_16x16x32_bf16 a[52:55], a[60:63], v[104:107], a[52:55]\n"
    "ds_read_b64_tr_b16 a[180:181], v149 offset:1024\n"
    "v_mfma_f32_16x16x32_bf16 v[96:99], a[96:99], a[16:19], v[132:135]\n"
    "v_exp_f32_e64 v120, v120\n"
    "v_mfma_f32_16x16x32_bf16 v[96:99], a[100:103], a[20:23], v[96:99]\n"
    "ds_read_b128 a[72:75], v140 offset:17408\n"
    "buffer_load_dwordx4 v153, s[36:39], s68 offen offset:1024 nt lds\n"
    "v_mfma_f32_16x16x32_bf16 v[96:99], a[104:107], a[24:27], v[96:99]\n"
    "v_exp_f32_e64 v121, v121\n"
    "v_mfma_f32_16x16x32_bf16 v[96:99], a[108:111], a[28:31], v[96:99]\n"
    "ds_read_b128 a[88:91], v140 offset:21504\n"
    "v_cvt_pk_bf16_f32 v120, v120, v121\n"
    "v_mfma_f32_16x16x32_bf16 v[64:67], a[128:131], v[112:115], v[64:67]\n"
    "v_exp_f32_e64 v122, v122\n"
    "v_mfma_f32_16x16x32_bf16 v[68:71], a[132:135], v[112:115], v[68:71]\n"
    "ds_read_b128 a[76:79], v141 offset:17408\n"
    "v_mfma_f32_16x16x32_bf16 v[72:75], a[136:139], v[112:115], v[72:75]\n"
    "v_exp_f32_e64 v123, v123\n"
    "v_mfma_f32_16x16x32_bf16 v[76:79], a[140:143], v[112:115], v[76:79]\n"
    "ds_read_b128 a[92:95], v141 offset:21504\n"
    "v_cvt_pk_bf16_f32 v121, v122, v123\n"
    "v_mfma_f32_16x16x32_bf16 v[100:103], a[112:115], a[16:19], v[132:135]\n"
    "v_exp_f32_e64 v124, v124\n"
    "v_mfma_f32_16x16x32_bf16 v[100:103], a[116:119], a[20:23], v[100:103]\n"
    "ds_read_b64_tr_b16 a[182:183], v149 offset:5120\n"
    "v_mfma_f32_16x16x32_bf16 v[100:103], a[120:123], a[24:27], v[100:103]\n"
    "v_exp_f32_e64 v125, v125\n"
    "v_mfma_f32_16x16x32_bf16 v[100:103], a[124:127], a[28:31], v[100:103]\n"
    "ds_read_b64_tr_b16 a[184:185], v150 offset:1024\n"
    "v_cvt_pk_bf16_f32 v122, v124, v125\n"
    "v_mfma_f32_16x16x32_bf16 v[80:83], a[144:147], v[112:115], v[80:83]\n"
    "v_exp_f32_e64 v126, v126\n"
    "v_mfma_f32_16x16x32_bf16 v[84:87], a[148:151], v[112:115], v[84:87]\n"
    "ds_read_b64_tr_b16 a[186:187], v150 offset:5120\n"
    "v_mfma_f32_16x16x32_bf16 v[88:91], a[152:155], v[112:115], v[88:91]\n"
    "v_exp_f32_e64 v127, v127\n"
    "v_mfma_f32_16x16x32_bf16 v[92:95], a[156:159], v[112:115], v[92:95]\n"
    "s_waitcnt lgkmcnt(3)\n"
    "s_nop 0\n"
    "ds_read_b64_tr_b16 a[188:189], v151 offset:1024\n"
    "v_cvt_pk_bf16_f32 v123, v126, v127\n"
    "v_mfma_f32_16x16x32_bf16 a[56:59], a[60:63], v[112:115], a[56:59]\n"
    "ds_read_b64_tr_b16 a[190:191], v151 offset:5120\n"
    "v_mfma_f32_16x16x32_bf16 v[104:107], a[64:67], a[0:3], v[128:131]\n"
    "v_exp_f32_e64 v96, v96\n"
    "v_mfma_f32_16x16x32_bf16 v[104:107], a[68:71], a[4:7], v[104:107]\n"
    "ds_read_b64_tr_b16 a[128:129], v144 offset:16384\n"
    "buffer_load_dwordx4 v152, s[36:39], s68 offen offset:2048 nt lds\n"
    "v_mfma_f32_16x16x32_bf16 v[104:107], a[72:75], a[8:11], v[104:107]\n"
    "v_exp_f32_e64 v97, v97\n"
    "v_mfma_f32_16x16x32_bf16 v[104:107], a[76:79], a[12:15], v[104:107]\n"
    "ds_read_b64_tr_b16 a[130:131], v144 offset:20480\n"
    "v_cvt_pk_bf16_f32 v96, v96, v97\n"
    "v_mfma_f32_16x16x32_bf16 v[0:3], a[160:163], v[120:123], v[0:3]\n"
    "v_exp_f32_e64 v98, v98\n"
    "v_mfma_f32_16x16x32_bf16 v[4:7], a[164:167], v[120:123], v[4:7]\n"
    "ds_read_b64_tr_b16 a[132:133], v145 offset:16384\n"
    "v_mfma_f32_16x16x32_bf16 v[8:11], a[168:171], v[120:123], v[8:11]\n"
    "v_exp_f32_e64 v99, v99\n"
    "v_mfma_f32_16x16x32_bf16 v[12:15], a[172:175], v[120:123], v[12:15]\n"
    "ds_read_b64_tr_b16 a[134:135], v145 offset:20480\n"
    "v_cvt_pk_bf16_f32 v97, v98, v99\n"
    "v_mfma_f32_16x16x32_bf16 v[108:111], a[80:83], a[0:3], v[128:131]\n"
    "v_exp_f32_e64 v100, v100\n"
    "v_mfma_f32_16x16x32_bf16 v[108:111], a[84:87], a[4:7], v[108:111]\n"
    "ds_read_b64_tr_b16 a[136:137], v146 offset:16384\n"
    "ds_read_b64_tr_b16 a[138:139], v146 offset:20480\n"
    "v_mfma_f32_16x16x32_bf16 v[108:111], a[88:91], a[8:11], v[108:111]\n"
    "v_exp_f32_e64 v101, v101\n"
    "v_mfma_f32_16x16x32_bf16 v[108:111], a[92:95], a[12:15], v[108:111]\n"
    "ds_read_b64_tr_b16 a[140:141], v147 offset:16384\n"
    "v_cvt_pk_bf16_f32 v98, v100, v101\n"
    "v_mfma_f32_16x16x32_bf16 v[16:19], a[176:179], v[120:123], v[16:19]\n"
    "v_exp_f32_e32 v102, v102\n"
    "s_waitcnt lgkmcnt(4)\n"
    "v_mfma_f32_16x16x32_bf16 v[20:23], a[180:183], v[120:123], v[20:23]\n"
    "ds_read_b64_tr_b16 a[142:143], v147 offset:20480\n"
    "v_mfma_f32_16x16x32_bf16 v[24:27], a[184:187], v[120:123], v[24:27]\n"
    "v_exp_f32_e64 v103, v103\n"
    "v_mfma_f32_16x16x32_bf16 v[28:31], a[188:191], v[120:123], v[28:31]\n"
    "ds_read_b128 a[96:99], v142 offset:16384\n"
    "v_cvt_pk_bf16_f32 v99, v102, v103\n"
    "v_mfma_f32_16x16x32_bf16 a[48:51], a[60:63], v[120:123], a[48:51]\n"
    "ds_read_b128 a[112:115], v142 offset:20480\n"
    "v_mfma_f32_16x16x32_bf16 v[112:115], a[64:67], a[16:19], v[132:135]\n"
    "v_exp_f32_e64 v104, v104\n"
    "v_mfma_f32_16x16x32_bf16 v[112:115], a[68:71], a[20:23], v[112:115]\n"
    "ds_read_b64_tr_b16 a[144:145], v144 offset:17408\n"
    "buffer_load_dwordx4 v153, s[36:39], s68 offen offset:3072 nt lds\n"
    "v_mfma_f32_16x16x32_bf16 v[112:115], a[72:75], a[24:27], v[112:115]\n"
    "v_exp_f32_e64 v105, v105\n"
    "v_mfma_f32_16x16x32_bf16 v[112:115], a[76:79], a[28:31], v[112:115]\n"
    "ds_read_b64_tr_b16 a[146:147], v144 offset:21504\n"
    "v_cvt_pk_bf16_f32 v104, v104, v105\n"
    "v_mfma_f32_16x16x32_bf16 v[32:35], a[160:163], v[96:99], v[32:35]\n"
    "v_exp_f32_e64 v106, v106\n"
    "v_mfma_f32_16x16x32_bf16 v[36:39], a[164:167], v[96:99], v[36:39]\n"
    "ds_read_b64_tr_b16 a[148:149], v145 offset:17408\n"
    "v_mfma_f32_16x16x32_bf16 v[40:43], a[168:171], v[96:99], v[40:43]\n"
    "v_exp_f32_e64 v107, v107\n"
    "v_mfma_f32_16x16x32_bf16 v[44:47], a[172:175], v[96:99], v[44:47]\n"
    "ds_read_b64_tr_b16 a[150:151], v145 offset:21504\n"
    "v_cvt_pk_bf16_f32 v105, v106, v107\n"
    "v_mfma_f32_16x16x32_bf16 v[116:119], a[80:83], a[16:19], v[132:135]\n"
    "v_exp_f32_e64 v108, v108\n"
    "v_mfma_f32_16x16x32_bf16 v[116:119], a[84:87], a[20:23], v[116:119]\n"
    "ds_read_b64_tr_b16 a[152:153], v146 offset:17408\n"
    "ds_read_b64_tr_b16 a[154:155], v146 offset:21504\n"
    "v_mfma_f32_16x16x32_bf16 v[116:119], a[88:91], a[24:27], v[116:119]\n"
    "v_exp_f32_e64 v109, v109\n"
    "v_mfma_f32_16x16x32_bf16 v[116:119], a[92:95], a[28:31], v[116:119]\n"
    "ds_read_b64_tr_b16 a[156:157], v147 offset:17408\n"
    "v_cvt_pk_bf16_f32 v106, v108, v109\n"
    "v_mfma_f32_16x16x32_bf16 v[48:51], a[176:179], v[96:99], v[48:51]\n"
    "v_exp_f32_e64 v110, v110\n"
    "v_mfma_f32_16x16x32_bf16 v[52:55], a[180:183], v[96:99], v[52:55]\n"
    "ds_read_b64_tr_b16 a[158:159], v147 offset:21504\n"
    "v_mfma_f32_16x16x32_bf16 v[56:59], a[184:187], v[96:99], v[56:59]\n"
    "v_exp_f32_e64 v111, v111\n"
    "v_mfma_f32_16x16x32_bf16 v[60:63], a[188:191], v[96:99], v[60:63]\n"
    "s_waitcnt lgkmcnt(4)\n"
    "s_nop 0\n"
    "ds_read_b128 a[100:103], v143 offset:16384\n"
    "v_cvt_pk_bf16_f32 v107, v110, v111\n"
    "v_mfma_f32_16x16x32_bf16 a[52:55], a[60:63], v[96:99], a[52:55]\n"
    "ds_read_b128 a[116:119], v143 offset:20480\n"
    "v_mfma_f32_16x16x32_bf16 v[120:123], a[64:67], a[32:35], v[136:139]\n"
    "v_exp_f32_e64 v112, v112\n"
    "v_mfma_f32_16x16x32_bf16 v[120:123], a[68:71], a[36:39], v[120:123]\n"
    "ds_read_b128 a[104:107], v142 offset:17408\n"
    "s_add_u32 m0, s63, 0x1c000\n"
    "v_mfma_f32_16x16x32_bf16 v[120:123], a[72:75], a[40:43], v[120:123]\n"
    "v_exp_f32_e64 v113, v113\n"
    "v_mfma_f32_16x16x32_bf16 v[120:123], a[76:79], a[44:47], v[120:123]\n"
    "ds_read_b128 a[120:123], v142 offset:21504\n"
    "v_cvt_pk_bf16_f32 v112, v112, v113\n"
    "v_mfma_f32_16x16x32_bf16 v[0:3], a[128:131], v[104:107], v[0:3]\n"
    "v_exp_f32_e64 v114, v114\n"
    "v_mfma_f32_16x16x32_bf16 v[4:7], a[132:135], v[104:107], v[4:7]\n"
    "ds_read_b128 a[108:111], v143 offset:17408\n"
    "ds_read_b128 a[124:127], v143 offset:21504\n"
    "v_mfma_f32_16x16x32_bf16 v[8:11], a[136:139], v[104:107], v[8:11]\n"
    "v_exp_f32_e64 v115, v115\n"
    "v_mfma_f32_16x16x32_bf16 v[12:15], a[140:143], v[104:107], v[12:15]\n"
    "s_add_u32 s68, s63, 0xc000\n"
    "v_cvt_pk_bf16_f32 v113, v114, v115\n"
    "v_mfma_f32_16x16x32_bf16 v[124:127], a[80:83], a[32:35], v[136:139]\n"
    "v_exp_f32_e64 v116, v116\n"
    "v_mfma_f32_16x16x32_bf16 v[124:127], a[84:87], a[36:39], v[124:127]\n"
    "ds_read_b64_tr_b16 a[160:161], v148 offset:16384\n"
    "ds_read_b64_tr_b16 a[162:163], v148 offset:20480\n"
    "v_mfma_f32_16x16x32_bf16 v[124:127], a[88:91], a[40:43], v[124:127]\n"
    "v_exp_f32_e64 v117, v117\n"
    "v_mfma_f32_16x16x32_bf16 v[124:127], a[92:95], a[44:47], v[124:127]\n"
    "ds_read_b64_tr_b16 a[164:165], v149 offset:16384\n"
    "v_cvt_pk_bf16_f32 v114, v116, v117\n"
    "v_mfma_f32_16x16x32_bf16 v[16:19], a[144:147], v[104:107], v[16:19]\n"
    "v_exp_f32_e64 v118, v118\n"
    "v_mfma_f32_16x16x32_bf16 v[20:23], a[148:151], v[104:107], v[20:23]\n"
    "ds_read_b64_tr_b16 a[166:167], v149 offset:20480\n"
    "buffer_load_dwordx4 v152, s[40:43], s68 offen nt lds\n"
    "s_waitcnt lgkmcnt(4)\n"
    "v_mfma_f32_16x16x32_bf16 v[24:27], a[152:155], v[104:107], v[24:27]\n"
    "v_exp_f32_e32 v119, v119\n"
    "v_mfma_f32_16x16x32_bf16 v[28:31], a[156:159], v[104:107], v[28:31]\n"
    "ds_read_b64_tr_b16 a[168:169], v150 offset:16384\n"
    "v_cvt_pk_bf16_f32 v115, v118, v119\n"
    "v_mfma_f32_16x16x32_bf16 a[48:51], a[60:63], v[104:107], a[48:51]\n"
    "ds_read_b64_tr_b16 a[170:171], v150 offset:20480\n"
    "ds_read_b64_tr_b16 a[172:173], v151 offset:16384\n"
    "buffer_load_dwordx4 v153, s[40:43], s68 offen offset:1024 nt lds\n"
    "buffer_load_dwordx4 v152, s[40:43], s68 offen offset:2048 nt lds\n"
    "buffer_load_dwordx4 v153, s[40:43], s68 offen offset:3072 nt lds\n"
    "s_memrealtime s[78:79]\n"
    "s_add_u32 s99, s99, s62\n"
    "s_waitcnt lgkmcnt(0)\n"
    "s_sub_u32 s67, s78, s99\n"
    "s_cmp_lt_u32 s67, 0x80000000\n"
    "s_cbranch_scc1 L_rt1x\n"
    "L_rt1:\n"
    "s_sleep 1\n"
    "s_add_u32 s67, s67, 4\n"
    "s_cbranch_scc0 L_rt1\n"
    "L_rt1x:\n"
    "s_waitcnt vmcnt(8)\n"
    "s_barrier\n"
    "v_mfma_f32_16x16x32_bf16 v[96:99], a[96:99], a[0:3], v[128:131]\n"
    "v_exp_f32_e32 v120, v120\n"
    "v_mfma_f32_16x16x32_bf16 v[96:99], a[100:103], a[4:7], v[96:99]\n"
    "ds_read_b128 a[64:67], v140 offset:32768\n"
    "s_add_u32 m0, s63, 0\n"
    "v_mfma_f32_16x16x32_bf16 v[96:99], a[104:107], a[8:11], v[96:99]\n"
    "v_exp_f32_e32 v121, v121\n"
    "v_mfma_f32_16x16x32_bf16 v[96:99], a[108:111], a[12:15], v[96:99]\n"
    "ds_read_b128 a[80:83], v140 offset:36864\n"
    "v_cvt_pk_bf16_f32 v120, v120, v121\n"
    "v_mfma_f32_16x16x32_bf16 v[32:35], a[128:131], v[112:115], v[32:35]\n"
    "v_exp_f32_e64 v122, v122\n"
    "v_mfma_f32_16x16x32_bf16 v[36:39], a[132:135], v[112:115], v[36:39]\n"
    "ds_read_b128 a[68:71], v141 offset:32768\n"
    "v_mfma_f32_16x16x32_bf16 v[40:43], a[136:139], v[112:115], v[40:43]\n"
    "v_exp_f32_e64 v123, v123\n"
    "v_mfma_f32_16x16x32_bf16 v[44:47], a[140:143], v[112:115], v[44:47]\n"
    "s_add_u32 s68, s63, 0x10000\n"
    "v_cvt_pk_bf16_f32 v121, v122, v123\n"
    "v_mfma_f32_16x16x32_bf16 v[100:103], a[112:115], a[0:3], v[128:131]\n"
    "v_exp_f32_e64 v124, v124\n"
    "v_mfma_f32_16x16x32_bf16 v[100:103], a[116:119], a[4:7], v[100:103]\n"
    "ds_read_b128 a[84:87], v141 offset:36864\n"
    "ds_read_b64_tr_b16 a[174:175], v151 offset:20480\n"
    "v_mfma_f32_16x16x32_bf16 v[100:103], a[120:123], a[8:11], v[100:103]\n"
    "v_exp_f32_e64 v125, v125\n"
    "v_mfma_f32_16x16x32_bf16 v[100:103], a[124:127], a[12:15], v[100:103]\n"
    "ds_read_b64_tr_b16 a[176:177], v148 offset:17408\n"
    "v_cvt_pk_bf16_f32 v122, v124, v125\n"
    "v_mfma_f32_16x16x32_bf16 v[48:51], a[144:147], v[112:115], v[48:51]\n"
    "v_exp_f32_e64 v126, v126\n"
    "v_mfma_f32_16x16x32_bf16 v[52:55], a[148:151], v[112:115], v[52:55]\n"
    "buffer_load_dwordx4 v152, s[36:39], s68 offen nt lds\n"
    "v_mfma_f32_16x16x32_bf16 v[56:59], a[152:155], v[112:115], v[56:59]\n"
    "v_exp_f32_e64 v127, v127\n"
    "v_mfma_f32_16x16x32_bf16 v[60:63], a[156:159], v[112:115], v[60:63]\n"
    "ds_read_b64_tr_b16 a[178:179], v148 offset:21504\n"
    "v_cvt_pk_bf16_f32 v123, v126, v127\n"
    "v_mfma_f32_16x16x32_bf16 a[52:55], a[60:63], v[112:115], a[52:55]\n"
    "ds_read_b64_tr_b16 a[180:181], v149 offset:17408\n"
    "v_mfma_f32_16x16x32_bf16 v[104:107], a[96:99], a[16:19], v[132:135]\n"
    "v_exp_f32_e64 v96, v96\n"
    "v_mfma_f32_16x16x32_bf16 v[104:107], a[100:103], a[20:23], v[104:107]\n"
    "ds_read_b128 a[72:75], v140 offset:33792\n"
    "buffer_load_dwordx4 v153, s[36:39], s68 offen offset:1024 nt lds\n"
    "v_mfma_f32_16x16x32_bf16 v[104:107], a[104:107], a[24:27], v[104:107]\n"
    "v_exp_f32_e64 v97, v97\n"
    "v_mfma_f32_16x16x32_bf16 v[104:107], a[108:111], a[28:31], v[104:107]\n"
    "ds_read_b128 a[88:91], v140 offset:37888\n"
    "v_cvt_pk_bf16_f32 v96, v96, v97\n"
    "v_mfma_f32_16x16x32_bf16 v[64:67], a[128:131], v[120:123], v[64:67]\n"
    "v_exp_f32_e64 v98, v98\n"
    "v_mfma_f32_16x16x32_bf16 v[68:71], a[132:135], v[120:123], v[68:71]\n"
    "ds_read_b128 a[76:79], v141 offset:33792\n"
    "v_mfma_f32_16x16x32_bf16 v[72:75], a[136:139], v[120:123], v[72:75]\n"
    "v_exp_f32_e64 v99, v99\n"
    "v_mfma_f32_16x16x32_bf16 v[76:79], a[140:143], v[120:123], v[76:79]\n"
    "ds_read_b128 a[92:95], v141 offset:37888\n"
    "v_cvt_pk_bf16_f32 v97, v98, v99\n"
    "v_mfma_f32_16x16x32_bf16 v[108:111], a[112:115], a[16:19], v[132:135]\n"
    "v_exp_f32_e64 v100, v100\n"
    "v_mfma_f32_16x16x32_bf16 v[108:111], a[116:119], a[20:23], v[108:111]\n"
    "ds_read_b64_tr_b16 a[182:183], v149 offset:21504\n"
    "v_mfma_f32_16x16x32_bf16 v[108:111], a[120:123], a[24:27], v[108:111]\n"
    "v_exp_f32_e64 v101, v101\n"
    "v_mfma_f32_16x16x32_bf16 v[108:111], a[124:127], a[28:31], v[108:111]\n"
    "ds_read_b64_tr_b16 a[184:185], v150 offset:17408\n"
    "v_cvt_pk_bf16_f32 v98, v100, v101\n"
    "v_mfma_f32_16x16x32_bf16 v[80:83], a[144:147], v[120:123], v[80:83]\n"
    "v_exp_f32_e64 v102, v102\n"
    "v_mfma_f32_16x16x32_bf16 v[84:87], a[148:151], v[120:123], v[84:87]\n"
    "ds_read_b64_tr_b16 a[186:187], v150 offset:21504\n"
    "v_mfma_f32_16x16x32_bf16 v[88:91], a[152:155], v[120:123], v[88:91]\n"
    "v_exp_f32_e64 v103, v103\n"
    "v_mfma_f32_16x16x32_bf16 v[92:95], a[156:159], v[120:123], v[92:95]\n"
    "s_waitcnt lgkmcnt(3)\n"
    "s_nop 0\n"
    "ds_read_b64_tr_b16 a[188:189], v151 offset:17408\n"
    "v_cvt_pk_bf16_f32 v99, v102, v103\n"
    "v_mfma_f32_16x16x32_bf16 a[56:59], a[60:63], v[120:123], a[56:59]\n"
    "ds_read_b64_tr_b16 a[190:191], v151 offset:21504\n"
    "v_mfma_f32_16x16x32_bf16 v[112:115], a[64:67], a[0:3], v[128:131]\n"
    "v_exp_f32_e64 v104, v104\n"
    "v_mfma_f32_16x16x32_bf16 v[112:115], a[68:71], a[4:7], v[112:115]\n"
    "ds_read_b64_tr_b16 a[128:129], v144 offset:32768\n"
    "buffer_load_dwordx4 v152, s[36:39], s68 offen offset:2048 nt lds\n"
    "v_mfma_f32_16x16x32_bf16 v[112:115], a[72:75], a[8:11], v[112:115]\n"
    "v_exp_f32_e64 v105, v105\n"
    "v_mfma_f32_16x16x32_bf16 v[112:115], a[76:79], a[12:15], v[112:115]\n"
    "ds_read_b64_tr_b16 a[130:131], v144 offset:36864\n"
    "v_cvt_pk_bf16_f32 v104, v104, v105\n"
    "v_mfma_f32_16x16x32_bf16 v[0:3], a[160:163], v[96:99], v[0:3]\n"
    "v_exp_f32_e64 v106, v106\n"
    "v_mfma_f32_16x16x32_bf16 v[4:7], a[164:167], v[96:99], v[4:7]\n"
    "ds_read_b64_tr_b16 a[132:133], v145 offset:32768\n"
    "v_mfma_f32_16x16x32_bf16 v[8:11], a[168:171], v[96:99], v[8:11]\n"
    "v_exp_f32_e64 v107, v107\n"
    "v_mfma_f32_16x16x32_bf16 v[12:15], a[172:175], v[96:99], v[12:15]\n"
    "ds_read_b64_tr_b16 a[134:135], v145 offset:36864\n"
    "v_cvt_pk_bf16_f32 v105, v106, v107\n"
    "v_mfma_f32_16x16x32_bf16 v[116:119], a[80:83], a[0:3], v[128:131]\n"
    "v_exp_f32_e64 v108, v108\n"
    "v_mfma_f32_16x16x32_bf16 v[116:119], a[84:87], a[4:7], v[116:119]\n"
    "ds_read_b64_tr_b16 a[136:137], v146 offset:32768\n"
    "ds_read_b64_tr_b16 a[138:139], v146 offset:36864\n"
    "v_mfma_f32_16x16x32_bf16 v[116:119], a[88:91], a[8:11], v[116:119]\n"
    "v_exp_f32_e64 v109, v109\n"
    "v_mfma_f32_16x16x32_bf16 v[116:119], a[92:95], a[12:15], v[116:119]\n"
    "ds_read_b64_tr_b16 a[140:141], v147 offset:32768\n"
    "v_cvt_pk_bf16_f32 v106, v108, v109\n"
    "v_mfma_f32_16x16x32_bf16 v[16:19], a[176:179], v[96:99], v[16:19]\n"
    "v_exp_f32_e32 v110, v110\n"
    "s_waitcnt lgkmcnt(4)\n"
    "v_mfma_f32_16x16x32_bf16 v[20:23], a[180:183], v[96:99], v[20:23]\n"
    "ds_read_b64_tr_b16 a[142:143], v147 offset:36864\n"
    "v_mfma_f32_16x16x32_bf16 v[24:27], a[184:187], v[96:99], v[24:27]\n"
    "v_exp_f32_e64 v111, v111\n"
    "v_mfma_f32_16x16x32_bf16 v[28:31], a[188:191], v[96:99], v[28:31]\n"
    "ds_read_b128 a[96:99], v142 offset:32768\n"
    "v_cvt_pk_bf16_f32 v107, v110, v111\n"
    "v_mfma_f32_16x16x32_bf16 a[48:51], a[60:63], v[96:99], a[48:51]\n"
    "ds_read_b128 a[112:115], v142 offset:36864\n"
    "v_mfma_f32_16x16x32_bf16 v[120:123], a[64:67], a[16:19], v[132:135]\n"
    "v_exp_f32_e64 v112, v112\n"
    "v_mfma_f32_16x16x32_bf16 v[120:123], a[68:71], a[20:23], v[120:123]\n"
    "ds_read_b64_tr_b16 a[144:145], v144 offset:33792\n"
    "buffer_load_dwordx4 v153, s[36:39], s68 offen offset:3072 nt lds\n"
    "v_mfma_f32_16x16x32_bf16 v[120:123], a[72:75], a[24:27], v[120:123]\n"
    "v_exp_f32_e64 v113, v113\n"
    "v_mfma_f32_16x16x32_bf16 v[120:123], a[76:79], a[28:31], v[120:123]\n"
    "ds_read_b64_tr_b16 a[146:147], v144 offset:37888\n"
    "v_cvt_pk_bf16_f32 v112, v112, v113\n"
    "v_mfma_f32_16x16x32_bf16 v[32:35], a[160:163], v[104:107], v[32:35]\n"
    "v_exp_f32_e64 v114, v114\n"
    "v_mfma_f32_16x16x32_bf16 v[36:39], a[164:167], v[104:107], v[36:39]\n"
    "ds_read_b64_tr_b16 a[148:149], v145 offset:33792\n"
    "v_mfma_f32_16x16x32_bf16 v[40:43], a[168:171], v[104:107], v[40:43]\n"
    "v_exp_f32_e64 v115, v115\n"
    "v_mfma_f32_16x16x32_bf16 v[44:47], a[172:175], v[104:107], v[44:47]\n"
    "ds_read_b64_tr_b16 a[150:151], v145 offset:37888\n"
    "v_cvt_pk_bf16_f32 v113, v114, v115\n"
    "v_mfma_f32_16x16x32_bf16 v[124:127], a[80:83], a[16:19], v[132:135]\n"
    "v_exp_f32_e64 v116, v116\n"
    "v_mfma_f32_16x16x32_bf16 v[124:127], a[84:87], a[20:23], v[124:127]\n"
    "ds_read_b64_tr_b16 a[152:153], v146 offset:33792\n"
    "ds_read_b64_tr_b16 a[154:155], v146 offset:37888\n"
    "v_mfma_f32_16x16x32_bf16 v[124:127], a[88:91], a[24:27], v[124:127]\n"
    "v_exp_f32_e64 v117, v117\n"
    "v_mfma_f32_16x16x32_bf16 v[124:127], a[92:95], a[28:31], v[124:127]\n"
    "ds_read_b64_tr_b16 a[156:157], v147 offset:33792\n"
    "v_cvt_pk_bf16_f32 v114, v116, v117\n"
    "v_mfma_f32_16x16x32_bf16 v[48:51], a[176:179], v[104:107], v[48:51]\n"
    "v_exp_f32_e64 v118, v118\n"
    "v_mfma_f32_16x16x32_bf16 v[52:55], a[180:183], v[104:107], v[52:55]\n"
    "ds_read_b64_tr_b16 a[158:159], v147 offset:37888\n"
    "v_mfma_f32_16x16x32_bf16 v[56:59], a[184:187], v[104:107], v[56:59]\n"
    "v_exp_f32_e64 v119, v119\n"
    "v_mfma_f32_16x16x32_bf16 v[60:63], a[188:191], v[104:107], v[60:63]\n"
    "s_waitcnt lgkmcnt(4)\n"
    "s_nop 0\n"
    "ds_read_b128 a[100:103], v143 offset:32768\n"
    "v_cvt_pk_bf16_f32 v115, v118, v119\n"
    "v_mfma_f32_16x16x32_bf16 a[52:55], a[60:63], v[104:107], a[52:55]\n"
    "ds_read_b128 a[116:119], v143 offset:36864\n"
    "v_mfma_f32_16x16x32_bf16 v[96:99], a[64:67], a[32:35], v[136:139]\n"
    "v_exp_f32_e64 v120, v120\n"
    "v_mfma_f32_16x16x32_bf16 v[96:99], a[68:71], a[36:39], v[96:99]\n"
    "ds_read_b128 a[104:107], v142 offset:33792\n"
    "s_add_u32 m0, s63, 0x10000\n"
    "v_mfma_f32_16x16x32_bf16 v[96:99], a[72:75], a[40:43], v[96:99]\n"
    "v_exp_f32_e64 v121, v121\n"
    "v_mfma_f32_16x16x32_bf16 v[96:99], a[76:79], a[44:47], v[96:99]\n"
    "ds_read_b128 a[120:123], v142 offset:37888\n"
    "v_cvt_pk_bf16_f32 v120, v120, v121\n"
    "v_mfma_f32_16x16x32_bf16 v[0:3], a[128:131], v[112:115], v[0:3]\n"
    "v_exp_f32_e64 v122, v122\n"
    "v_mfma_f32_16x16x32_bf16 v[4:7], a[132:135], v[112:115], v[4:7]\n"
    "ds_read_b128 a[108:111], v143 offset:33792\n"
    "ds_read_b128 a[124:127], v143 offset:37888\n"
    "v_mfma_f32_16x16x32_bf16 v[8:11], a[136:139], v[112:115], v[8:11]\n"
    "v_exp_f32_e64 v123, v123\n"
    "v_mfma_f32_16x16x32_bf16 v[12:15], a[140:143], v[112:115], v[12:15]\n"
    "s_add_u32 s68, s63, 0x10000\n"
    "v_cvt_pk_bf16_f32 v121, v122, v123\n"
    "v_mfma_f32_16x16x32_bf16 v[100:103], a[80:83], a[32:35], v[136:139]\n"
    "v_exp_f32_e64 v124, v124\n"
    "v_mfma_f32_16x16x32_bf16 v[100:103], a[84:87], a[36:39], v[100:103]\n"
    "ds_read_b64_tr_b16 a[160:161], v148 offset:32768\n"
    "ds_read_b64_tr_b16 a[162:163], v148 offset:36864\n"
    "v_mfma_f32_16x16x32_bf16 v[100:103], a[88:91], a[40:43], v[100:103]\n"
    "v_exp_f32_e64 v125, v125\n"
    "v_mfma_f32_16x16x32_bf16 v[100:103], a[92:95], a[44:47], v[100:103]\n"
    "ds_read_b64_tr_b16 a[164:165], v149 offset:32768\n"
    "v_cvt_pk_bf16_f32 v122, v124, v125\n"
    "v_mfma_f32_16x16x32_bf16 v[16:19], a[144:147], v[112:115], v[16:19]\n"
    "v_exp_f32_e64 v126, v126\n"
    "v_mfma_f32_16x16x32_bf16 v[20:23], a[148:151], v[112:115], v[20:23]\n"
    "ds_read_b64_tr_b16 a[166:167], v149 offset:36864\n"
    "buffer_load_dwordx4 v152, s[40:43], s68 offen nt lds\n"
    "s_waitcnt lgkmcnt(4)\n"
    "v_mfma_f32_16x16x32_bf16 v[24:27], a[152:155], v[112:115], v[24:27]\n"
    "v_exp_f32_e32 v127, v127\n"
    "v_mfma_f32_16x16x32_bf16 v[28:31], a[156:159], v[112:115], v[28:31]\n"
    "ds_read_b64_tr_b16 a[168:169], v150 offset:32768\n"
    "v_cvt_pk_bf16_f32 v123, v126, v127\n"
    "v_mfma_f32_16x16x32_bf16 a[48:51], a[60:63], v[112:115], a[48:51]\n"
    "ds_read_b64_tr_b16 a[170:171], v150 offset:36864\n"
    "ds_read_b64_tr_b16 a[172:173], v151 offset:32768\n"
    "buffer_load_dwordx4 v153, s[40:43], s68 offen offset:1024 nt lds\n"
    "buffer_load_dwordx4 v152, s[40:43], s68 offen offset:2048 nt lds\n"
    "buffer_load_dwordx4 v153, s[40:43], s68 offen offset:3072 nt lds\n"
    "s_memrealtime s[78:79]\n"
    "s_add_u32 s99, s99, s62\n"
    "s_waitcnt lgkmcnt(0)\n"
    "s_sub_u32 s67, s78, s99\n"
    "s_cmp_lt_u32 s67, 0x80000000\n"
    "s_cbranch_scc1 L_rt2x\n"
    "L_rt2:\n"
    "s_sleep 1\n"
    "s_add_u32 s67, s67, 4\n"
    "s_cbranch_scc0 L_rt2\n"
    "L_rt2x:\n"
    "s_waitcnt vmcnt(8)\n"
    "s_barrier\n"
    "v_mfma_f32_16x16x32_bf16 v[104:107], a[96:99], a[0:3], v[128:131]\n"
    "v_exp_f32_e32 v96, v96\n"
    "v_mfma_f32_16x16x32_bf16 v[104:107], a[100:103], a[4:7], v[104:107]\n"
    "ds_read_b128 a[64:67], v140 offset:49152\n"
    "s_add_u32 m0, s63, 0x4000\n"
    "v_mfma_f32_16x16x32_bf16 v[104:107], a[104:107], a[8:11], v[104:107]\n"
    "v_exp_f32_e64 v97, v97\n"
    "v_mfma_f32_16x16x32_bf16 v[104:107], a[108:111], a[12:15], v[104:107]\n"
    "ds_read_b128 a[80:83], v140 offset:53248\n"
    "v_cvt_pk_bf16_f32 v96, v96, v97\n"
    "v_mfma_f32_16x16x32_bf16 v[32:35], a[128:131], v[120:123], v[32:35]\n"
    "v_exp_f32_e64 v98, v98\n"
    "v_mfma_f32_16x16x32_bf16 v[36:39], a[132:135], v[120:123], v[36:39]\n"
    "ds_read_b128 a[68:71], v141 offset:49152\n"
    "v_mfma_f32_16x16x32_bf16 v[40:43], a[136:139], v[120:123], v[40:43]\n"
    "v_exp_f32_e64 v99, v99\n"
    "v_mfma_f32_16x16x32_bf16 v[44:47], a[140:143], v[120:123], v[44:47]\n"
    "s_add_u32 s68, s63, 0x14000\n"
    "v_cvt_pk_bf16_f32 v97, v98, v99\n"
    "v_mfma_f32_16x16x32_bf16 v[108:111], a[112:115], a[0:3], v[128:131]\n"
    "v_exp_f32_e64 v100, v100\n"
    "v_mfma_f32_16x16x32_bf16 v[108:111], a[116:119], a[4:7], v[108:111]\n"
    "ds_read_b128 a[84:87], v141 offset:53248\n"
    "ds_read_b64_tr_b16 a[174:175], v151 offset:36864\n"
    "v_mfma_f32_16x16x32_bf16 v[108:111], a[120:123], a[8:11], v[108:111]\n"
    "v_exp_f32_e64 v101, v101\n"
    "v_mfma_f32_16x16x32_bf16 v[108:111], a[124:127], a[12:15], v[108:111]\n"
    "ds_read_b64_tr_b16 a[176:177], v148 offset:33792\n"
    "v_cvt_pk_bf16_f32 v98, v100, v101\n"
    "v_mfma_f32_16x16x32_bf16 v[48:51], a[144:147], v[120:123], v[48:51]\n"
    "v_exp_f32_e64 v102, v102\n"
    "v_mfma_f32_16x16x32_bf16 v[52:55], a[148:151], v[120:123], v[52:55]\n"
    "buffer_load_dwordx4 v152, s[36:39], s68 offen nt lds\n"
    "v_mfma_f32_16x16x32_bf16 v[56:59], a[152:155], v[120:123], v[56:59]\n"
    "v_exp_f32_e64 v103, v103\n"
    "v_mfma_f32_16x16x32_bf16 v[60:63], a[156:159], v[120:123], v[60:63]\n"
    "ds_read_b64_tr_b16 a[178:179], v148 offset:37888\n"
    "v_cvt_pk_bf16_f32 v99, v102, v103\n"
    "v_mfma_f32_16x16x32_bf16 a[52:55], a[60:63], v[120:123], a[52:55]\n"
    "ds_read_b64_tr_b16 a[180:181], v149 offset:33792\n"
    "v_mfma_f32_16x16x32_bf16 v[112:115], a[96:99], a[16:19], v[132:135]\n"
    "v_exp_f32_e64 v104, v104\n"
    "v_mfma_f32_16x16x32_bf16 v[112:115], a[100:103], a[20:23], v[112:115]\n"
    "ds_read_b128 a[72:75], v140 offset:50176\n"
    "buffer_load_dwordx4 v153, s[36:39], s68 offen offset:1024 nt lds\n"
    "v_mfma_f32_16x16x32_bf16 v[112:115], a[104:107], a[24:27], v[112:115]\n"
    "v_exp_f32_e64 v105, v105\n"
    "v_mfma_f32_16x16x32_bf16 v[112:115], a[108:111], a[28:31], v[112:115]\n"
    "ds_read_b128 a[88:91], v140 offset:54272\n"
    "v_cvt_pk_bf16_f32 v104, v104, v105\n"
    "v_mfma_f32_16x16x32_bf16 v[64:67], a[128:131], v[96:99], v[64:67]\n"
    "v_exp_f32_e64 v106, v106\n"
    "v_mfma_f32_16x16x32_bf16 v[68:71], a[132:135], v[96:99], v[68:71]\n"
    "ds_read_b128 a[76:79], v141 offset:50176\n"
    "v_mfma_f32_16x16x32_bf16 v[72:75], a[136:139], v[96:99], v[72:75]\n"
    "v_exp_f32_e64 v107, v107\n"
    "v_mfma_f32_16x16x32_bf16 v[76:79], a[140:143], v[96:99], v[76:79]\n"
    "ds_read_b128 a[92:95], v141 offset:54272\n"
    "v_cvt_pk_bf16_f32 v105, v106, v107\n"
    "v_mfma_f32_16x16x32_bf16 v[116:119], a[112:115], a[16:19], v[132:135]\n"
    "v_exp_f32_e64 v108, v108\n"
    "v_mfma_f32_16x16x32_bf16 v[116:119], a[116:119], a[20:23], v[116:119]\n"
    "ds_read_b64_tr_b16 a[182:183], v149 offset:37888\n"
    "v_mfma_f32_16x16x32_bf16 v[116:119], a[120:123], a[24:27], v[116:119]\n"
    "v_exp_f32_e64 v109, v109\n"
    "v_mfma_f32_16x16x32_bf16 v[116:119], a[124:127], a[28:31], v[116:119]\n"
    "ds_read_b64_tr_b16 a[184:185], v150 offset:33792\n"
    "v_cvt_pk_bf16_f32 v106, v108, v109\n"
    "v_mfma_f32_16x16x32_bf16 v[80:83], a[144:147], v[96:99], v[80:83]\n"
    "v_exp_f32_e64 v110, v110\n"
    "v_mfma_f32_16x16x32_bf16 v[84:87], a[148:151], v[96:99], v[84:87]\n"
    "ds_read_b64_tr_b16 a[186:187], v150 offset:37888\n"
    "v_mfma_f32_16x16x32_bf16 v[88:91], a[152:155], v[96:99], v[88:91]\n"
    "v_exp_f32_e64 v111, v111\n"
    "v_mfma_f32_16x16x32_bf16 v[92:95], a[156:159], v[96:99], v[92:95]\n"
    "s_waitcnt lgkmcnt(3)\n"
    "s_nop 0\n"
    "ds_read_b64_tr_b16 a[188:189], v151 offset:33792\n"
    "v_cvt_pk_bf16_f32 v107, v110, v111\n"
    "v_mfma_f32_16x16x32_bf16 a[56:59], a[60:63], v[96:99], a[56:59]\n"
    "ds_read_b64_tr_b16 a[190:191], v151 offset:37888\n"
    "v_mfma_f32_16x16x32_bf16 v[120:123], a[64:67], a[0:3], v[128:131]\n"
    "v_exp_f32_e64 v112, v112\n"
    "v_mfma_f32_16x16x32_bf16 v[120:123], a[68:71], a[4:7], v[120:123]\n"
    "ds_read_b64_tr_b16 a[128:129], v144 offset:49152\n"
    "buffer_load_dwordx4 v152, s[36:39], s68 offen offset:2048 nt lds\n"
    "v_mfma_f32_16x16x32_bf16 v[120:123], a[72:75], a[8:11], v[120:123]\n"
    "v_exp_f32_e64 v113, v113\n"
    "v_mfma_f32_16x16x32_bf16 v[120:123], a[76:79], a[12:15], v[120:123]\n"
    "ds_read_b64_tr_b16 a[130:131], v144 offset:53248\n"
    "v_cvt_pk_bf16_f32 v112, v112, v113\n"
    "v_mfma_f32_16x16x32_bf16 v[0:3], a[160:163], v[104:107], v[0:3]\n"
    "v_exp_f32_e64 v114, v114\n"
    "v_mfma_f32_16x16x32_bf16 v[4:7], a[164:167], v[104:107], v[4:7]\n"
    "ds_read_b64_tr_b16 a[132:133], v145 offset:49152\n"
    "v_mfma_f32_16x16x32_bf16 v[8:11], a[168:171], v[104:107], v[8:11]\n"
    "v_exp_f32_e64 v115, v115\n"
    "v_mfma_f32_16x16x32_bf16 v[12:15], a[172:175], v[104:107], v[12:15]\n"
    "ds_read_b64_tr_b16 a[134:135], v145 offset:53248\n"
    "v_cvt_pk_bf16_f32 v113, v114, v115\n"
    "v_mfma_f32_16x16x32_bf16 v[124:127], a[80:83], a[0:3], v[128:131]\n"
    "v_exp_f32_e64 v116, v116\n"
    "v_mfma_f32_16x16x32_bf16 v[124:127], a[84:87], a[4:7], v[124:127]\n"
    "ds_read_b64_tr_b16 a[136:137], v146 offset:49152\n"
    "ds_read_b64_tr_b16 a[138:139], v146 offset:53248\n"
    "v_mfma_f32_16x16x32_bf16 v[124:127], a[88:91], a[8:11], v[124:127]\n"
    "v_exp_f32_e64 v117, v117\n"
    "v_mfma_f32_16x16x32_bf16 v[124:127], a[92:95], a[12:15], v[124:127]\n"
    "ds_read_b64_tr_b16 a[140:141], v147 offset:49152\n"
    "v_cvt_pk_bf16_f32 v114, v116, v117\n"
    "v_mfma_f32_16x16x32_bf16 v[16:19], a[176:179], v[104:107], v[16:19]\n"
    "v_exp_f32_e32 v118, v118\n"
    "s_waitcnt lgkmcnt(4)\n"
    "v_mfma_f32_16x16x32_bf16 v[20:23], a[180:183], v[104:107], v[20:23]\n"
    "ds_read_b64_tr_b16 a[142:143], v147 offset:53248\n"
    "v_mfma_f32_16x16x32_bf16 v[24:27], a[184:187], v[104:107], v[24:27]\n"
    "v_exp_f32_e64 v119, v119\n"
    "v_mfma_f32_16x16x32_bf16 v[28:31], a[188:191], v[104:107], v[28:31]\n"
    "ds_read_b128 a[96:99], v142 offset:49152\n"
    "v_cvt_pk_bf16_f32 v115, v118, v119\n"
    "v_mfma_f32_16x16x32_bf16 a[48:51], a[60:63], v[104:107], a[48:51]\n"
    "ds_read_b128 a[112:115], v142 offset:53248\n"
    "v_mfma_f32_16x16x32_bf16 v[96:99], a[64:67], a[16:19], v[132:135]\n"
    "v_exp_f32_e64 v120, v120\n"
    "v_mfma_f32_16x16x32_bf16 v[96:99], a[68:71], a[20:23], v[96:99]\n"
    "ds_read_b64_tr_b16 a[144:145], v144 offset:50176\n"
    "buffer_load_dwordx4 v153, s[36:39], s68 offen offset:3072 nt lds\n"
    "v_mfma_f32_16x16x32_bf16 v[96:99], a[72:75], a[24:27], v[96:99]\n"
    "v_exp_f32_e64 v121, v121\n"
    "v_mfma_f32_16x16x32_bf16 v[96:99], a[76:79], a[28:31], v[96:99]\n"
    "ds_read_b64_tr_b16 a[146:147], v144 offset:54272\n"
    "v_cvt_pk_bf16_f32 v120, v120, v121\n"
    "v_mfma_f32_16x16x32_bf16 v[32:35], a[160:163], v[112:115], v[32:35]\n"
    "v_exp_f32_e64 v122, v122\n"
    "v_mfma_f32_16x16x32_bf16 v[36:39], a[164:167], v[112:115], v[36:39]\n"
    "ds_read_b64_tr_b16 a[148:149], v145 offset:50176\n"
    "v_mfma_f32_16x16x32_bf16 v[40:43], a[168:171], v[112:115], v[40:43]\n"
    "v_exp_f32_e64 v123, v123\n"
    "v_mfma_f32_16x16x32_bf16 v[44:47], a[172:175], v[112:115], v[44:47]\n"
    "ds_read_b64_tr_b16 a[150:151], v145 offset:54272\n"
    "v_cvt_pk_bf16_f32 v121, v122, v123\n"
    "v_mfma_f32_16x16x32_bf16 v[100:103], a[80:83], a[16:19], v[132:135]\n"
    "v_exp_f32_e64 v124, v124\n"
    "v_mfma_f32_16x16x32_bf16 v[100:103], a[84:87], a[20:23], v[100:103]\n"
    "ds_read_b64_tr_b16 a[152:153], v146 offset:50176\n"
    "ds_read_b64_tr_b16 a[154:155], v146 offset:54272\n"
    "v_mfma_f32_16x16x32_bf16 v[100:103], a[88:91], a[24:27], v[100:103]\n"
    "v_exp_f32_e64 v125, v125\n"
    "v_mfma_f32_16x16x32_bf16 v[100:103], a[92:95], a[28:31], v[100:103]\n"
    "ds_read_b64_tr_b16 a[156:157], v147 offset:50176\n"
    "v_cvt_pk_bf16_f32 v122, v124, v125\n"
    "v_mfma_f32_16x16x32_bf16 v[48:51], a[176:179], v[112:115], v[48:51]\n"
    "v_exp_f32_e64 v126, v126\n"
    "v_mfma_f32_16x16x32_bf16 v[52:55], a[180:183], v[112:115], v[52:55]\n"
    "ds_read_b64_tr_b16 a[158:159], v147 offset:54272\n"
    "v_mfma_f32_16x16x32_bf16 v[56:59], a[184:187], v[112:115], v[56:59]\n"
    "v_exp_f32_e64 v127, v127\n"
    "v_mfma_f32_16x16x32_bf16 v[60:63], a[188:191], v[112:115], v[60:63]\n"
    "s_waitcnt lgkmcnt(4)\n"
    "s_nop 0\n"
    "ds_read_b128 a[100:103], v143 offset:49152\n"
    "v_cvt_pk_bf16_f32 v123, v126, v127\n"
    "v_mfma_f32_16x16x32_bf16 a[52:55], a[60:63], v[112:115], a[52:55]\n"
    "ds_read_b128 a[116:119], v143 offset:53248\n"
    "v_mfma_f32_16x16x32_bf16 v[104:107], a[64:67], a[32:35], v[136:139]\n"
    "v_exp_f32_e64 v96, v96\n"
    "v_mfma_f32_16x16x32_bf16 v[104:107], a[68:71], a[36:39], v[104:107]\n"
    "ds_read_b128 a[104:107], v142 offset:50176\n"
    "s_add_u32 m0, s63, 0x14000\n"
    "v_mfma_f32_16x16x32_bf16 v[104:107], a[72:75], a[40:43], v[104:107]\n"
    "v_exp_f32_e64 v97, v97\n"
    "v_mfma_f32_16x16x32_bf16 v[104:107], a[76:79], a[44:47], v[104:107]\n"
    "ds_read_b128 a[120:123], v142 offset:54272\n"
    "v_cvt_pk_bf16_f32 v96, v96, v97\n"
    "v_mfma_f32_16x16x32_bf16 v[0:3], a[128:131], v[120:123], v[0:3]\n"
    "v_exp_f32_e64 v98, v98\n"
    "v_mfma_f32_16x16x32_bf16 v[4:7], a[132:135], v[120:123], v[4:7]\n"
    "ds_read_b128 a[108:111], v143 offset:50176\n"
    "ds_read_b128 a[124:127], v143 offset:54272\n"
    "v_mfma_f32_16x16x32_bf16 v[8:11], a[136:139], v[120:123], v[8:11]\n"
    "v_exp_f32_e64 v99, v99\n"
    "v_mfma_f32_16x16x32_bf16 v[12:15], a[140:143], v[120:123], v[12:15]\n"
    "s_add_u32 s68, s63, 0x14000\n"
    "v_cvt_pk_bf16_f32 v97, v98, v99\n"
    "v_mfma_f32_16x16x32_bf16 v[108:111], a[80:83], a[32:35], v[136:139]\n"
    "v_exp_f32_e64 v100, v100\n"
    "v_mfma_f32_16x16x32_bf16 v[108:111], a[84:87], a[36:39], v[108:111]\n"
    "ds_read_b64_tr_b16 a[160:161], v148 offset:49152\n"
    "ds_read_b64_tr_b16 a[162:163], v148 offset:53248\n"
    "v_mfma_f32_16x16x32_bf16 v[108:111], a[88:91], a[40:43], v[108:111]\n"
    "v_exp_f32_e64 v101, v101\n"
    "v_mfma_f32_16x16x32_bf16 v[108:111], a[92:95], a[44:47], v[108:111]\n"
    "ds_read_b64_tr_b16 a[164:165], v149 offset:49152\n"
    "v_cvt_pk_bf16_f32 v98, v100, v101\n"
    "v_mfma_f32_16x16x32_bf16 v[16:19], a[144:147], v[120:123], v[16:19]\n"
    "v_exp_f32_e64 v102, v102\n"
    "v_mfma_f32_16x16x32_bf16 v[20:23], a[148:151], v[120:123], v[20:23]\n"
    "ds_read_b64_tr_b16 a[166:167], v149 offset:53248\n"
    "buffer_load_dwordx4 v152, s[40:43], s68 offen nt lds\n"
    "s_waitcnt lgkmcnt(4)\n"
    "v_mfma_f32_16x16x32_bf16 v[24:27], a[152:155], v[120:123], v[24:27]\n"
    "v_exp_f32_e32 v103, v103\n"
    "v_mfma_f32_16x16x32_bf16 v[28:31], a[156:159], v[120:123], v[28:31]\n"
    "ds_read_b64_tr_b16 a[168:169], v150 offset:49152\n"
    "v_cvt_pk_bf16_f32 v99, v102, v103\n"
    "v_mfma_f32_16x16x32_bf16 a[48:51], a[60:63], v[120:123], a[48:51]\n"
    "ds_read_b64_tr_b16 a[170:171], v150 offset:53248\n"
    "ds_read_b64_tr_b16 a[172:173], v151 offset:49152\n"
    "buffer_load_dwordx4 v153, s[40:43], s68 offen offset:1024 nt lds\n"
    "buffer_load_dwordx4 v152, s[40:43], s68 offen offset:2048 nt lds\n"
    "buffer_load_dwordx4 v153, s[40:43], s68 offen offset:3072 nt lds\n"
    "s_memrealtime s[78:79]\n"
    "s_add_u32 s99, s99, s62\n"
    "s_waitcnt lgkmcnt(0)\n"
    "s_sub_u32 s67, s78, s99\n"
    "s_cmp_lt_u32 s67, 0x80000000\n"
    "s_cbranch_scc1 L_rt3x\n"
    "L_rt3:\n"
    "s_sleep 1\n"
    "s_add_u32 s67, s67, 4\n"
    "s_cbranch_scc0 L_rt3\n"
    "L_rt3x:\n"
    "s_waitcnt vmcnt(8)\n"
    "s_barrier\n"
    "v_mfma_f32_16x16x32_bf16 v[112:115], a[96:99], a[0:3], v[128:131]\n"
    "v_exp_f32_e32 v104, v104\n"
    "v_mfma_f32_16x16x32_bf16 v[112:115], a[100:103], a[4:7], v[112:115]\n"
    "ds_read_b128 a[64:67], v140\n"
    "s_add_u32 m0, s63, 0x8000\n"
    "v_mfma_f32_16x16x32_bf16 v[112:115], a[104:107], a[8:11], v[112:115]\n"
    "v_exp_f32_e64 v105, v105\n"
    "v_mfma_f32_16x16x32_bf16 v[112:115], a[108:111], a[12:15], v[112:115]\n"
    "ds_read_b128 a[80:83], v140 offset:4096\n"
    "v_cvt_pk_bf16_f32 v104, v104, v105\n"
    "v_mfma_f32_16x16x32_bf16 v[32:35], a[128:131], v[96:99], v[32:35]\n"
    "v_exp_f32_e64 v106, v106\n"
    "v_mfma_f32_16x16x32_bf16 v[36:39], a[132:135], v[96:99], v[36:39]\n"
    "ds_read_b128 a[68:71], v141\n"
    "v_mfma_f32_16x16x32_bf16 v[40:43], a[136:139], v[96:99], v[40:43]\n"
    "v_exp_f32_e64 v107, v107\n"
    "v_mfma_f32_16x16x32_bf16 v[44:47], a[140:143], v[96:99], v[44:47]\n"
    "s_add_u32 s68, s63, 0x18000\n"
    "v_cvt_pk_bf16_f32 v105, v106, v107\n"
    "v_mfma_f32_16x16x32_bf16 v[116:119], a[112:115], a[0:3], v[128:131]\n"
    "v_exp_f32_e64 v108, v108\n"
    "v_mfma_f32_16x16x32_bf16 v[116:119], a[116:119], a[4:7], v[116:119]\n"
    "ds_read_b128 a[84:87], v141 offset:4096\n"
    "ds_read_b64_tr_b16 a[174:175], v151 offset:53248\n"
    "v_mfma_f32_16x16x32_bf16 v[116:119], a[120:123], a[8:11], v[116:119]\n"
    "v_exp_f32_e64 v109, v109\n"
    "v_mfma_f32_16x16x32_bf16 v[116:119], a[124:127], a[12:15], v[116:119]\n"
    "ds_read_b64_tr_b16 a[176:177], v148 offset:50176\n"
    "v_cvt_pk_bf16_f32 v106, v108, v109\n"
    "v_mfma_f32_16x16x32_bf16 v[48:51], a[144:147], v[96:99], v[48:51]\n"
    "v_exp_f32_e64 v110, v110\n"
    "v_mfma_f32_16x16x32_bf16 v[52:55], a[148:151], v[96:99], v[52:55]\n"
    "buffer_load_dwordx4 v152, s[36:39], s68 offen nt lds\n"
    "v_mfma_f32_16x16x32_bf16 v[56:59], a[152:155], v[96:99], v[56:59]\n"
    "v_exp_f32_e64 v111, v111\n"
    "v_mfma_f32_16x16x32_bf16 v[60:63], a[156:159], v[96:99], v[60:63]\n"
    "ds_read_b64_tr_b16 a[178:179], v148 offset:54272\n"
    "v_cvt_pk_bf16_f32 v107, v110, v111\n"
    "v_mfma_f32_16x16x32_bf16 a[52:55], a[60:63], v[96:99], a[52:55]\n"
    "ds_read_b64_tr_b16 a[180:181], v149 offset:50176\n"
    "v_mfma_f32_16x16x32_bf16 v[120:123], a[96:99], a[16:19], v[132:135]\n"
    "v_exp_f32_e64 v112, v112\n"
    "v_mfma_f32_16x16x32_bf16 v[120:123], a[100:103], a[20:23], v[120:123]\n"
    "ds_read_b128 a[72:75], v140 offset:1024\n"
    "buffer_load_dwordx4 v153, s[36:39], s68 offen offset:1024 nt lds\n"
    "v_mfma_f32_16x16x32_bf16 v[120:123], a[104:107], a[24:27], v[120:123]\n"
    "v_exp_f32_e64 v113, v113\n"
    "v_mfma_f32_16x16x32_bf16 v[120:123], a[108:111], a[28:31], v[120:123]\n"
    "ds_read_b128 a[88:91], v140 offset:5120\n"
    "v_cvt_pk_bf16_f32 v112, v112, v113\n"
    "v_mfma_f32_16x16x32_bf16 v[64:67], a[128:131], v[104:107], v[64:67]\n"
    "v_exp_f32_e64 v114, v114\n"
    "v_mfma_f32_16x16x32_bf16 v[68:71], a[132:135], v[104:107], v[68:71]\n"
    "ds_read_b128 a[76:79], v141 offset:1024\n"
    "v_mfma_f32_16x16x32_bf16 v[72:75], a[136:139], v[104:107], v[72:75]\n"
    "v_exp_f32_e64 v115, v115\n"
    "v_mfma_f32_16x16x32_bf16 v[76:79], a[140:143], v[104:107], v[76:79]\n"
    "ds_read_b128 a[92:95], v141 offset:5120\n"
    "v_cvt_pk_bf16_f32 v113, v114, v115\n"
    "v_mfma_f32_16x16x32_bf16 v[124:127], a[112:115], a[16:19], v[132:135]\n"
    "v_exp_f32_e64 v116, v116\n"
    "v_mfma_f32_16x16x32_bf16 v[124:127], a[116:119], a[20:23], v[124:127]\n"
    "ds_read_b64_tr_b16 a[182:183], v149 offset:54272\n"
    "v_mfma_f32_16x16x32_bf16 v[124:127], a[120:123], a[24:27], v[124:127]\n"
    "v_exp_f32_e64 v117, v117\n"
    "v_mfma_f32_16x16x32_bf16 v[124:127], a[124:127], a[28:31], v[124:127]\n"
    "ds_read_b64_tr_b16 a[184:185], v150 offset:50176\n"
    "v_cvt_pk_bf16_f32 v114, v116, v117\n"
    "v_mfma_f32_16x16x32_bf16 v[80:83], a[144:147], v[104:107], v[80:83]\n"
    "v_exp_f32_e64 v118, v118\n"
    "v_mfma_f32_16x16x32_bf16 v[84:87], a[148:151], v[104:107], v[84:87]\n"
    "ds_read_b64_tr_b16 a[186:187], v150 offset:54272\n"
    "v_mfma_f32_16x16x32_bf16 v[88:91], a[152:155], v[104:107], v[88:91]\n"
    "v_exp_f32_e64 v119, v119\n"
    "v_mfma_f32_16x16x32_bf16 v[92:95], a[156:159], v[104:107], v[92:95]\n"
    "s_waitcnt lgkmcnt(3)\n"
    "s_nop 0\n"
    "ds_read_b64_tr_b16 a[188:189], v151 offset:50176\n"
    "v_cvt_pk_bf16_f32 v115, v118, v119\n"
    "v_mfma_f32_16x16x32_bf16 a[56:59], a[60:63], v[104:107], a[56:59]\n"
    "ds_read_b64_tr_b16 a[190:191], v151 offset:54272\n"
    "v_mfma_f32_16x16x32_bf16 v[96:99], a[64:67], a[0:3], v[128:131]\n"
    "v_exp_f32_e64 v120, v120\n"
    "v_mfma_f32_16x16x32_bf16 v[96:99], a[68:71], a[4:7], v[96:99]\n"
    "ds_read_b64_tr_b16 a[128:129], v144\n"
    "buffer_load_dwordx4 v152, s[36:39], s68 offen offset:2048 nt lds\n"
    "v_mfma_f32_16x16x32_bf16 v[96:99], a[72:75], a[8:11], v[96:99]\n"
    "v_exp_f32_e64 v121, v121\n"
    "v_mfma_f32_16x16x32_bf16 v[96:99], a[76:79], a[12:15], v[96:99]\n"
    "ds_read_b64_tr_b16 a[130:131], v144 offset:4096\n"
    "v_cvt_pk_bf16_f32 v120, v120, v121\n"
    "v_mfma_f32_16x16x32_bf16 v[0:3], a[160:163], v[112:115], v[0:3]\n"
    "v_exp_f32_e64 v122, v122\n"
    "v_mfma_f32_16x16x32_bf16 v[4:7], a[164:167], v[112:115], v[4:7]\n"
    "ds_read_b64_tr_b16 a[132:133], v145\n"
    "v_mfma_f32_16x16x32_bf16 v[8:11], a[168:171], v[112:115], v[8:11]\n"
    "v_exp_f32_e64 v123, v123\n"
    "v_mfma_f32_16x16x32_bf16 v[12:15], a[172:175], v[112:115], v[12:15]\n"
    "ds_read_b64_tr_b16 a[134:135], v145 offset:4096\n"
    "v_cvt_pk_bf16_f32 v121, v122, v123\n"
    "v_mfma_f32_16x16x32_bf16 v[100:103], a[80:83], a[0:3], v[128:131]\n"
    "v_exp_f32_e64 v124, v124\n"
    "v_mfma_f32_16x16x32_bf16 v[100:103], a[84:87], a[4:7], v[100:103]\n"
    "ds_read_b64_tr_b16 a[136:137], v146\n"
    "ds_read_b64_tr_b16 a[138:139], v146 offset:4096\n"
    "v_mfma_f32_16x16x32_bf16 v[100:103], a[88:91], a[8:11], v[100:103]\n"
    "v_exp_f32_e64 v125, v125\n"
    "v_mfma_f32_16x16x32_bf16 v[100:103], a[92:95], a[12:15], v[100:103]\n"
    "ds_read_b64_tr_b16 a[140:141], v147\n"
    "v_cvt_pk_bf16_f32 v122, v124, v125\n"
    "v_mfma_f32_16x16x32_bf16 v[16:19], a[176:179], v[112:115], v[16:19]\n"
    "v_exp_f32_e32 v126, v126\n"
    "s_waitcnt lgkmcnt(4)\n"
    "v_mfma_f32_16x16x32_bf16 v[20:23], a[180:183], v[112:115], v[20:23]\n"
    "ds_read_b64_tr_b16 a[142:143], v147 offset:4096\n"
    "v_mfma_f32_16x16x32_bf16 v[24:27], a[184:187], v[112:115], v[24:27]\n"
    "v_exp_f32_e64 v127, v127\n"
    "v_mfma_f32_16x16x32_bf16 v[28:31], a[188:191], v[112:115], v[28:31]\n"
    "ds_read_b128 a[96:99], v142\n"
    "v_cvt_pk_bf16_f32 v123, v126, v127\n"
    "v_mfma_f32_16x16x32_bf16 a[48:51], a[60:63], v[112:115], a[48:51]\n"
    "ds_read_b128 a[112:115], v142 offset:4096\n"
    "v_mfma_f32_16x16x32_bf16 v[104:107], a[64:67], a[16:19], v[132:135]\n"
    "v_exp_f32_e64 v96, v96\n"
    "v_mfma_f32_16x16x32_bf16 v[104:107], a[68:71], a[20:23], v[104:107]\n"
    "ds_read_b64_tr_b16 a[144:145], v144 offset:1024\n"
    "buffer_load_dwordx4 v153, s[36:39], s68 offen offset:3072 nt lds\n"
    "v_mfma_f32_16x16x32_bf16 v[104:107], a[72:75], a[24:27], v[104:107]\n"
    "v_exp_f32_e64 v97, v97\n"
    "v_mfma_f32_16x16x32_bf16 v[104:107], a[76:79], a[28:31], v[104:107]\n"
    "ds_read_b64_tr_b16 a[146:147], v144 offset:5120\n"
    "v_cvt_pk_bf16_f32 v96, v96, v97\n"
    "v_mfma_f32_16x16x32_bf16 v[32:35], a[160:163], v[120:123], v[32:35]\n"
    "v_exp_f32_e64 v98, v98\n"
    "v_mfma_f32_16x16x32_bf16 v[36:39], a[164:167], v[120:123], v[36:39]\n"
    "ds_read_b64_tr_b16 a[148:149], v145 offset:1024\n"
    "v_mfma_f32_16x16x32_bf16 v[40:43], a[168:171], v[120:123], v[40:43]\n"
    "v_exp_f32_e64 v99, v99\n"
    "v_mfma_f32_16x16x32_bf16 v[44:47], a[172:175], v[120:123], v[44:47]\n"
    "ds_read_b64_tr_b16 a[150:151], v145 offset:5120\n"
    "v_cvt_pk_bf16_f32 v97, v98, v99\n"
    "v_mfma_f32_16x16x32_bf16 v[108:111], a[80:83], a[16:19], v[132:135]\n"
    "v_exp_f32_e64 v100, v100\n"
    "v_mfma_f32_16x16x32_bf16 v[108:111], a[84:87], a[20:23], v[108:111]\n"
    "ds_read_b64_tr_b16 a[152:153], v146 offset:1024\n"
    "ds_read_b64_tr_b16 a[154:155], v146 offset:5120\n"
    "v_mfma_f32_16x16x32_bf16 v[108:111], a[88:91], a[24:27], v[108:111]\n"
    "v_exp_f32_e64 v101, v101\n"
    "v_mfma_f32_16x16x32_bf16 v[108:111], a[92:95], a[28:31], v[108:111]\n"
    "ds_read_b64_tr_b16 a[156:157], v147 offset:1024\n"
    "v_cvt_pk_bf16_f32 v98, v100, v101\n"
    "v_mfma_f32_16x16x32_bf16 v[48:51], a[176:179], v[120:123], v[48:51]\n"
    "v_exp_f32_e64 v102, v102\n"
    "v_mfma_f32_16x16x32_bf16 v[52:55], a[180:183], v[120:123], v[52:55]\n"
    "ds_read_b64_tr_b16 a[158:159], v147 offset:5120\n"
    "v_mfma_f32_16x16x32_bf16 v[56:59], a[184:187], v[120:123], v[56:59]\n"
    "v_exp_f32_e64 v103, v103\n"
    "v_mfma_f32_16x16x32_bf16 v[60:63], a[188:191], v[120:123], v[60:63]\n"
    "s_waitcnt lgkmcnt(4)\n"
    "s_nop 0\n"
    "ds_read_b128 a[100:103], v143\n"
    "v_cvt_pk_bf16_f32 v99, v102, v103\n"
    "v_mfma_f32_16x16x32_bf16 a[52:55], a[60:63], v[120:123], a[52:55]\n"
    "ds_read_b128 a[116:119], v143 offset:4096\n"
    "v_mfma_f32_16x16x32_bf16 v[112:115], a[64:67], a[32:35], v[136:139]\n"
    "v_exp_f32_e64 v104, v104\n"
    "v_mfma_f32_16x16x32_bf16 v[112:115], a[68:71], a[36:39], v[112:115]\n"
    "ds_read_b128 a[104:107], v142 offset:1024\n"
    "s_add_u32 m0, s63, 0x18000\n"
    "v_mfma_f32_16x16x32_bf16 v[112:115], a[72:75], a[40:43], v[112:115]\n"
    "v_exp_f32_e64 v105, v105\n"
    "v_mfma_f32_16x16x32_bf16 v[112:115], a[76:79], a[44:47], v[112:115]\n"
    "ds_read_b128 a[120:123], v142 offset:5120\n"
    "v_cvt_pk_bf16_f32 v104, v104, v105\n"
    "v_mfma_f32_16x16x32_bf16 v[0:3], a[128:131], v[96:99], v[0:3]\n"
    "v_exp_f32_e64 v106, v106\n"
    "v_mfma_f32_16x16x32_bf16 v[4:7], a[132:135], v[96:99], v[4:7]\n"
    "ds_read_b128 a[108:111], v143 offset:1024\n"
    "ds_read_b128 a[124:127], v143 offset:5120\n"
    "v_mfma_f32_16x16x32_bf16 v[8:11], a[136:139], v[96:99], v[8:11]\n"
    "v_exp_f32_e64 v107, v107\n"
    "v_mfma_f32_16x16x32_bf16 v[12:15], a[140:143], v[96:99], v[12:15]\n"
    "s_add_u32 s68, s63, 0x18000\n"
    "v_cvt_pk_bf16_f32 v105, v106, v107\n"
    "v_mfma_f32_16x16x32_bf16 v[116:119], a[80:83], a[32:35], v[136:139]\n"
    "v_exp_f32_e64 v108, v108\n"
    "v_mfma_f32_16x16x32_bf16 v[116:119], a[84:87], a[36:39], v[116:119]\n"
    "ds_read_b64_tr_b16 a[160:161], v148\n"
    "ds_read_b64_tr_b16 a[162:163], v148 offset:4096\n"
    "v_mfma_f32_16x16x32_bf16 v[116:119], a[88:91], a[40:43], v[116:119]\n"
    "v_exp_f32_e64 v109, v109\n"
    "v_mfma_f32_16x16x32_bf16 v[116:119], a[92:95], a[44:47], v[116:119]\n"
    "ds_read_b64_tr_b16 a[164:165], v149\n"
    "v_cvt_pk_bf16_f32 v106, v108, v109\n"
    "v_mfma_f32_16x16x32_bf16 v[16:19], a[144:147], v[96:99], v[16:19]\n"
    "v_exp_f32_e64 v110, v110\n"
    "v_mfma_f32_16x16x32_bf16 v[20:23], a[148:151], v[96:99], v[20:23]\n"
    "ds_read_b64_tr_b16 a[166:167], v149 offset:4096\n"
    "buffer_load_dwordx4 v152, s[40:43], s68 offen nt lds\n"
    "s_waitcnt lgkmcnt(4)\n"
    "v_mfma_f32_16x16x32_bf16 v[24:27], a[152:155], v[96:99], v[24:27]\n"
    "v_exp_f32_e32 v111, v111\n"
    "v_mfma_f32_16x16x32_bf16 v[28:31], a[156:159], v[96:99], v[28:31]\n"
    "ds_read_b64_tr_b16 a[168:169], v150\n"
    "v_cvt_pk_bf16_f32 v107, v110, v111\n"
    "v_mfma_f32_16x16x32_bf16 a[48:51], a[60:63], v[96:99], a[48:51]\n"
    "ds_read_b64_tr_b16 a[170:171], v150 offset:4096\n"
    "ds_read_b64_tr_b16 a[172:173], v151\n"
    "buffer_load_dwordx4 v153, s[40:43], s68 offen offset:1024 nt lds\n"
    "buffer_load_dwordx4 v152, s[40:43], s68 offen offset:2048 nt lds\n"
    "buffer_load_dwordx4 v153, s[40:43], s68 offen offset:3072 nt lds\n"
    "s_memrealtime s[78:79]\n"
    "s_add_u32 s99, s99, s62\n"
    "s_waitcnt lgkmcnt(0)\n"
    "s_sub_u32 s67, s78, s99\n"
    "s_cmp_lt_u32 s67, 0x80000000\n"
    "s_cbranch_scc1 L_rt4x\n"
    "L_rt4:\n"
    "s_sleep 1\n"
    "s_add_u32 s67, s67, 4\n"
    "s_cbranch_scc0 L_rt4\n"
    "L_rt4x:\n"
    "s_waitcnt vmcnt(8)\n"
    "s_barrier\n"
    "v_mfma_f32_16x16x32_bf16 v[120:123], a[96:99], a[0:3], v[128:131]\n"
    "v_exp_f32_e32 v112, v112\n"
    "v_mfma_f32_16x16x32_bf16 v[120:123], a[100:103], a[4:7], v[120:123]\n"
    "ds_read_b128 a[64:67], v140 offset:16384\n"
    "s_add_u32 m0, s63, 0xc000\n"
    "v_mfma_f32_16x16x32_bf16 v[120:123], a[104:107], a[8:11], v[120:123]\n"
    "v_exp_f32_e64 v113, v113\n"
    "v_mfma_f32_16x16x32_bf16 v[120:123], a[108:111], a[12:15], v[120:123]\n"
    "ds_read_b128 a[80:83], v140 offset:20480\n"
    "v_cvt_pk_bf16_f32 v112, v112, v113\n"
    "v_mfma_f32_16x16x32_bf16 v[32:35], a[128:131], v[104:107], v[32:35]\n"
    "v_exp_f32_e64 v114, v114\n"
    "v_mfma_f32_16x16x32_bf16 v[36:39], a[132:135], v[104:107], v[36:39]\n"
    "ds_read_b128 a[68:71], v141 offset:16384\n"
    "v_mfma_f32_16x16x32_bf16 v[40:43], a[136:139], v[104:107], v[40:43]\n"
    "v_exp_f32_e64 v115, v115\n"
    "v_mfma_f32_16x16x32_bf16 v[44:47], a[140:143], v[104:107], v[44:47]\n"
    "s_add_u32 s68, s63, 0x1c000\n"
    "v_cvt_pk_bf16_f32 v113, v114, v115\n"
    "v_mfma_f32_16x16x32_bf16 v[124:127], a[112:115], a[0:3], v[128:131]\n"
    "v_exp_f32_e64 v116, v116\n"
    "v_mfma_f32_16x16x32_bf16 v[124:127], a[116:119], a[4:7], v[124:127]\n"
    "ds_read_b128 a[84:87], v141 offset:20480\n"
    "ds_read_b64_tr_b16 a[174:175], v151 offset:4096\n"
    "v_mfma_f32_16x16x32_bf16 v[124:127], a[120:123], a[8:11], v[124:127]\n"
    "v_exp_f32_e64 v117, v117\n"
    "v_mfma_f32_16x16x32_bf16 v[124:127], a[124:127], a[12:15], v[124:127]\n"
    "ds_read_b64_tr_b16 a[176:177], v148 offset:1024\n"
    "v_cvt_pk_bf16_f32 v114, v116, v117\n"
    "v_mfma_f32_16x16x32_bf16 v[48:51], a[144:147], v[104:107], v[48:51]\n"
    "v_exp_f32_e64 v118, v118\n"
    "v_mfma_f32_16x16x32_bf16 v[52:55], a[148:151], v[104:107], v[52:55]\n"
    "buffer_load_dwordx4 v152, s[36:39], s68 offen nt lds\n"
    "v_mfma_f32_16x16x32_bf16 v[56:59], a[152:155], v[104:107], v[56:59]\n"
    "v_exp_f32_e64 v119, v119\n"
    "v_mfma_f32_16x16x32_bf16 v[60:63], a[156:159], v[104:107], v[60:63]\n"
    "ds_read_b64_tr_b16 a[178:179], v148 offset:5120\n"
    "v_cvt_pk_bf16_f32 v115, v118, v119\n"
    "v_mfma_f32_16x16x32_bf16 a[52:55], a[60:63], v[104:107], a[52:55]\n"
    "ds_read_b64_tr_b16 a[180:181], v149 offset:1024\n"
    "v_mfma_f32_16x16x32_bf16 v[96:99], a[96:99], a[16:19], v[132:135]\n"
    "v_exp_f32_e64 v120, v120\n"
    "v_mfma_f32_16x16x32_bf16 v[96:99], a[100:103], a[20:23], v[96:99]\n"
    "ds_read_b128 a[72:75], v140 offset:17408\n"
    "buffer_load_dwordx4 v153, s[36:39], s68 offen offset:1024 nt lds\n"
    "v_mfma_f32_16x16x32_bf16 v[96:99], a[104:107], a[24:27], v[96:99]\n"
    "v_exp_f32_e64 v121, v121\n"
    "v_mfma_f32_16x16x32_bf16 v[96:99], a[108:111], a[28:31], v[96:99]\n"
    "ds_read_b128 a[88:91], v140 offset:21504\n"
    "v_cvt_pk_bf16_f32 v120, v120, v121\n"
    "v_mfma_f32_16x16x32_bf16 v[64:67], a[128:131], v[112:115], v[64:67]\n"
    "v_exp_f32_e64 v122, v122\n"
    "v_mfma_f32_16x16x32_bf16 v[68:71], a[132:135], v[112:115], v[68:71]\n"
    "ds_read_b128 a[76:79], v141 offset:17408\n"
    "v_mfma_f32_16x16x32_bf16 v[72:75], a[136:139], v[112:115], v[72:75]\n"
    "v_exp_f32_e64 v123, v123\n"
    "v_mfma_f32_16x16x32_bf16 v[76:79], a[140:143], v[112:115], v[76:79]\n"
    "ds_read_b128 a[92:95], v141 offset:21504\n"
    "v_cvt_pk_bf16_f32 v121, v122, v123\n"
    "v_mfma_f32_16x16x32_bf16 v[100:103], a[112:115], a[16:19], v[132:135]\n"
    "v_exp_f32_e64 v124, v124\n"
    "v_mfma_f32_16x16x32_bf16 v[100:103], a[116:119], a[20:23], v[100:103]\n"
    "ds_read_b64_tr_b16 a[182:183], v149 offset:5120\n"
    "v_mfma_f32_16x16x32_bf16 v[100:103], a[120:123], a[24:27], v[100:103]\n"
    "v_exp_f32_e64 v125, v125\n"
    "v_mfma_f32_16x16x32_bf16 v[100:103], a[124:127], a[28:31], v[100:103]\n"
    "ds_read_b64_tr_b16 a[184:185], v150 offset:1024\n"
    "v_cvt_pk_bf16_f32 v122, v124, v125\n"
    "v_mfma_f32_16x16x32_bf16 v[80:83], a[144:147], v[112:115], v[80:83]\n"
    "v_exp_f32_e64 v126, v126\n"
    "v_mfma_f32_16x16x32_bf16 v[84:87], a[148:151], v[112:115], v[84:87]\n"
    "ds_read_b64_tr_b16 a[186:187], v150 offset:5120\n"
    "v_mfma_f32_16x16x32_bf16 v[88:91], a[152:155], v[112:115], v[88:91]\n"
    "v_exp_f32_e64 v127, v127\n"
    "v_mfma_f32_16x16x32_bf16 v[92:95], a[156:159], v[112:115], v[92:95]\n"
    "s_waitcnt lgkmcnt(3)\n"
    "s_nop 0\n"
    "ds_read_b64_tr_b16 a[188:189], v151 offset:1024\n"
    "v_cvt_pk_bf16_f32 v123, v126, v127\n"
    "v_mfma_f32_16x16x32_bf16 a[56:59], a[60:63], v[112:115], a[56:59]\n"
    "ds_read_b64_tr_b16 a[190:191], v151 offset:5120\n"
    "v_mfma_f32_16x16x32_bf16 v[104:107], a[64:67], a[0:3], v[128:131]\n"
    "v_exp_f32_e64 v96, v96\n"
    "v_mfma_f32_16x16x32_bf16 v[104:107], a[68:71], a[4:7], v[104:107]\n"
    "ds_read_b64_tr_b16 a[128:129], v144 offset:16384\n"
    "buffer_load_dwordx4 v152, s[36:39], s68 offen offset:2048 nt lds\n"
    "v_mfma_f32_16x16x32_bf16 v[104:107], a[72:75], a[8:11], v[104:107]\n"
    "v_exp_f32_e64 v97, v97\n"
    "v_mfma_f32_16x16x32_bf16 v[104:107], a[76:79], a[12:15], v[104:107]\n"
    "ds_read_b64_tr_b16 a[130:131], v144 offset:20480\n"
    "v_cvt_pk_bf16_f32 v96, v96, v97\n"
    "v_mfma_f32_16x16x32_bf16 v[0:3], a[160:163], v[120:123], v[0:3]\n"
    "v_exp_f32_e64 v98, v98\n"
    "v_mfma_f32_16x16x32_bf16 v[4:7], a[164:167], v[120:123], v[4:7]\n"
    "ds_read_b64_tr_b16 a[132:133], v145 offset:16384\n"
    "v_mfma_f32_16x16x32_bf16 v[8:11], a[168:171], v[120:123], v[8:11]\n"
    "v_exp_f32_e64 v99, v99\n"
    "v_mfma_f32_16x16x32_bf16 v[12:15], a[172:175], v[120:123], v[12:15]\n"
    "ds_read_b64_tr_b16 a[134:135], v145 offset:20480\n"
    "v_cvt_pk_bf16_f32 v97, v98, v99\n"
    "v_mfma_f32_16x16x32_bf16 v[108:111], a[80:83], a[0:3], v[128:131]\n"
    "v_exp_f32_e64 v100, v100\n"
    "v_mfma_f32_16x16x32_bf16 v[108:111], a[84:87], a[4:7], v[108:111]\n"
    "ds_read_b64_tr_b16 a[136:137], v146 offset:16384\n"
    "ds_read_b64_tr_b16 a[138:139], v146 offset:20480\n"
    "v_mfma_f32_16x16x32_bf16 v[108:111], a[88:91], a[8:11], v[108:111]\n"
    "v_exp_f32_e64 v101, v101\n"
    "v_mfma_f32_16x16x32_bf16 v[108:111], a[92:95], a[12:15], v[108:111]\n"
    "ds_read_b64_tr_b16 a[140:141], v147 offset:16384\n"
    "v_cvt_pk_bf16_f32 v98, v100, v101\n"
    "v_mfma_f32_16x16x32_bf16 v[16:19], a[176:179], v[120:123], v[16:19]\n"
    "v_exp_f32_e32 v102, v102\n"
    "s_waitcnt lgkmcnt(4)\n"
    "v_mfma_f32_16x16x32_bf16 v[20:23], a[180:183], v[120:123], v[20:23]\n"
    "ds_read_b64_tr_b16 a[142:143], v147 offset:20480\n"
    "v_mfma_f32_16x16x32_bf16 v[24:27], a[184:187], v[120:123], v[24:27]\n"
    "v_exp_f32_e64 v103, v103\n"
    "v_mfma_f32_16x16x32_bf16 v[28:31], a[188:191], v[120:123], v[28:31]\n"
    "ds_read_b128 a[96:99], v142 offset:16384\n"
    "v_cvt_pk_bf16_f32 v99, v102, v103\n"
    "v_mfma_f32_16x16x32_bf16 a[48:51], a[60:63], v[120:123], a[48:51]\n"
    "ds_read_b128 a[112:115], v142 offset:20480\n"
    "v_mfma_f32_16x16x32_bf16 v[112:115], a[64:67], a[16:19], v[132:135]\n"
    "v_exp_f32_e64 v104, v104\n"
    "v_mfma_f32_16x16x32_bf16 v[112:115], a[68:71], a[20:23], v[112:115]\n"
    "ds_read_b64_tr_b16 a[144:145], v144 offset:17408\n"
    "buffer_load_dwordx4 v153, s[36:39], s68 offen offset:3072 nt lds\n"
    "v_mfma_f32_16x16x32_bf16 v[112:115], a[72:75], a[24:27], v[112:115]\n"
    "v_exp_f32_e64 v105, v105\n"
    "v_mfma_f32_16x16x32_bf16 v[112:115], a[76:79], a[28:31], v[112:115]\n"
    "ds_read_b64_tr_b16 a[146:147], v144 offset:21504\n"
    "v_cvt_pk_bf16_f32 v104, v104, v105\n"
    "v_mfma_f32_16x16x32_bf16 v[32:35], a[160:163], v[96:99], v[32:35]\n"
    "v_exp_f32_e64 v106, v106\n"
    "v_mfma_f32_16x16x32_bf16 v[36:39], a[164:167], v[96:99], v[36:39]\n"
    "ds_read_b64_tr_b16 a[148:149], v145 offset:17408\n"
    "v_mfma_f32_16x16x32_bf16 v[40:43], a[168:171], v[96:99], v[40:43]\n"
    "v_exp_f32_e64 v107, v107\n"
    "v_mfma_f32_16x16x32_bf16 v[44:47], a[172:175], v[96:99], v[44:47]\n"
    "ds_read_b64_tr_b16 a[150:151], v145 offset:21504\n"
    "v_cvt_pk_bf16_f32 v105, v106, v107\n"
    "v_mfma_f32_16x16x32_bf16 v[116:119], a[80:83], a[16:19], v[132:135]\n"
    "v_exp_f32_e64 v108, v108\n"
    "v_mfma_f32_16x16x32_bf16 v[116:119], a[84:87], a[20:23], v[116:119]\n"
    "ds_read_b64_tr_b16 a[152:153], v146 offset:17408\n"
    "ds_read_b64_tr_b16 a[154:155], v146 offset:21504\n"
    "v_mfma_f32_16x16x32_bf16 v[116:119], a[88:91], a[24:27], v[116:119]\n"
    "v_exp_f32_e64 v109, v109\n"
    "v_mfma_f32_16x16x32_bf16 v[116:119], a[92:95], a[28:31], v[116:119]\n"
    "ds_read_b64_tr_b16 a[156:157], v147 offset:17408\n"
    "v_cvt_pk_bf16_f32 v106, v108, v109\n"
    "v_mfma_f32_16x16x32_bf16 v[48:51], a[176:179], v[96:99], v[48:51]\n"
    "v_exp_f32_e64 v110, v110\n"
    "v_mfma_f32_16x16x32_bf16 v[52:55], a[180:183], v[96:99], v[52:55]\n"
    "ds_read_b64_tr_b16 a[158:159], v147 offset:21504\n"
    "v_mfma_f32_16x16x32_bf16 v[56:59], a[184:187], v[96:99], v[56:59]\n"
    "v_exp_f32_e64 v111, v111\n"
    "v_mfma_f32_16x16x32_bf16 v[60:63], a[188:191], v[96:99], v[60:63]\n"
    "s_waitcnt lgkmcnt(4)\n"
    "s_nop 0\n"
    "ds_read_b128 a[100:103], v143 offset:16384\n"
    "v_cvt_pk_bf16_f32 v107, v110, v111\n"
    "v_mfma_f32_16x16x32_bf16 a[52:55], a[60:63], v[96:99], a[52:55]\n"
    "ds_read_b128 a[116:119], v143 offset:20480\n"
    "v_mfma_f32_16x16x32_bf16 v[120:123], a[64:67], a[32:35], v[136:139]\n"
    "v_exp_f32_e64 v112, v112\n"
    "v_mfma_f32_16x16x32_bf16 v[120:123], a[68:71], a[36:39], v[120:123]\n"
    "ds_read_b128 a[104:107], v142 offset:17408\n"
    "s_add_u32 m0, s63, 0x1c000\n"
    "v_mfma_f32_16x16x32_bf16 v[120:123], a[72:75], a[40:43], v[120:123]\n"
    "v_exp_f32_e64 v113, v113\n"
    "v_mfma_f32_16x16x32_bf16 v[120:123], a[76:79], a[44:47], v[120:123]\n"
    "ds_read_b128 a[120:123], v142 offset:21504\n"
    "v_cvt_pk_bf16_f32 v112, v112, v113\n"
    "v_mfma_f32_16x16x32_bf16 v[0:3], a[128:131], v[104:107], v[0:3]\n"
    "v_exp_f32_e64 v114, v114\n"
    "v_mfma_f32_16x16x32_bf16 v[4:7], a[132:135], v[104:107], v[4:7]\n"
    "ds_read_b128 a[108:111], v143 offset:17408\n"
    "ds_read_b128 a[124:127], v143 offset:21504\n"
    "v_mfma_f32_16x16x32_bf16 v[8:11], a[136:139], v[104:107], v[8:11]\n"
    "v_exp_f32_e64 v115, v115\n"
    "v_mfma_f32_16x16x32_bf16 v[12:15], a[140:143], v[104:107], v[12:15]\n"
    "s_add_u32 s68, s63, 0x1c000\n"
    "v_cvt_pk_bf16_f32 v113, v114, v115\n"
    "v_mfma_f32_16x16x32_bf16 v[124:127], a[80:83], a[32:35], v[136:139]\n"
    "v_exp_f32_e64 v116, v116\n"
    "v_mfma_f32_16x16x32_bf16 v[124:127], a[84:87], a[36:39], v[124:127]\n"
    "ds_read_b64_tr_b16 a[160:161], v148 offset:16384\n"
    "ds_read_b64_tr_b16 a[162:163], v148 offset:20480\n"
    "v_mfma_f32_16x16x32_bf16 v[124:127], a[88:91], a[40:43], v[124:127]\n"
    "v_exp_f32_e64 v117, v117\n"
    "v_mfma_f32_16x16x32_bf16 v[124:127], a[92:95], a[44:47], v[124:127]\n"
    "ds_read_b64_tr_b16 a[164:165], v149 offset:16384\n"
    "v_cvt_pk_bf16_f32 v114, v116, v117\n"
    "v_mfma_f32_16x16x32_bf16 v[16:19], a[144:147], v[104:107], v[16:19]\n"
    "v_exp_f32_e64 v118, v118\n"
    "v_mfma_f32_16x16x32_bf16 v[20:23], a[148:151], v[104:107], v[20:23]\n"
    "ds_read_b64_tr_b16 a[166:167], v149 offset:20480\n"
    "buffer_load_dwordx4 v152, s[40:43], s68 offen nt lds\n"
    "s_waitcnt lgkmcnt(4)\n"
    "v_mfma_f32_16x16x32_bf16 v[24:27], a[152:155], v[104:107], v[24:27]\n"
    "v_exp_f32_e32 v119, v119\n"
    "v_mfma_f32_16x16x32_bf16 v[28:31], a[156:159], v[104:107], v[28:31]\n"
    "ds_read_b64_tr_b16 a[168:169], v150 offset:16384\n"
    "v_cvt_pk_bf16_f32 v115, v118, v119\n"
    "v_mfma_f32_16x16x32_bf16 a[48:51], a[60:63], v[104:107], a[48:51]\n"
    "ds_read_b64_tr_b16 a[170:171], v150 offset:20480\n"
    "ds_read_b64_tr_b16 a[172:173], v151 offset:16384\n"
    "buffer_load_dwordx4 v153, s[40:43], s68 offen offset:1024 nt lds\n"
    "buffer_load_dwordx4 v152, s[40:43], s68 offen offset:2048 nt lds\n"
    "buffer_load_dwordx4 v153, s[40:43], s68 offen offset:3072 nt lds\n"
    "s_memrealtime s[78:79]\n"
    "s_add_u32 s99, s99, s62\n"
    "s_waitcnt lgkmcnt(0)\n"
    "s_sub_u32 s67, s78, s99\n"
    "s_cmp_lt_u32 s67, 0x80000000\n"
    "s_cbranch_scc1 L_rt5x\n"
    "L_rt5:\n"
    "s_sleep 1\n"
    "s_add_u32 s67, s67, 4\n"
    "s_cbranch_scc0 L_rt5\n"
    "L_rt5x:\n"
    "s_waitcnt vmcnt(8)\n"
    "s_barrier\n"
    "v_mfma_f32_16x16x32_bf16 v[96:99], a[96:99], a[0:3], v[128:131]\n"
    "v_exp_f32_e32 v120, v120\n"
    "v_mfma_f32_16x16x32_bf16 v[96:99], a[100:103], a[4:7], v[96:99]\n"
    "ds_read_b128 a[64:67], v140 offset:32768\n"
    "s_add_u32 m0, s63, 0\n"
    "v_mfma_f32_16x16x32_bf16 v[96:99], a[104:107], a[8:11], v[96:99]\n"
    "v_exp_f32_e32 v121, v121\n"
    "v_mfma_f32_16x16x32_bf16 v[96:99], a[108:111], a[12:15], v[96:99]\n"
    "ds_read_b128 a[80:83], v140 offset:36864\n"
    "v_cvt_pk_bf16_f32 v120, v120, v121\n"
    "v_mfma_f32_16x16x32_bf16 v[32:35], a[128:131], v[112:115], v[32:35]\n"
    "v_exp_f32_e64 v122, v122\n"
    "v_mfma_f32_16x16x32_bf16 v[36:39], a[132:135], v[112:115], v[36:39]\n"
    "ds_read_b128 a[68:71], v141 offset:32768\n"
    "v_mfma_f32_16x16x32_bf16 v[40:43], a[136:139], v[112:115], v[40:43]\n"
    "v_exp_f32_e64 v123, v123\n"
    "v_mfma_f32_16x16x32_bf16 v[44:47], a[140:143], v[112:115], v[44:47]\n"
    "s_add_u32 s68, s63, 0x20000\n"
    "v_cvt_pk_bf16_f32 v121, v122, v123\n"
    "v_mfma_f32_16x16x32_bf16 v[100:103], a[112:115], a[0:3], v[128:131]\n"
    "v_exp_f32_e64 v124, v124\n"
    "v_mfma_f32_16x16x32_bf16 v[100:103], a[116:119], a[4:7], v[100:103]\n"
    "ds_read_b128 a[84:87], v141 offset:36864\n"
    "ds_read_b64_tr_b16 a[174:175], v151 offset:20480\n"
    "v_mfma_f32_16x16x32_bf16 v[100:103], a[120:123], a[8:11], v[100:103]\n"
    "v_exp_f32_e64 v125, v125\n"
    "v_mfma_f32_16x16x32_bf16 v[100:103], a[124:127], a[12:15], v[100:103]\n"
    "ds_read_b64_tr_b16 a[176:177], v148 offset:17408\n"
    "v_cvt_pk_bf16_f32 v122, v124, v125\n"
    "v_mfma_f32_16x16x32_bf16 v[48:51], a[144:147], v[112:115], v[48:51]\n"
    "v_exp_f32_e64 v126, v126\n"
    "v_mfma_f32_16x16x32_bf16 v[52:55], a[148:151], v[112:115], v[52:55]\n"
    "buffer_load_dwordx4 v152, s[36:39], s68 offen nt lds\n"
    "v_mfma_f32_16x16x32_bf16 v[56:59], a[152:155], v[112:115], v[56:59]\n"
    "v_exp_f32_e64 v127, v127\n"
    "v_mfma_f32_16x16x32_bf16 v[60:63], a[156:159], v[112:115], v[60:63]\n"
    "ds_read_b64_tr_b16 a[178:179], v148 offset:21504\n"
    "v_cvt_pk_bf16_f32 v123, v126, v127\n"
    "v_mfma_f32_16x16x32_bf16 a[52:55], a[60:63], v[112:115], a[52:55]\n"
    "ds_read_b64_tr_b16 a[180:181], v149 offset:17408\n"
    "v_mfma_f32_16x16x32_bf16 v[104:107], a[96:99], a[16:19], v[132:135]\n"
    "v_exp_f32_e64 v96, v96\n"
    "v_mfma_f32_16x16x32_bf16 v[104:107], a[100:103], a[20:23], v[104:107]\n"
    "ds_read_b128 a[72:75], v140 offset:33792\n"
    "buffer_load_dwordx4 v153, s[36:39], s68 offen offset:1024 nt lds\n"
    "v_mfma_f32_16x16x32_bf16 v[104:107], a[104:107], a[24:27], v[104:107]\n"
    "v_exp_f32_e64 v97, v97\n"
    "v_mfma_f32_16x16x32_bf16 v[104:107], a[108:111], a[28:31], v[104:107]\n"
    "ds_read_b128 a[88:91], v140 offset:37888\n"
    "v_cvt_pk_bf16_f32 v96, v96, v97\n"
    "v_mfma_f32_16x16x32_bf16 v[64:67], a[128:131], v[120:123], v[64:67]\n"
    "v_exp_f32_e64 v98, v98\n"
    "v_mfma_f32_16x16x32_bf16 v[68:71], a[132:135], v[120:123], v[68:71]\n"
    "ds_read_b128 a[76:79], v141 offset:33792\n"
    "v_mfma_f32_16x16x32_bf16 v[72:75], a[136:139], v[120:123], v[72:75]\n"
    "v_exp_f32_e64 v99, v99\n"
    "v_mfma_f32_16x16x32_bf16 v[76:79], a[140:143], v[120:123], v[76:79]\n"
    "ds_read_b128 a[92:95], v141 offset:37888\n"
    "v_cvt_pk_bf16_f32 v97, v98, v99\n"
    "v_mfma_f32_16x16x32_bf16 v[108:111], a[112:115], a[16:19], v[132:135]\n"
    "v_exp_f32_e64 v100, v100\n"
    "v_mfma_f32_16x16x32_bf16 v[108:111], a[116:119], a[20:23], v[108:111]\n"
    "ds_read_b64_tr_b16 a[182:183], v149 offset:21504\n"
    "v_mfma_f32_16x16x32_bf16 v[108:111], a[120:123], a[24:27], v[108:111]\n"
    "v_exp_f32_e64 v101, v101\n"
    "v_mfma_f32_16x16x32_bf16 v[108:111], a[124:127], a[28:31], v[108:111]\n"
    "ds_read_b64_tr_b16 a[184:185], v150 offset:17408\n"
    "v_cvt_pk_bf16_f32 v98, v100, v101\n"
    "v_mfma_f32_16x16x32_bf16 v[80:83], a[144:147], v[120:123], v[80:83]\n"
    "v_exp_f32_e64 v102, v102\n"
    "v_mfma_f32_16x16x32_bf16 v[84:87], a[148:151], v[120:123], v[84:87]\n"
    "ds_read_b64_tr_b16 a[186:187], v150 offset:21504\n"
    "v_mfma_f32_16x16x32_bf16 v[88:91], a[152:155], v[120:123], v[88:91]\n"
    "v_exp_f32_e64 v103, v103\n"
    "v_mfma_f32_16x16x32_bf16 v[92:95], a[156:159], v[120:123], v[92:95]\n"
    "s_waitcnt lgkmcnt(3)\n"
    "s_nop 0\n"
    "ds_read_b64_tr_b16 a[188:189], v151 offset:17408\n"
    "v_cvt_pk_bf16_f32 v99, v102, v103\n"
    "v_mfma_f32_16x16x32_bf16 a[56:59], a[60:63], v[120:123], a[56:59]\n"
    "ds_read_b64_tr_b16 a[190:191], v151 offset:21504\n"
    "v_mfma_f32_16x16x32_bf16 v[112:115], a[64:67], a[0:3], v[128:131]\n"
    "v_exp_f32_e64 v104, v104\n"
    "v_mfma_f32_16x16x32_bf16 v[112:115], a[68:71], a[4:7], v[112:115]\n"
    "ds_read_b64_tr_b16 a[128:129], v144 offset:32768\n"
    "buffer_load_dwordx4 v152, s[36:39], s68 offen offset:2048 nt lds\n"
    "v_mfma_f32_16x16x32_bf16 v[112:115], a[72:75], a[8:11], v[112:115]\n"
    "v_exp_f32_e64 v105, v105\n"
    "v_mfma_f32_16x16x32_bf16 v[112:115], a[76:79], a[12:15], v[112:115]\n"
    "ds_read_b64_tr_b16 a[130:131], v144 offset:36864\n"
    "v_cvt_pk_bf16_f32 v104, v104, v105\n"
    "v_mfma_f32_16x16x32_bf16 v[0:3], a[160:163], v[96:99], v[0:3]\n"
    "v_exp_f32_e64 v106, v106\n"
    "v_mfma_f32_16x16x32_bf16 v[4:7], a[164:167], v[96:99], v[4:7]\n"
    "ds_read_b64_tr_b16 a[132:133], v145 offset:32768\n"
    "v_mfma_f32_16x16x32_bf16 v[8:11], a[168:171], v[96:99], v[8:11]\n"
    "v_exp_f32_e64 v107, v107\n"
    "v_mfma_f32_16x16x32_bf16 v[12:15], a[172:175], v[96:99], v[12:15]\n"
    "ds_read_b64_tr_b16 a[134:135], v145 offset:36864\n"
    "v_cvt_pk_bf16_f32 v105, v106, v107\n"
    "v_mfma_f32_16x16x32_bf16 v[116:119], a[80:83], a[0:3], v[128:131]\n"
    "v_exp_f32_e64 v108, v108\n"
    "v_mfma_f32_16x16x32_bf16 v[116:119], a[84:87], a[4:7], v[116:119]\n"
    "ds_read_b64_tr_b16 a[136:137], v146 offset:32768\n"
    "ds_read_b64_tr_b16 a[138:139], v146 offset:36864\n"
    "v_mfma_f32_16x16x32_bf16 v[116:119], a[88:91], a[8:11], v[116:119]\n"
    "v_exp_f32_e64 v109, v109\n"
    "v_mfma_f32_16x16x32_bf16 v[116:119], a[92:95], a[12:15], v[116:119]\n"
    "ds_read_b64_tr_b16 a[140:141], v147 offset:32768\n"
    "v_cvt_pk_bf16_f32 v106, v108, v109\n"
    "v_mfma_f32_16x16x32_bf16 v[16:19], a[176:179], v[96:99], v[16:19]\n"
    "v_exp_f32_e32 v110, v110\n"
    "s_waitcnt lgkmcnt(4)\n"
    "v_mfma_f32_16x16x32_bf16 v[20:23], a[180:183], v[96:99], v[20:23]\n"
    "ds_read_b64_tr_b16 a[142:143], v147 offset:36864\n"
    "v_mfma_f32_16x16x32_bf16 v[24:27], a[184:187], v[96:99], v[24:27]\n"
    "v_exp_f32_e64 v111, v111\n"
    "v_mfma_f32_16x16x32_bf16 v[28:31], a[188:191], v[96:99], v[28:31]\n"
    "ds_read_b128 a[96:99], v142 offset:32768\n"
    "v_cvt_pk_bf16_f32 v107, v110, v111\n"
    "v_mfma_f32_16x16x32_bf16 a[48:51], a[60:63], v[96:99], a[48:51]\n"
    "ds_read_b128 a[112:115], v142 offset:36864\n"
    "v_mfma_f32_16x16x32_bf16 v[120:123], a[64:67], a[16:19], v[132:135]\n"
    "v_exp_f32_e64 v112, v112\n"
    "v_mfma_f32_16x16x32_bf16 v[120:123], a[68:71], a[20:23], v[120:123]\n"
    "ds_read_b64_tr_b16 a[144:145], v144 offset:33792\n"
    "buffer_load_dwordx4 v153, s[36:39], s68 offen offset:3072 nt lds\n"
    "v_mfma_f32_16x16x32_bf16 v[120:123], a[72:75], a[24:27], v[120:123]\n"
    "v_exp_f32_e64 v113, v113\n"
    "v_mfma_f32_16x16x32_bf16 v[120:123], a[76:79], a[28:31], v[120:123]\n"
    "ds_read_b64_tr_b16 a[146:147], v144 offset:37888\n"
    "v_cvt_pk_bf16_f32 v112, v112, v113\n"
    "v_mfma_f32_16x16x32_bf16 v[32:35], a[160:163], v[104:107], v[32:35]\n"
    "v_exp_f32_e64 v114, v114\n"
    "v_mfma_f32_16x16x32_bf16 v[36:39], a[164:167], v[104:107], v[36:39]\n"
    "ds_read_b64_tr_b16 a[148:149], v145 offset:33792\n"
    "v_mfma_f32_16x16x32_bf16 v[40:43], a[168:171], v[104:107], v[40:43]\n"
    "v_exp_f32_e64 v115, v115\n"
    "v_mfma_f32_16x16x32_bf16 v[44:47], a[172:175], v[104:107], v[44:47]\n"
    "ds_read_b64_tr_b16 a[150:151], v145 offset:37888\n"
    "v_cvt_pk_bf16_f32 v113, v114, v115\n"
    "v_mfma_f32_16x16x32_bf16 v[124:127], a[80:83], a[16:19], v[132:135]\n"
    "v_exp_f32_e64 v116, v116\n"
    "v_mfma_f32_16x16x32_bf16 v[124:127], a[84:87], a[20:23], v[124:127]\n"
    "ds_read_b64_tr_b16 a[152:153], v146 offset:33792\n"
    "ds_read_b64_tr_b16 a[154:155], v146 offset:37888\n"
    "v_mfma_f32_16x16x32_bf16 v[124:127], a[88:91], a[24:27], v[124:127]\n"
    "v_exp_f32_e64 v117, v117\n"
    "v_mfma_f32_16x16x32_bf16 v[124:127], a[92:95], a[28:31], v[124:127]\n"
    "ds_read_b64_tr_b16 a[156:157], v147 offset:33792\n"
    "v_cvt_pk_bf16_f32 v114, v116, v117\n"
    "v_mfma_f32_16x16x32_bf16 v[48:51], a[176:179], v[104:107], v[48:51]\n"
    "v_exp_f32_e64 v118, v118\n"
    "v_mfma_f32_16x16x32_bf16 v[52:55], a[180:183], v[104:107], v[52:55]\n"
    "ds_read_b64_tr_b16 a[158:159], v147 offset:37888\n"
    "v_mfma_f32_16x16x32_bf16 v[56:59], a[184:187], v[104:107], v[56:59]\n"
    "v_exp_f32_e64 v119, v119\n"
    "v_mfma_f32_16x16x32_bf16 v[60:63], a[188:191], v[104:107], v[60:63]\n"
    "s_waitcnt lgkmcnt(4)\n"
    "s_nop 0\n"
    "ds_read_b128 a[100:103], v143 offset:32768\n"
    "v_cvt_pk_bf16_f32 v115, v118, v119\n"
    "v_mfma_f32_16x16x32_bf16 a[52:55], a[60:63], v[104:107], a[52:55]\n"
    "ds_read_b128 a[116:119], v143 offset:36864\n"
    "v_mfma_f32_16x16x32_bf16 v[96:99], a[64:67], a[32:35], v[136:139]\n"
    "v_exp_f32_e64 v120, v120\n"
    "v_mfma_f32_16x16x32_bf16 v[96:99], a[68:71], a[36:39], v[96:99]\n"
    "ds_read_b128 a[104:107], v142 offset:33792\n"
    "s_add_u32 m0, s63, 0x10000\n"
    "v_mfma_f32_16x16x32_bf16 v[96:99], a[72:75], a[40:43], v[96:99]\n"
    "v_exp_f32_e64 v121, v121\n"
    "v_mfma_f32_16x16x32_bf16 v[96:99], a[76:79], a[44:47], v[96:99]\n"
    "ds_read_b128 a[120:123], v142 offset:37888\n"
    "v_cvt_pk_bf16_f32 v120, v120, v121\n"
    "v_mfma_f32_16x16x32_bf16 v[0:3], a[128:131], v[112:115], v[0:3]\n"
    "v_exp_f32_e64 v122, v122\n"
    "v_mfma_f32_16x16x32_bf16 v[4:7], a[132:135], v[112:115], v[4:7]\n"
    "ds_read_b128 a[108:111], v143 offset:33792\n"
    "ds_read_b128 a[124:127], v143 offset:37888\n"
    "v_mfma_f32_16x16x32_bf16 v[8:11], a[136:139], v[112:115], v[8:11]\n"
    "v_exp_f32_e64 v123, v123\n"
    "v_mfma_f32_16x16x32_bf16 v[12:15], a[140:143], v[112:115], v[12:15]\n"
    "s_add_u32 s68, s63, 0x20000\n"
    "v_cvt_pk_bf16_f32 v121, v122, v123\n"
    "v_mfma_f32_16x16x32_bf16 v[100:103], a[80:83], a[32:35], v[136:139]\n"
    "v_exp_f32_e64 v124, v124\n"
    "v_mfma_f32_16x16x32_bf16 v[100:103], a[84:87], a[36:39], v[100:103]\n"
    "ds_read_b64_tr_b16 a[160:161], v148 offset:32768\n"
    "ds_read_b64_tr_b16 a[162:163], v148 offset:36864\n"
    "v_mfma_f32_16x16x32_bf16 v[100:103], a[88:91], a[40:43], v[100:103]\n"
    "v_exp_f32_e64 v125, v125\n"
    "v_mfma_f32_16x16x32_bf16 v[100:103], a[92:95], a[44:47], v[100:103]\n"
    "ds_read_b64_tr_b16 a[164:165], v149 offset:32768\n"
    "v_cvt_pk_bf16_f32 v122, v124, v125\n"
    "v_mfma_f32_16x16x32_bf16 v[16:19], a[144:147], v[112:115], v[16:19]\n"
    "v_exp_f32_e64 v126, v126\n"
    "v_mfma_f32_16x16x32_bf16 v[20:23], a[148:151], v[112:115], v[20:23]\n"
    "ds_read_b64_tr_b16 a[166:167], v149 offset:36864\n"
    "buffer_load_dwordx4 v152, s[40:43], s68 offen nt lds\n"
    "s_waitcnt lgkmcnt(4)\n"
    "v_mfma_f32_16x16x32_bf16 v[24:27], a[152:155], v[112:115], v[24:27]\n"
    "v_exp_f32_e32 v127, v127\n"
    "v_mfma_f32_16x16x32_bf16 v[28:31], a[156:159], v[112:115], v[28:31]\n"
    "ds_read_b64_tr_b16 a[168:169], v150 offset:32768\n"
    "v_cvt_pk_bf16_f32 v123, v126, v127\n"
    "v_mfma_f32_16x16x32_bf16 a[48:51], a[60:63], v[112:115], a[48:51]\n"
    "ds_read_b64_tr_b16 a[170:171], v150 offset:36864\n"
    "ds_read_b64_tr_b16 a[172:173], v151 offset:32768\n"
    "buffer_load_dwordx4 v153, s[40:43], s68 offen offset:1024 nt lds\n"
    "buffer_load_dwordx4 v152, s[40:43], s68 offen offset:2048 nt lds\n"
    "buffer_load_dwordx4 v153, s[40:43], s68 offen offset:3072 nt lds\n"
    "s_memrealtime s[78:79]\n"
    "s_add_u32 s99, s99, s62\n"
    "s_waitcnt lgkmcnt(0)\n"
    "s_sub_u32 s67, s78, s99\n"
    "s_cmp_lt_u32 s67, 0x80000000\n"
    "s_cbranch_scc1 L_rt6x\n"
    "L_rt6:\n"
    "s_sleep 1\n"
    "s_add_u32 s67, s67, 4\n"
    "s_cbranch_scc0 L_rt6\n"
    "L_rt6x:\n"
    "s_waitcnt vmcnt(8)\n"
    "s_barrier\n"
    "v_mfma_f32_16x16x32_bf16 v[104:107], a[96:99], a[0:3], v[128:131]\n"
    "v_exp_f32_e32 v96, v96\n"
    "v_mfma_f32_16x16x32_bf16 v[104:107], a[100:103], a[4:7], v[104:107]\n"
    "ds_read_b128 a[64:67], v140 offset:49152\n"
    "s_add_u32 m0, s63, 0x4000\n"
    "v_mfma_f32_16x16x32_bf16 v[104:107], a[104:107], a[8:11], v[104:107]\n"
    "v_exp_f32_e64 v97, v97\n"
    "v_mfma_f32_16x16x32_bf16 v[104:107], a[108:111], a[12:15], v[104:107]\n"
    "ds_read_b128 a[80:83], v140 offset:53248\n"
    "v_cvt_pk_bf16_f32 v96, v96, v97\n"
    "v_mfma_f32_16x16x32_bf16 v[32:35], a[128:131], v[120:123], v[32:35]\n"
    "v_exp_f32_e64 v98, v98\n"
    "v_mfma_f32_16x16x32_bf16 v[36:39], a[132:135], v[120:123], v[36:39]\n"
    "ds_read_b128 a[68:71], v141 offset:49152\n"
    "v_mfma_f32_16x16x32_bf16 v[40:43], a[136:139], v[120:123], v[40:43]\n"
    "v_exp_f32_e64 v99, v99\n"
    "v_mfma_f32_16x16x32_bf16 v[44:47], a[140:143], v[120:123], v[44:47]\n"
    "s_add_u32 s68, s63, 0x24000\n"
    "v_cvt_pk_bf16_f32 v97, v98, v99\n"
    "v_mfma_f32_16x16x32_bf16 v[108:111], a[112:115], a[0:3], v[128:131]\n"
    "v_exp_f32_e64 v100, v100\n"
    "v_mfma_f32_16x16x32_bf16 v[108:111], a[116:119], a[4:7], v[108:111]\n"
    "ds_read_b128 a[84:87], v141 offset:53248\n"
    "ds_read_b64_tr_b16 a[174:175], v151 offset:36864\n"
    "v_mfma_f32_16x16x32_bf16 v[108:111], a[120:123], a[8:11], v[108:111]\n"
    "v_exp_f32_e64 v101, v101\n"
    "v_mfma_f32_16x16x32_bf16 v[108:111], a[124:127], a[12:15], v[108:111]\n"
    "ds_read_b64_tr_b16 a[176:177], v148 offset:33792\n"
    "v_cvt_pk_bf16_f32 v98, v100, v101\n"
    "v_mfma_f32_16x16x32_bf16 v[48:51], a[144:147], v[120:123], v[48:51]\n"
    "v_exp_f32_e64 v102, v102\n"
    "v_mfma_f32_16x16x32_bf16 v[52:55], a[148:151], v[120:123], v[52:55]\n"
    "buffer_load_dwordx4 v152, s[36:39], s68 offen nt lds\n"
    "v_mfma_f32_16x16x32_bf16 v[56:59], a[152:155], v[120:123], v[56:59]\n"
    "v_exp_f32_e64 v103, v103\n"
    "v_mfma_f32_16x16x32_bf16 v[60:63], a[156:159], v[120:123], v[60:63]\n"
    "ds_read_b64_tr_b16 a[178:179], v148 offset:37888\n"
    "v_cvt_pk_bf16_f32 v99, v102, v103\n"
    "v_mfma_f32_16x16x32_bf16 a[52:55], a[60:63], v[120:123], a[52:55]\n"
    "ds_read_b64_tr_b16 a[180:181], v149 offset:33792\n"
    "v_mfma_f32_16x16x32_bf16 v[112:115], a[96:99], a[16:19], v[132:135]\n"
    "v_exp_f32_e64 v104, v104\n"
    "v_mfma_f32_16x16x32_bf16 v[112:115], a[100:103], a[20:23], v[112:115]\n"
    "ds_read_b128 a[72:75], v140 offset:50176\n"
    "buffer_load_dwordx4 v153, s[36:39], s68 offen offset:1024 nt lds\n"
    "v_mfma_f32_16x16x32_bf16 v[112:115], a[104:107], a[24:27], v[112:115]\n"
    "v_exp_f32_e64 v105, v105\n"
    "v_mfma_f32_16x16x32_bf16 v[112:115], a[108:111], a[28:31], v[112:115]\n"
    "ds_read_b128 a[88:91], v140 offset:54272\n"
    "v_cvt_pk_bf16_f32 v104, v104, v105\n"
    "v_mfma_f32_16x16x32_bf16 v[64:67], a[128:131], v[96:99], v[64:67]\n"
    "v_exp_f32_e64 v106, v106\n"
    "v_mfma_f32_16x16x32_bf16 v[68:71], a[132:135], v[96:99], v[68:71]\n"
    "ds_read_b128 a[76:79], v141 offset:50176\n"
    "v_mfma_f32_16x16x32_bf16 v[72:75], a[136:139], v[96:99], v[72:75]\n"
    "v_exp_f32_e64 v107, v107\n"
    "v_mfma_f32_16x16x32_bf16 v[76:79], a[140:143], v[96:99], v[76:79]\n"
    "ds_read_b128 a[92:95], v141 offset:54272\n"
    "v_cvt_pk_bf16_f32 v105, v106, v107\n"
    "v_mfma_f32_16x16x32_bf16 v[116:119], a[112:115], a[16:19], v[132:135]\n"
    "v_exp_f32_e64 v108, v108\n"
    "v_mfma_f32_16x16x32_bf16 v[116:119], a[116:119], a[20:23], v[116:119]\n"
    "ds_read_b64_tr_b16 a[182:183], v149 offset:37888\n"
    "v_mfma_f32_16x16x32_bf16 v[116:119], a[120:123], a[24:27], v[116:119]\n"
    "v_exp_f32_e64 v109, v109\n"
    "v_mfma_f32_16x16x32_bf16 v[116:119], a[124:127], a[28:31], v[116:119]\n"
    "ds_read_b64_tr_b16 a[184:185], v150 offset:33792\n"
    "v_cvt_pk_bf16_f32 v106, v108, v109\n"
    "v_mfma_f32_16x16x32_bf16 v[80:83], a[144:147], v[96:99], v[80:83]\n"
    "v_exp_f32_e64 v110, v110\n"
    "v_mfma_f32_16x16x32_bf16 v[84:87], a[148:151], v[96:99], v[84:87]\n"
    "ds_read_b64_tr_b16 a[186:187], v150 offset:37888\n"
    "v_mfma_f32_16x16x32_bf16 v[88:91], a[152:155], v[96:99], v[88:91]\n"
    "v_exp_f32_e64 v111, v111\n"
    "v_mfma_f32_16x16x32_bf16 v[92:95], a[156:159], v[96:99], v[92:95]\n"
    "s_waitcnt lgkmcnt(3)\n"
    "s_nop 0\n"
    "ds_read_b64_tr_b16 a[188:189], v151 offset:33792\n"
    "v_cvt_pk_bf16_f32 v107, v110, v111\n"
    "v_mfma_f32_16x16x32_bf16 a[56:59], a[60:63], v[96:99], a[56:59]\n"
    "ds_read_b64_tr_b16 a[190:191], v151 offset:37888\n"
    "v_mfma_f32_16x16x32_bf16 v[120:123], a[64:67], a[0:3], v[128:131]\n"
    "v_exp_f32_e64 v112, v112\n"
    "v_mfma_f32_16x16x32_bf16 v[120:123], a[68:71], a[4:7], v[120:123]\n"
    "ds_read_b64_tr_b16 a[128:129], v144 offset:49152\n"
    "buffer_load_dwordx4 v152, s[36:39], s68 offen offset:2048 nt lds\n"
    "v_mfma_f32_16x16x32_bf16 v[120:123], a[72:75], a[8:11], v[120:123]\n"
    "v_exp_f32_e64 v113, v113\n"
    "v_mfma_f32_16x16x32_bf16 v[120:123], a[76:79], a[12:15], v[120:123]\n"
    "ds_read_b64_tr_b16 a[130:131], v144 offset:53248\n"
    "v_cvt_pk_bf16_f32 v112, v112, v113\n"
    "v_mfma_f32_16x16x32_bf16 v[0:3], a[160:163], v[104:107], v[0:3]\n"
    "v_exp_f32_e64 v114, v114\n"
    "v_mfma_f32_16x16x32_bf16 v[4:7], a[164:167], v[104:107], v[4:7]\n"
    "ds_read_b64_tr_b16 a[132:133], v145 offset:49152\n"
    "v_mfma_f32_16x16x32_bf16 v[8:11], a[168:171], v[104:107], v[8:11]\n"
    "v_exp_f32_e64 v115, v115\n"
    "v_mfma_f32_16x16x32_bf16 v[12:15], a[172:175], v[104:107], v[12:15]\n"
    "ds_read_b64_tr_b16 a[134:135], v145 offset:53248\n"
    "v_cvt_pk_bf16_f32 v113, v114, v115\n"
    "v_mfma_f32_16x16x32_bf16 v[124:127], a[80:83], a[0:3], v[128:131]\n"
    "v_exp_f32_e64 v116, v116\n"
    "v_mfma_f32_16x16x32_bf16 v[124:127], a[84:87], a[4:7], v[124:127]\n"
    "ds_read_b64_tr_b16 a[136:137], v146 offset:49152\n"
    "ds_read_b64_tr_b16 a[138:139], v146 offset:53248\n"
    "v_mfma_f32_16x16x32_bf16 v[124:127], a[88:91], a[8:11], v[124:127]\n"
    "v_exp_f32_e64 v117, v117\n"
    "v_mfma_f32_16x16x32_bf16 v[124:127], a[92:95], a[12:15], v[124:127]\n"
    "ds_read_b64_tr_b16 a[140:141], v147 offset:49152\n"
    "v_cvt_pk_bf16_f32 v114, v116, v117\n"
    "v_mfma_f32_16x16x32_bf16 v[16:19], a[176:179], v[104:107], v[16:19]\n"
    "v_exp_f32_e32 v118, v118\n"
    "s_waitcnt lgkmcnt(4)\n"
    "v_mfma_f32_16x16x32_bf16 v[20:23], a[180:183], v[104:107], v[20:23]\n"
    "ds_read_b64_tr_b16 a[142:143], v147 offset:53248\n"
    "v_mfma_f32_16x16x32_bf16 v[24:27], a[184:187], v[104:107], v[24:27]\n"
    "v_exp_f32_e64 v119, v119\n"
    "v_mfma_f32_16x16x32_bf16 v[28:31], a[188:191], v[104:107], v[28:31]\n"
    "ds_read_b128 a[96:99], v142 offset:49152\n"
    "v_cvt_pk_bf16_f32 v115, v118, v119\n"
    "v_mfma_f32_16x16x32_bf16 a[48:51], a[60:63], v[104:107], a[48:51]\n"
    "ds_read_b128 a[112:115], v142 offset:53248\n"
    "v_mfma_f32_16x16x32_bf16 v[96:99], a[64:67], a[16:19], v[132:135]\n"
    "v_exp_f32_e64 v120, v120\n"
    "v_mfma_f32_16x16x32_bf16 v[96:99], a[68:71], a[20:23], v[96:99]\n"
    "ds_read_b64_tr_b16 a[144:145], v144 offset:50176\n"
    "buffer_load_dwordx4 v153, s[36:39], s68 offen offset:3072 nt lds\n"
    "v_mfma_f32_16x16x32_bf16 v[96:99], a[72:75], a[24:27], v[96:99]\n"
    "v_exp_f32_e64 v121, v121\n"
    "v_mfma_f32_16x16x32_bf16 v[96:99], a[76:79], a[28:31], v[96:99]\n"
    "ds_read_b64_tr_b16 a[146:147], v144 offset:54272\n"
    "v_cvt_pk_bf16_f32 v120, v120, v121\n"
    "v_mfma_f32_16x16x32_bf16 v[32:35], a[160:163], v[112:115], v[32:35]\n"
    "v_exp_f32_e64 v122, v122\n"
    "v_mfma_f32_16x16x32_bf16 v[36:39], a[164:167], v[112:115], v[36:39]\n"
    "ds_read_b64_tr_b16 a[148:149], v145 offset:50176\n"
    "v_mfma_f32_16x16x32_bf16 v[40:43], a[168:171], v[112:115], v[40:43]\n"
    "v_exp_f32_e64 v123, v123\n"
    "v_mfma_f32_16x16x32_bf16 v[44:47], a[172:175], v[112:115], v[44:47]\n"
    "ds_read_b64_tr_b16 a[150:151], v145 offset:54272\n"
    "v_cvt_pk_bf16_f32 v121, v122, v123\n"
    "v_mfma_f32_16x16x32_bf16 v[100:103], a[80:83], a[16:19], v[132:135]\n"
    "v_exp_f32_e64 v124, v124\n"
    "v_mfma_f32_16x16x32_bf16 v[100:103], a[84:87], a[20:23], v[100:103]\n"
    "ds_read_b64_tr_b16 a[152:153], v146 offset:50176\n"
    "ds_read_b64_tr_b16 a[154:155], v146 offset:54272\n"
    "v_mfma_f32_16x16x32_bf16 v[100:103], a[88:91], a[24:27], v[100:103]\n"
    "v_exp_f32_e64 v125, v125\n"
    "v_mfma_f32_16x16x32_bf16 v[100:103], a[92:95], a[28:31], v[100:103]\n"
    "ds_read_b64_tr_b16 a[156:157], v147 offset:50176\n"
    "v_cvt_pk_bf16_f32 v122, v124, v125\n"
    "v_mfma_f32_16x16x32_bf16 v[48:51], a[176:179], v[112:115], v[48:51]\n"
    "v_exp_f32_e64 v126, v126\n"
    "v_mfma_f32_16x16x32_bf16 v[52:55], a[180:183], v[112:115], v[52:55]\n"
    "ds_read_b64_tr_b16 a[158:159], v147 offset:54272\n"
    "v_mfma_f32_16x16x32_bf16 v[56:59], a[184:187], v[112:115], v[56:59]\n"
    "v_exp_f32_e64 v127, v127\n"
    "v_mfma_f32_16x16x32_bf16 v[60:63], a[188:191], v[112:115], v[60:63]\n"
    "s_waitcnt lgkmcnt(4)\n"
    "s_nop 0\n"
    "ds_read_b128 a[100:103], v143 offset:49152\n"
    "v_cvt_pk_bf16_f32 v123, v126, v127\n"
    "v_mfma_f32_16x16x32_bf16 a[52:55], a[60:63], v[112:115], a[52:55]\n"
    "ds_read_b128 a[116:119], v143 offset:53248\n"
    "v_mfma_f32_16x16x32_bf16 v[104:107], a[64:67], a[32:35], v[136:139]\n"
    "v_exp_f32_e64 v96, v96\n"
    "v_mfma_f32_16x16x32_bf16 v[104:107], a[68:71], a[36:39], v[104:107]\n"
    "ds_read_b128 a[104:107], v142 offset:50176\n"
    "s_add_u32 m0, s63, 0x14000\n"
    "v_mfma_f32_16x16x32_bf16 v[104:107], a[72:75], a[40:43], v[104:107]\n"
    "v_exp_f32_e64 v97, v97\n"
    "v_mfma_f32_16x16x32_bf16 v[104:107], a[76:79], a[44:47], v[104:107]\n"
    "ds_read_b128 a[120:123], v142 offset:54272\n"
    "v_cvt_pk_bf16_f32 v96, v96, v97\n"
    "v_mfma_f32_16x16x32_bf16 v[0:3], a[128:131], v[120:123], v[0:3]\n"
    "v_exp_f32_e64 v98, v98\n"
    "v_mfma_f32_16x16x32_bf16 v[4:7], a[132:135], v[120:123], v[4:7]\n"
    "ds_read_b128 a[108:111], v143 offset:50176\n"
    "ds_read_b128 a[124:127], v143 offset:54272\n"
    "v_mfma_f32_16x16x32_bf16 v[8:11], a[136:139], v[120:123], v[8:11]\n"
    "v_exp_f32_e64 v99, v99\n"
    "v_mfma_f32_16x16x32_bf16 v[12:15], a[140:143], v[120:123], v[12:15]\n"
    "s_add_u32 s68, s63, 0x24000\n"
    "v_cvt_pk_bf16_f32 v97, v98, v99\n"
    "v_mfma_f32_16x16x32_bf16 v[108:111], a[80:83], a[32:35], v[136:139]\n"
    "v_exp_f32_e64 v100, v100\n"
    "v_mfma_f32_16x16x32_bf16 v[108:111], a[84:87], a[36:39], v[108:111]\n"
    "ds_read_b64_tr_b16 a[160:161], v148 offset:49152\n"
    "ds_read_b64_tr_b16 a[162:163], v148 offset:53248\n"
    "v_mfma_f32_16x16x32_bf16 v[108:111], a[88:91], a[40:43], v[108:111]\n"
    "v_exp_f32_e64 v101, v101\n"
    "v_mfma_f32_16x16x32_bf16 v[108:111], a[92:95], a[44:47], v[108:111]\n"
    "ds_read_b64_tr_b16 a[164:165], v149 offset:49152\n"
    "v_cvt_pk_bf16_f32 v98, v100, v101\n"
    "v_mfma_f32_16x16x32_bf16 v[16:19], a[144:147], v[120:123], v[16:19]\n"
    "v_exp_f32_e64 v102, v102\n"
    "v_mfma_f32_16x16x32_bf16 v[20:23], a[148:151], v[120:123], v[20:23]\n"
    "ds_read_b64_tr_b16 a[166:167], v149 offset:53248\n"
    "buffer_load_dwordx4 v152, s[40:43], s68 offen nt lds\n"
    "s_waitcnt lgkmcnt(4)\n"
    "v_mfma_f32_16x16x32_bf16 v[24:27], a[152:155], v[120:123], v[24:27]\n"
    "v_exp_f32_e32 v103, v103\n"
    "v_mfma_f32_16x16x32_bf16 v[28:31], a[156:159], v[120:123], v[28:31]\n"
    "ds_read_b64_tr_b16 a[168:169], v150 offset:49152\n"
    "v_cvt_pk_bf16_f32 v99, v102, v103\n"
    "v_mfma_f32_16x16x32_bf16 a[48:51], a[60:63], v[120:123], a[48:51]\n"
    "ds_read_b64_tr_b16 a[170:171], v150 offset:53248\n"
    "ds_read_b64_tr_b16 a[172:173], v151 offset:49152\n"
    "buffer_load_dwordx4 v153, s[40:43], s68 offen offset:1024 nt lds\n"
    "buffer_load_dwordx4 v152, s[40:43], s68 offen offset:2048 nt lds\n"
    "buffer_load_dwordx4 v153, s[40:43], s68 offen offset:3072 nt lds\n"
    "s_memrealtime s[78:79]\n"
    "s_add_u32 s99, s99, s62\n"
    "s_waitcnt lgkmcnt(0)\n"
    "s_sub_u32 s67, s78, s99\n"
    "s_cmp_lt_u32 s67, 0x80000000\n"
    "s_cbranch_scc1 L_rt7x\n"
    "L_rt7:\n"
    "s_sleep 1\n"
    "s_add_u32 s67, s67, 4\n"
    "s_cbranch_scc0 L_rt7\n"
    "L_rt7x:\n"
    "s_waitcnt vmcnt(8)\n"
    "s_barrier\n"
    "v_mfma_f32_16x16x32_bf16 v[112:115], a[96:99], a[0:3], v[128:131]\n"
    "v_exp_f32_e32 v104, v104\n"
    "v_mfma_f32_16x16x32_bf16 v[112:115], a[100:103], a[4:7], v[112:115]\n"
    "ds_read_b128 a[64:67], v140\n"
    "s_add_u32 m0, s63, 0x8000\n"
    "v_mfma_f32_16x16x32_bf16 v[112:115], a[104:107], a[8:11], v[112:115]\n"
    "v_exp_f32_e64 v105, v105\n"
    "v_mfma_f32_16x16x32_bf16 v[112:115], a[108:111], a[12:15], v[112:115]\n"
    "ds_read_b128 a[80:83], v140 offset:4096\n"
    "v_cvt_pk_bf16_f32 v104, v104, v105\n"
    "v_mfma_f32_16x16x32_bf16 v[32:35], a[128:131], v[96:99], v[32:35]\n"
    "v_exp_f32_e64 v106, v106\n"
    "v_mfma_f32_16x16x32_bf16 v[36:39], a[132:135], v[96:99], v[36:39]\n"
    "ds_read_b128 a[68:71], v141\n"
    "v_mfma_f32_16x16x32_bf16 v[40:43], a[136:139], v[96:99], v[40:43]\n"
    "v_exp_f32_e64 v107, v107\n"
    "v_mfma_f32_16x16x32_bf16 v[44:47], a[140:143], v[96:99], v[44:47]\n"
    "s_add_u32 s68, s63, 0x28000\n"
    "v_cvt_pk_bf16_f32 v105, v106, v107\n"
    "v_mfma_f32_16x16x32_bf16 v[116:119], a[112:115], a[0:3], v[128:131]\n"
    "v_exp_f32_e64 v108, v108\n"
    "v_mfma_f32_16x16x32_bf16 v[116:119], a[116:119], a[4:7], v[116:119]\n"
    "ds_read_b128 a[84:87], v141 offset:4096\n"
    "ds_read_b64_tr_b16 a[174:175], v151 offset:53248\n"
    "v_mfma_f32_16x16x32_bf16 v[116:119], a[120:123], a[8:11], v[116:119]\n"
    "v_exp_f32_e64 v109, v109\n"
    "v_mfma_f32_16x16x32_bf16 v[116:119], a[124:127], a[12:15], v[116:119]\n"
    "ds_read_b64_tr_b16 a[176:177], v148 offset:50176\n"
    "v_cvt_pk_bf16_f32 v106, v108, v109\n"
    "v_mfma_f32_16x16x32_bf16 v[48:51], a[144:147], v[96:99], v[48:51]\n"
    "v_exp_f32_e64 v110, v110\n"
    "v_mfma_f32_16x16x32_bf16 v[52:55], a[148:151], v[96:99], v[52:55]\n"
    "buffer_load_dwordx4 v152, s[36:39], s68 offen nt lds\n"
    "v_mfma_f32_16x16x32_bf16 v[56:59], a[152:155], v[96:99], v[56:59]\n"
    "v_exp_f32_e64 v111, v111\n"
    "v_mfma_f32_16x16x32_bf16 v[60:63], a[156:159], v[96:99], v[60:63]\n"
    "ds_read_b64_tr_b16 a[178:179], v148 offset:54272\n"
    "v_cvt_pk_bf16_f32 v107, v110, v111\n"
    "v_mfma_f32_16x16x32_bf16 a[52:55], a[60:63], v[96:99], a[52:55]\n"
    "ds_read_b64_tr_b16 a[180:181], v149 offset:50176\n"
    "v_mfma_f32_16x16x32_bf16 v[120:123], a[96:99], a[16:19], v[132:135]\n"
    "v_exp_f32_e64 v112, v112\n"
    "v_mfma_f32_16x16x32_bf16 v[120:123], a[100:103], a[20:23], v[120:123]\n"
    "ds_read_b128 a[72:75], v140 offset:1024\n"
    "buffer_load_dwordx4 v153, s[36:39], s68 offen offset:1024 nt lds\n"
    "v_mfma_f32_16x16x32_bf16 v[120:123], a[104:107], a[24:27], v[120:123]\n"
    "v_exp_f32_e64 v113, v113\n"
    "v_mfma_f32_16x16x32_bf16 v[120:123], a[108:111], a[28:31], v[120:123]\n"
    "ds_read_b128 a[88:91], v140 offset:5120\n"
    "v_cvt_pk_bf16_f32 v112, v112, v113\n"
    "v_mfma_f32_16x16x32_bf16 v[64:67], a[128:131], v[104:107], v[64:67]\n"
    "v_exp_f32_e64 v114, v114\n"
    "v_mfma_f32_16x16x32_bf16 v[68:71], a[132:135], v[104:107], v[68:71]\n"
    "ds_read_b128 a[76:79], v141 offset:1024\n"
    "v_mfma_f32_16x16x32_bf16 v[72:75], a[136:139], v[104:107], v[72:75]\n"
    "v_exp_f32_e64 v115, v115\n"
    "v_mfma_f32_16x16x32_bf16 v[76:79], a[140:143], v[104:107], v[76:79]\n"
    "ds_read_b128 a[92:95], v141 offset:5120\n"
    "v_cvt_pk_bf16_f32 v113, v114, v115\n"
    "v_mfma_f32_16x16x32_bf16 v[124:127], a[112:115], a[16:19], v[132:135]\n"
    "v_exp_f32_e64 v116, v116\n"
    "v_mfma_f32_16x16x32_bf16 v[124:127], a[116:119], a[20:23], v[124:127]\n"
    "ds_read_b64_tr_b16 a[182:183], v149 offset:54272\n"
    "v_mfma_f32_16x16x32_bf16 v[124:127], a[120:123], a[24:27], v[124:127]\n"
    "v_exp_f32_e64 v117, v117\n"
    "v_mfma_f32_16x16x32_bf16 v[124:127], a[124:127], a[28:31], v[124:127]\n"
    "ds_read_b64_tr_b16 a[184:185], v150 offset:50176\n"
    "v_cvt_pk_bf16_f32 v114, v116, v117\n"
    "v_mfma_f32_16x16x32_bf16 v[80:83], a[144:147], v[104:107], v[80:83]\n"
    "v_exp_f32_e64 v118, v118\n"
    "v_mfma_f32_16x16x32_bf16 v[84:87], a[148:151], v[104:107], v[84:87]\n"
    "ds_read_b64_tr_b16 a[186:187], v150 offset:54272\n"
    "v_mfma_f32_16x16x32_bf16 v[88:91], a[152:155], v[104:107], v[88:91]\n"
    "v_exp_f32_e64 v119, v119\n"
    "v_mfma_f32_16x16x32_bf16 v[92:95], a[156:159], v[104:107], v[92:95]\n"
    "s_waitcnt lgkmcnt(3)\n"
    "s_nop 0\n"
    "ds_read_b64_tr_b16 a[188:189], v151 offset:50176\n"
    "v_cvt_pk_bf16_f32 v115, v118, v119\n"
    "v_mfma_f32_16x16x32_bf16 a[56:59], a[60:63], v[104:107], a[56:59]\n"
    "ds_read_b64_tr_b16 a[190:191], v151 offset:54272\n"
    "v_mfma_f32_16x16x32_bf16 v[96:99], a[64:67], a[0:3], v[128:131]\n"
    "v_exp_f32_e64 v120, v120\n"
    "v_mfma_f32_16x16x32_bf16 v[96:99], a[68:71], a[4:7], v[96:99]\n"
    "ds_read_b64_tr_b16 a[128:129], v144\n"
    "buffer_load_dwordx4 v152, s[36:39], s68 offen offset:2048 nt lds\n"
    "v_mfma_f32_16x16x32_bf16 v[96:99], a[72:75], a[8:11], v[96:99]\n"
    "v_exp_f32_e64 v121, v121\n"
    "v_mfma_f32_16x16x32_bf16 v[96:99], a[76:79], a[12:15], v[96:99]\n"
    "ds_read_b64_tr_b16 a[130:131], v144 offset:4096\n"
    "v_cvt_pk_bf16_f32 v120, v120, v121\n"
    "v_mfma_f32_16x16x32_bf16 v[0:3], a[160:163], v[112:115], v[0:3]\n"
    "v_exp_f32_e64 v122, v122\n"
    "v_mfma_f32_16x16x32_bf16 v[4:7], a[164:167], v[112:115], v[4:7]\n"
    "ds_read_b64_tr_b16 a[132:133], v145\n"
    "v_mfma_f32_16x16x32_bf16 v[8:11], a[168:171], v[112:115], v[8:11]\n"
    "v_exp_f32_e64 v123, v123\n"
    "v_mfma_f32_16x16x32_bf16 v[12:15], a[172:175], v[112:115], v[12:15]\n"
    "ds_read_b64_tr_b16 a[134:135], v145 offset:4096\n"
    "v_cvt_pk_bf16_f32 v121, v122, v123\n"
    "v_mfma_f32_16x16x32_bf16 v[100:103], a[80:83], a[0:3], v[128:131]\n"
    "v_exp_f32_e64 v124, v124\n"
    "v_mfma_f32_16x16x32_bf16 v[100:103], a[84:87], a[4:7], v[100:103]\n"
    "ds_read_b64_tr_b16 a[136:137], v146\n"
    "ds_read_b64_tr_b16 a[138:139], v146 offset:4096\n"
    "v_mfma_f32_16x16x32_bf16 v[100:103], a[88:91], a[8:11], v[100:103]\n"
    "v_exp_f32_e64 v125, v125\n"
    "v_mfma_f32_16x16x32_bf16 v[100:103], a[92:95], a[12:15], v[100:103]\n"
    "ds_read_b64_tr_b16 a[140:141], v147\n"
    "v_cvt_pk_bf16_f32 v122, v124, v125\n"
    "v_mfma_f32_16x16x32_bf16 v[16:19], a[176:179], v[112:115], v[16:19]\n"
    "v_exp_f32_e32 v126, v126\n"
    "s_waitcnt lgkmcnt(4)\n"
    "v_mfma_f32_16x16x32_bf16 v[20:23], a[180:183], v[112:115], v[20:23]\n"
    "ds_read_b64_tr_b16 a[142:143], v147 offset:4096\n"
    "v_mfma_f32_16x16x32_bf16 v[24:27], a[184:187], v[112:115], v[24:27]\n"
    "v_exp_f32_e64 v127, v127\n"
    "v_mfma_f32_16x16x32_bf16 v[28:31], a[188:191], v[112:115], v[28:31]\n"
    "ds_read_b128 a[96:99], v142\n"
    "v_cvt_pk_bf16_f32 v123, v126, v127\n"
    "v_mfma_f32_16x16x32_bf16 a[48:51], a[60:63], v[112:115], a[48:51]\n"
    "ds_read_b128 a[112:115], v142 offset:4096\n"
    "v_mfma_f32_16x16x32_bf16 v[104:107], a[64:67], a[16:19], v[132:135]\n"
    "v_exp_f32_e64 v96, v96\n"
    "v_mfma_f32_16x16x32_bf16 v[104:107], a[68:71], a[20:23], v[104:107]\n"
    "ds_read_b64_tr_b16 a[144:145], v144 offset:1024\n"
    "buffer_load_dwordx4 v153, s[36:39], s68 offen offset:3072 nt lds\n"
    "v_mfma_f32_16x16x32_bf16 v[104:107], a[72:75], a[24:27], v[104:107]\n"
    "v_exp_f32_e64 v97, v97\n"
    "v_mfma_f32_16x16x32_bf16 v[104:107], a[76:79], a[28:31], v[104:107]\n"
    "ds_read_b64_tr_b16 a[146:147], v144 offset:5120\n"
    "v_cvt_pk_bf16_f32 v96, v96, v97\n"
    "v_mfma_f32_16x16x32_bf16 v[32:35], a[160:163], v[120:123], v[32:35]\n"
    "v_exp_f32_e64 v98, v98\n"
    "v_mfma_f32_16x16x32_bf16 v[36:39], a[164:167], v[120:123], v[36:39]\n"
    "ds_read_b64_tr_b16 a[148:149], v145 offset:1024\n"
    "v_mfma_f32_16x16x32_bf16 v[40:43], a[168:171], v[120:123], v[40:43]\n"
    "v_exp_f32_e64 v99, v99\n"
    "v_mfma_f32_16x16x32_bf16 v[44:47], a[172:175], v[120:123], v[44:47]\n"
    "ds_read_b64_tr_b16 a[150:151], v145 offset:5120\n"
    "v_cvt_pk_bf16_f32 v97, v98, v99\n"
    "v_mfma_f32_16x16x32_bf16 v[108:111], a[80:83], a[16:19], v[132:135]\n"
    "v_exp_f32_e64 v100, v100\n"
    "v_mfma_f32_16x16x32_bf16 v[108:111], a[84:87], a[20:23], v[108:111]\n"
    "ds_read_b64_tr_b16 a[152:153], v146 offset:1024\n"
    "ds_read_b64_tr_b16 a[154:155], v146 offset:5120\n"
    "v_mfma_f32_16x16x32_bf16 v[108:111], a[88:91], a[24:27], v[108:111]\n"
    "v_exp_f32_e64 v101, v101\n"
    "v_mfma_f32_16x16x32_bf16 v[108:111], a[92:95], a[28:31], v[108:111]\n"
    "ds_read_b64_tr_b16 a[156:157], v147 offset:1024\n"
    "v_cvt_pk_bf16_f32 v98, v100, v101\n"
    "v_mfma_f32_16x16x32_bf16 v[48:51], a[176:179], v[120:123], v[48:51]\n"
    "v_exp_f32_e64 v102, v102\n"
    "v_mfma_f32_16x16x32_bf16 v[52:55], a[180:183], v[120:123], v[52:55]\n"
    "ds_read_b64_tr_b16 a[158:159], v147 offset:5120\n"
    "v_mfma_f32_16x16x32_bf16 v[56:59], a[184:187], v[120:123], v[56:59]\n"
    "v_exp_f32_e64 v103, v103\n"
    "v_mfma_f32_16x16x32_bf16 v[60:63], a[188:191], v[120:123], v[60:63]\n"
    "s_waitcnt lgkmcnt(4)\n"
    "s_nop 0\n"
    "ds_read_b128 a[100:103], v143\n"
    "v_cvt_pk_bf16_f32 v99, v102, v103\n"
    "v_mfma_f32_16x16x32_bf16 a[52:55], a[60:63], v[120:123], a[52:55]\n"
    "ds_read_b128 a[116:119], v143 offset:4096\n"
    "s_mov_b64 s[48:49], %10\n"
    "s_mov_b32 s50, 0x48000\n"
    "s_mov_b32 s51, 0x20000\n"
    "v_lshrrev_b32_e32 v156, 3, v154\n"
    "v_and_b32_e32 v158, 0x70, v152\n"
    "s_mov_b32 s68, 0x480\n"
    "v_mad_u32_u24 v240, v156, s68, v158\n"
    "v_add_u32_e32 v241, 0x2000, v240\n"
    "v_add_u32_e32 v242, 0x4000, v240\n"
    "v_add_u32_e32 v243, 0x6000, v240\n"
    "v_and_b32_e32 v157, 0x3ff, v144\n"
    "v_lshrrev_b32_e64 v158, 5, v154\n"
    "v_lshl_add_u32 v157, v158, 10, v157\n"
    "s_lshl_b32 s68, s60, 5\n"
    "v_xor_b32_e32 v244, s68, v157\n"
    "v_add_u32_e32 v245, 0x10000, v244\n"
    "v_add_u32_e32 v246, 0x20000, v244\n"
    "v_add_u32_e32 v247, 0x10000, v155\n"
    "s_mul_i32 s77, s60, 0x12000\n"
    "s_add_u32 s76, s77, 0x480\n"
    "s_bfe_u32 s61, %7, 0x40008\n"
    "s_lshl_b32 s61, s61, 7\n"
    "s_add_u32 s61, s61, s77\n"
    "v_mfma_f32_16x16x32_bf16 v[112:115], a[64:67], a[32:35], v[136:139]\n"
    "v_exp_f32_e64 v104, v104\n"
    "v_mfma_f32_16x16x32_bf16 v[112:115], a[68:71], a[36:39], v[112:115]\n"
    "ds_read_b128 a[104:107], v142 offset:1024\n"
    "s_add_u32 m0, s63, 0x18000\n"
    "v_mfma_f32_16x16x32_bf16 v[112:115], a[72:75], a[40:43], v[112:115]\n"
    "v_exp_f32_e64 v105, v105\n"
    "v_mfma_f32_16x16x32_bf16 v[112:115], a[76:79], a[44:47], v[112:115]\n"
    "ds_read_b128 a[120:123], v142 offset:5120\n"
    "v_cvt_pk_bf16_f32 v104, v104, v105\n"
    "v_mfma_f32_16x16x32_bf16 v[0:3], a[128:131], v[96:99], v[0:3]\n"
    "v_exp_f32_e64 v106, v106\n"
    "v_mfma_f32_16x16x32_bf16 v[4:7], a[132:135], v[96:99], v[4:7]\n"
    "ds_read_b128 a[108:111], v143 offset:1024\n"
    "ds_read_b128 a[124:127], v143 offset:5120\n"
    "v_mfma_f32_16x16x32_bf16 v[8:11], a[136:139], v[96:99], v[8:11]\n"
    "v_exp_f32_e64 v107, v107\n"
    "v_mfma_f32_16x16x32_bf16 v[12:15], a[140:143], v[96:99], v[12:15]\n"
    "s_add_u32 s68, s63, 0x28000\n"
    "v_cvt_pk_bf16_f32 v105, v106, v107\n"
    "v_mfma_f32_16x16x32_bf16 v[116:119], a[80:83], a[32:35], v[136:139]\n"
    "v_exp_f32_e64 v108, v108\n"
    "v_mfma_f32_16x16x32_bf16 v[116:119], a[84:87], a[36:39], v[116:119]\n"
    "ds_read_b64_tr_b16 a[160:161], v148\n"
    "ds_read_b64_tr_b16 a[162:163], v148 offset:4096\n"
    "v_mfma_f32_16x16x32_bf16 v[116:119], a[88:91], a[40:43], v[116:119]\n"
    "v_exp_f32_e64 v109, v109\n"
    "v_mfma_f32_16x16x32_bf16 v[116:119], a[92:95], a[44:47], v[116:119]\n"
    "ds_read_b64_tr_b16 a[164:165], v149\n"
    "v_cvt_pk_bf16_f32 v106, v108, v109\n"
    "v_mfma_f32_16x16x32_bf16 v[16:19], a[144:147], v[96:99], v[16:19]\n"
    "v_exp_f32_e64 v110, v110\n"
    "v_mfma_f32_16x16x32_bf16 v[20:23], a[148:151], v[96:99], v[20:23]\n"
    "ds_read_b64_tr_b16 a[166:167], v149 offset:4096\n"
    "buffer_load_dwordx4 v152, s[40:43], s68 offen nt lds\n"
    "s_waitcnt lgkmcnt(4)\n"
    "v_mfma_f32_16x16x32_bf16 v[24:27], a[152:155], v[96:99], v[24:27]\n"
    "v_exp_f32_e32 v111, v111\n"
    "v_mfma_f32_16x16x32_bf16 v[28:31], a[156:159], v[96:99], v[28:31]\n"
    "ds_read_b64_tr_b16 a[168:169], v150\n"
    "v_cvt_pk_bf16_f32 v107, v110, v111\n"
    "v_mfma_f32_16x16x32_bf16 a[48:51], a[60:63], v[96:99], a[48:51]\n"
    "ds_read_b64_tr_b16 a[170:171], v150 offset:4096\n"
    "ds_read_b64_tr_b16 a[172:173], v151\n"
    "buffer_load_dwordx4 v153, s[40:43], s68 offen offset:1024 nt lds\n"
    "buffer_load_dwordx4 v152, s[40:43], s68 offen offset:2048 nt lds\n"
    "buffer_load_dwordx4 v153, s[40:43], s68 offen offset:3072 nt lds\n"
    "s_memrealtime s[78:79]\n"
    "s_add_u32 s99, s99, s62\n"
    "s_waitcnt lgkmcnt(0)\n"
    "s_sub_u32 s67, s78, s99\n"
    "s_cmp_lt_u32 s67, 0x80000000\n"
    "s_cbranch_scc1 L_rt8x\n"
    "L_rt8:\n"
    "s_sleep 1\n"
    "s_add_u32 s67, s67, 4\n"
    "s_cbranch_scc0 L_rt8\n"
    "L_rt8x:\n"
    "s_waitcnt vmcnt(8)\n"
    "s_barrier\n"
    "v_mfma_f32_16x16x32_bf16 v[120:123], a[96:99], a[0:3], v[128:131]\n"
    "v_exp_f32_e32 v112, v112\n"
    "v_mfma_f32_16x16x32_bf16 v[120:123], a[100:103], a[4:7], v[120:123]\n"
    "ds_read_b128 a[64:67], v140 offset:16384\n"
    "s_add_u32 m0, s63, 0xc000\n"
    "v_mfma_f32_16x16x32_bf16 v[120:123], a[104:107], a[8:11], v[120:123]\n"
    "v_exp_f32_e64 v113, v113\n"
    "v_mfma_f32_16x16x32_bf16 v[120:123], a[108:111], a[12:15], v[120:123]\n"
    "ds_read_b128 a[80:83], v140 offset:20480\n"
    "v_cvt_pk_bf16_f32 v112, v112, v113\n"
    "v_mfma_f32_16x16x32_bf16 v[32:35], a[128:131], v[104:107], v[32:35]\n"
    "v_exp_f32_e64 v114, v114\n"
    "v_mfma_f32_16x16x32_bf16 v[36:39], a[132:135], v[104:107], v[36:39]\n"
    "ds_read_b128 a[68:71], v141 offset:16384\n"
    "v_mfma_f32_16x16x32_bf16 v[40:43], a[136:139], v[104:107], v[40:43]\n"
    "v_exp_f32_e64 v115, v115\n"
    "v_mfma_f32_16x16x32_bf16 v[44:47], a[140:143], v[104:107], v[44:47]\n"
    "s_add_u32 s68, s63, 0x2c000\n"
    "v_cvt_pk_bf16_f32 v113, v114, v115\n"
    "v_mfma_f32_16x16x32_bf16 v[124:127], a[112:115], a[0:3], v[128:131]\n"
    "v_exp_f32_e64 v116, v116\n"
    "v_mfma_f32_16x16x32_bf16 v[124:127], a[116:119], a[4:7], v[124:127]\n"
    "ds_read_b128 a[84:87], v141 offset:20480\n"
    "ds_read_b64_tr_b16 a[174:175], v151 offset:4096\n"
    "v_mfma_f32_16x16x32_bf16 v[124:127], a[120:123], a[8:11], v[124:127]\n"
    "v_exp_f32_e64 v117, v117\n"
    "v_mfma_f32_16x16x32_bf16 v[124:127], a[124:127], a[12:15], v[124:127]\n"
    "ds_read_b64_tr_b16 a[176:177], v148 offset:1024\n"
    "v_cvt_pk_bf16_f32 v114, v116, v117\n"
    "v_mfma_f32_16x16x32_bf16 v[48:51], a[144:147], v[104:107], v[48:51]\n"
    "v_exp_f32_e64 v118, v118\n"
    "v_mfma_f32_16x16x32_bf16 v[52:55], a[148:151], v[104:107], v[52:55]\n"
    "buffer_load_dwordx4 v152, s[36:39], s68 offen nt lds\n"
    "v_mfma_f32_16x16x32_bf16 v[56:59], a[152:155], v[104:107], v[56:59]\n"
    "v_exp_f32_e64 v119, v119\n"
    "v_mfma_f32_16x16x32_bf16 v[60:63], a[156:159], v[104:107], v[60:63]\n"
    "ds_read_b64_tr_b16 a[178:179], v148 offset:5120\n"
    "v_cvt_pk_bf16_f32 v115, v118, v119\n"
    "v_mfma_f32_16x16x32_bf16 a[52:55], a[60:63], v[104:107], a[52:55]\n"
    "ds_read_b64_tr_b16 a[180:181], v149 offset:1024\n"
    "v_mfma_f32_16x16x32_bf16 v[96:99], a[96:99], a[16:19], v[132:135]\n"
    "v_exp_f32_e64 v120, v120\n"
    "v_mfma_f32_16x16x32_bf16 v[96:99], a[100:103], a[20:23], v[96:99]\n"
    "ds_read_b128 a[72:75], v140 offset:17408\n"
    "buffer_load_dwordx4 v153, s[36:39], s68 offen offset:1024 nt lds\n"
    "v_mfma_f32_16x16x32_bf16 v[96:99], a[104:107], a[24:27], v[96:99]\n"
    "v_exp_f32_e64 v121, v121\n"
    "v_mfma_f32_16x16x32_bf16 v[96:99], a[108:111], a[28:31], v[96:99]\n"
    "ds_read_b128 a[88:91], v140 offset:21504\n"
    "v_cvt_pk_bf16_f32 v120, v120, v121\n"
    "v_mfma_f32_16x16x32_bf16 v[64:67], a[128:131], v[112:115], v[64:67]\n"
    "v_exp_f32_e64 v122, v122\n"
    "v_mfma_f32_16x16x32_bf16 v[68:71], a[132:135], v[112:115], v[68:71]\n"
    "ds_read_b128 a[76:79], v141 offset:17408\n"
    "v_mfma_f32_16x16x32_bf16 v[72:75], a[136:139], v[112:115], v[72:75]\n"
    "v_exp_f32_e64 v123, v123\n"
    "v_mfma_f32_16x16x32_bf16 v[76:79], a[140:143], v[112:115], v[76:79]\n"
    "ds_read_b128 a[92:95], v141 offset:21504\n"
    "v_cvt_pk_bf16_f32 v121, v122, v123\n"
    "v_mfma_f32_16x16x32_bf16 v[100:103], a[112:115], a[16:19], v[132:135]\n"
    "v_exp_f32_e64 v124, v124\n"
    "v_mfma_f32_16x16x32_bf16 v[100:103], a[116:119], a[20:23], v[100:103]\n"
    "ds_read_b64_tr_b16 a[182:183], v149 offset:5120\n"
    "v_mfma_f32_16x16x32_bf16 v[100:103], a[120:123], a[24:27], v[100:103]\n"
    "v_exp_f32_e64 v125, v125\n"
    "v_mfma_f32_16x16x32_bf16 v[100:103], a[124:127], a[28:31], v[100:103]\n"
    "ds_read_b64_tr_b16 a[184:185], v150 offset:1024\n"
    "v_cvt_pk_bf16_f32 v122, v124, v125\n"
    "v_mfma_f32_16x16x32_bf16 v[80:83], a[144:147], v[112:115], v[80:83]\n"
    "v_exp_f32_e64 v126, v126\n"
    "v_mfma_f32_16x16x32_bf16 v[84:87], a[148:151], v[112:115], v[84:87]\n"
    "ds_read_b64_tr_b16 a[186:187], v150 offset:5120\n"
    "v_mfma_f32_16x16x32_bf16 v[88:91], a[152:155], v[112:115], v[88:91]\n"
    "v_exp_f32_e64 v127, v127\n"
    "v_mfma_f32_16x16x32_bf16 v[92:95], a[156:159], v[112:115], v[92:95]\n"
    "s_waitcnt lgkmcnt(3)\n"
    "s_nop 0\n"
    "ds_read_b64_tr_b16 a[188:189], v151 offset:1024\n"
    "v_cvt_pk_bf16_f32 v123, v126, v127\n"
    "v_mfma_f32_16x16x32_bf16 a[56:59], a[60:63], v[112:115], a[56:59]\n"
    "ds_read_b64_tr_b16 a[190:191], v151 offset:5120\n"
    "v_mfma_f32_16x16x32_bf16 v[104:107], a[64:67], a[0:3], v[128:131]\n"
    "v_exp_f32_e64 v96, v96\n"
    "v_mfma_f32_16x16x32_bf16 v[104:107], a[68:71], a[4:7], v[104:107]\n"
    "ds_read_b64_tr_b16 a[128:129], v144 offset:16384\n"
    "buffer_load_dwordx4 v152, s[36:39], s68 offen offset:2048 nt lds\n"
    "v_mfma_f32_16x16x32_bf16 v[104:107], a[72:75], a[8:11], v[104:107]\n"
    "v_exp_f32_e64 v97, v97\n"
    "v_mfma_f32_16x16x32_bf16 v[104:107], a[76:79], a[12:15], v[104:107]\n"
    "ds_read_b64_tr_b16 a[130:131], v144 offset:20480\n"
    "v_cvt_pk_bf16_f32 v96, v96, v97\n"
    "v_mfma_f32_16x16x32_bf16 v[0:3], a[160:163], v[120:123], v[0:3]\n"
    "v_exp_f32_e64 v98, v98\n"
    "v_mfma_f32_16x16x32_bf16 v[4:7], a[164:167], v[120:123], v[4:7]\n"
    "ds_read_b64_tr_b16 a[132:133], v145 offset:16384\n"
    "v_mfma_f32_16x16x32_bf16 v[8:11], a[168:171], v[120:123], v[8:11]\n"
    "v_exp_f32_e64 v99, v99\n"
    "v_mfma_f32_16x16x32_bf16 v[12:15], a[172:175], v[120:123], v[12:15]\n"
    "ds_read_b64_tr_b16 a[134:135], v145 offset:20480\n"
    "v_cvt_pk_bf16_f32 v97, v98, v99\n"
    "v_mfma_f32_16x16x32_bf16 v[108:111], a[80:83], a[0:3], v[128:131]\n"
    "v_exp_f32_e64 v100, v100\n"
    "v_mfma_f32_16x16x32_bf16 v[108:111], a[84:87], a[4:7], v[108:111]\n"
    "ds_read_b64_tr_b16 a[136:137], v146 offset:16384\n"
    "ds_read_b64_tr_b16 a[138:139], v146 offset:20480\n"
    "v_mfma_f32_16x16x32_bf16 v[108:111], a[88:91], a[8:11], v[108:111]\n"
    "v_exp_f32_e64 v101, v101\n"
    "v_mfma_f32_16x16x32_bf16 v[108:111], a[92:95], a[12:15], v[108:111]\n"
    "ds_read_b64_tr_b16 a[140:141], v147 offset:16384\n"
    "v_cvt_pk_bf16_f32 v98, v100, v101\n"
    "v_mfma_f32_16x16x32_bf16 v[16:19], a[176:179], v[120:123], v[16:19]\n"
    "v_exp_f32_e32 v102, v102\n"
    "s_waitcnt lgkmcnt(4)\n"
    "v_mfma_f32_16x16x32_bf16 v[20:23], a[180:183], v[120:123], v[20:23]\n"
    "ds_read_b64_tr_b16 a[142:143], v147 offset:20480\n"
    "v_mfma_f32_16x16x32_bf16 v[24:27], a[184:187], v[120:123], v[24:27]\n"
    "v_exp_f32_e64 v103, v103\n"
    "v_mfma_f32_16x16x32_bf16 v[28:31], a[188:191], v[120:123], v[28:31]\n"
    "ds_read_b128 a[96:99], v142 offset:16384\n"
    "v_cvt_pk_bf16_f32 v99, v102, v103\n"
    "v_mfma_f32_16x16x32_bf16 a[48:51], a[60:63], v[120:123], a[48:51]\n"
    "ds_read_b128 a[112:115], v142 offset:20480\n"
    "v_mfma_f32_16x16x32_bf16 v[112:115], a[64:67], a[16:19], v[132:135]\n"
    "v_exp_f32_e64 v104, v104\n"
    "v_mfma_f32_16x16x32_bf16 v[112:115], a[68:71], a[20:23], v[112:115]\n"
    "ds_read_b64_tr_b16 a[144:145], v144 offset:17408\n"
    "buffer_load_dwordx4 v153, s[36:39], s68 offen offset:3072 nt lds\n"
    "v_mfma_f32_16x16x32_bf16 v[112:115], a[72:75], a[24:27], v[112:115]\n"
    "v_exp_f32_e64 v105, v105\n"
    "v_mfma_f32_16x16x32_bf16 v[112:115], a[76:79], a[28:31], v[112:115]\n"
    "ds_read_b64_tr_b16 a[146:147], v144 offset:21504\n"
    "v_cvt_pk_bf16_f32 v104, v104, v105\n"
    "v_mfma_f32_16x16x32_bf16 v[32:35], a[160:163], v[96:99], v[32:35]\n"
    "v_exp_f32_e64 v106, v106\n"
    "v_mfma_f32_16x16x32_bf16 v[36:39], a[164:167], v[96:99], v[36:39]\n"
    "ds_read_b64_tr_b16 a[148:149], v145 offset:17408\n"
    "v_mfma_f32_16x16x32_bf16 v[40:43], a[168:171], v[96:99], v[40:43]\n"
    "v_exp_f32_e64 v107, v107\n"
    "v_mfma_f32_16x16x32_bf16 v[44:47], a[172:175], v[96:99], v[44:47]\n"
    "ds_read_b64_tr_b16 a[150:151], v145 offset:21504\n"
    "v_cvt_pk_bf16_f32 v105, v106, v107\n"
    "v_mfma_f32_16x16x32_bf16 v[116:119], a[80:83], a[16:19], v[132:135]\n"
    "v_exp_f32_e64 v108, v108\n"
    "v_mfma_f32_16x16x32_bf16 v[116:119], a[84:87], a[20:23], v[116:119]\n"
    "ds_read_b64_tr_b16 a[152:153], v146 offset:17408\n"
    "ds_read_b64_tr_b16 a[154:155], v146 offset:21504\n"
    "v_mfma_f32_16x16x32_bf16 v[116:119], a[88:91], a[24:27], v[116:119]\n"
    "v_exp_f32_e64 v109, v109\n"
    "v_mfma_f32_16x16x32_bf16 v[116:119], a[92:95], a[28:31], v[116:119]\n"
    "ds_read_b64_tr_b16 a[156:157], v147 offset:17408\n"
    "v_cvt_pk_bf16_f32 v106, v108, v109\n"
    "v_mfma_f32_16x16x32_bf16 v[48:51], a[176:179], v[96:99], v[48:51]\n"
    "v_exp_f32_e64 v110, v110\n"
    "v_mfma_f32_16x16x32_bf16 v[52:55], a[180:183], v[96:99], v[52:55]\n"
    "ds_read_b64_tr_b16 a[158:159], v147 offset:21504\n"
    "v_mfma_f32_16x16x32_bf16 v[56:59], a[184:187], v[96:99], v[56:59]\n"
    "v_exp_f32_e64 v111, v111\n"
    "v_mfma_f32_16x16x32_bf16 v[60:63], a[188:191], v[96:99], v[60:63]\n"
    "s_waitcnt lgkmcnt(4)\n"
    "s_nop 0\n"
    "ds_read_b128 a[100:103], v143 offset:16384\n"
    "v_cvt_pk_bf16_f32 v107, v110, v111\n"
    "v_mfma_f32_16x16x32_bf16 a[52:55], a[60:63], v[96:99], a[52:55]\n"
    "ds_read_b128 a[116:119], v143 offset:20480\n"
    "v_mfma_f32_16x16x32_bf16 v[120:123], a[64:67], a[32:35], v[136:139]\n"
    "v_exp_f32_e64 v112, v112\n"
    "v_mfma_f32_16x16x32_bf16 v[120:123], a[68:71], a[36:39], v[120:123]\n"
    "ds_read_b128 a[104:107], v142 offset:17408\n"
    "s_add_u32 m0, s63, 0x1c000\n"
    "v_mfma_f32_16x16x32_bf16 v[120:123], a[72:75], a[40:43], v[120:123]\n"
    "v_exp_f32_e64 v113, v113\n"
    "v_mfma_f32_16x16x32_bf16 v[120:123], a[76:79], a[44:47], v[120:123]\n"
    "ds_read_b128 a[120:123], v142 offset:21504\n"
    "v_cvt_pk_bf16_f32 v112, v112, v113\n"
    "v_mfma_f32_16x16x32_bf16 v[0:3], a[128:131], v[104:107], v[0:3]\n"
    "v_exp_f32_e64 v114, v114\n"
    "v_mfma_f32_16x16x32_bf16 v[4:7], a[132:135], v[104:107], v[4:7]\n"
    "ds_read_b128 a[108:111], v143 offset:17408\n"
    "ds_read_b128 a[124:127], v143 offset:21504\n"
    "v_mfma_f32_16x16x32_bf16 v[8:11], a[136:139], v[104:107], v[8:11]\n"
    "v_exp_f32_e64 v115, v115\n"
    "v_mfma_f32_16x16x32_bf16 v[12:15], a[140:143], v[104:107], v[12:15]\n"
    "s_add_u32 s68, s63, 0x2c000\n"
    "v_cvt_pk_bf16_f32 v113, v114, v115\n"
    "v_mfma_f32_16x16x32_bf16 v[124:127], a[80:83], a[32:35], v[136:139]\n"
    "v_exp_f32_e64 v116, v116\n"
    "v_mfma_f32_16x16x32_bf16 v[124:127], a[84:87], a[36:39], v[124:127]\n"
    "ds_read_b64_tr_b16 a[160:161], v148 offset:16384\n"
    "ds_read_b64_tr_b16 a[162:163], v148 offset:20480\n"
    "v_mfma_f32_16x16x32_bf16 v[124:127], a[88:91], a[40:43], v[124:127]\n"
    "v_exp_f32_e64 v117, v117\n"
    "v_mfma_f32_16x16x32_bf16 v[124:127], a[92:95], a[44:47], v[124:127]\n"
    "ds_read_b64_tr_b16 a[164:165], v149 offset:16384\n"
    "v_cvt_pk_bf16_f32 v114, v116, v117\n"
    "v_mfma_f32_16x16x32_bf16 v[16:19], a[144:147], v[104:107], v[16:19]\n"
    "v_exp_f32_e64 v118, v118\n"
    "v_mfma_f32_16x16x32_bf16 v[20:23], a[148:151], v[104:107], v[20:23]\n"
    "ds_read_b64_tr_b16 a[166:167], v149 offset:20480\n"
    "buffer_load_dwordx4 v152, s[40:43], s68 offen nt lds\n"
    "s_waitcnt lgkmcnt(4)\n"
    "v_mfma_f32_16x16x32_bf16 v[24:27], a[152:155], v[104:107], v[24:27]\n"
    "v_exp_f32_e32 v119, v119\n"
    "v_mfma_f32_16x16x32_bf16 v[28:31], a[156:159], v[104:107], v[28:31]\n"
    "ds_read_b64_tr_b16 a[168:169], v150 offset:16384\n"
    "v_cvt_pk_bf16_f32 v115, v118, v119\n"
    "v_mfma_f32_16x16x32_bf16 a[48:51], a[60:63], v[104:107], a[48:51]\n"
    "ds_read_b64_tr_b16 a[170:171], v150 offset:20480\n"
    "ds_read_b64_tr_b16 a[172:173], v151 offset:16384\n"
    "buffer_load_dwordx4 v153, s[40:43], s68 offen offset:1024 nt lds\n"
    "buffer_load_dwordx4 v152, s[40:43], s68 offen offset:2048 nt lds\n"
    "buffer_load_dwordx4 v153, s[40:43], s68 offen offset:3072 nt lds\n"
    "s_memrealtime s[78:79]\n"
    "s_add_u32 s99, s99, s62\n"
    "s_waitcnt lgkmcnt(0)\n"
    "s_sub_u32 s67, s78, s99\n"
    "s_cmp_lt_u32 s67, 0x80000000\n"
    "s_cbranch_scc1 L_rt9x\n"
    "L_rt9:\n"
    "s_sleep 1\n"
    "s_add_u32 s67, s67, 4\n"
    "s_cbranch_scc0 L_rt9\n"
    "L_rt9x:\n"
    "s_waitcnt vmcnt(8)\n"
    "s_barrier\n"
    "v_mfma_f32_16x16x32_bf16 v[96:99], a[96:99], a[0:3], v[128:131]\n"
    "v_exp_f32_e32 v120, v120\n"
    "v_mfma_f32_16x16x32_bf16 v[96:99], a[100:103], a[4:7], v[96:99]\n"
    "ds_read_b128 a[64:67], v140 offset:32768\n"
    "s_add_u32 m0, s63, 0\n"
    "v_mfma_f32_16x16x32_bf16 v[96:99], a[104:107], a[8:11], v[96:99]\n"
    "v_exp_f32_e32 v121, v121\n"
    "v_mfma_f32_16x16x32_bf16 v[96:99], a[108:111], a[12:15], v[96:99]\n"
    "ds_read_b128 a[80:83], v140 offset:36864\n"
    "v_cvt_pk_bf16_f32 v120, v120, v121\n"
    "v_mfma_f32_16x16x32_bf16 v[32:35], a[128:131], v[112:115], v[32:35]\n"
    "v_exp_f32_e64 v122, v122\n"
    "v_mfma_f32_16x16x32_bf16 v[36:39], a[132:135], v[112:115], v[36:39]\n"
    "ds_read_b128 a[68:71], v141 offset:32768\n"
    "v_mfma_f32_16x16x32_bf16 v[40:43], a[136:139], v[112:115], v[40:43]\n"
    "v_exp_f32_e64 v123, v123\n"
    "v_mfma_f32_16x16x32_bf16 v[44:47], a[140:143], v[112:115], v[44:47]\n"
    "s_add_u32 s68, s63, 0x30000\n"
    "v_cvt_pk_bf16_f32 v121, v122, v123\n"
    "v_mfma_f32_16x16x32_bf16 v[100:103], a[112:115], a[0:3], v[128:131]\n"
    "v_exp_f32_e64 v124, v124\n"
    "v_mfma_f32_16x16x32_bf16 v[100:103], a[116:119], a[4:7], v[100:103]\n"
    "ds_read_b128 a[84:87], v141 offset:36864\n"
    "ds_read_b64_tr_b16 a[174:175], v151 offset:20480\n"
    "v_mfma_f32_16x16x32_bf16 v[100:103], a[120:123], a[8:11], v[100:103]\n"
    "v_exp_f32_e64 v125, v125\n"
    "v_mfma_f32_16x16x32_bf16 v[100:103], a[124:127], a[12:15], v[100:103]\n"
    "ds_read_b64_tr_b16 a[176:177], v148 offset:17408\n"
    "v_cvt_pk_bf16_f32 v122, v124, v125\n"
    "v_mfma_f32_16x16x32_bf16 v[48:51], a[144:147], v[112:115], v[48:51]\n"
    "v_exp_f32_e64 v126, v126\n"
    "v_mfma_f32_16x16x32_bf16 v[52:55], a[148:151], v[112:115], v[52:55]\n"
    "buffer_load_dwordx4 v152, s[36:39], s68 offen nt lds\n"
    "v_mfma_f32_16x16x32_bf16 v[56:59], a[152:155], v[112:115], v[56:59]\n"
    "v_exp_f32_e64 v127, v127\n"
    "v_mfma_f32_16x16x32_bf16 v[60:63], a[156:159], v[112:115], v[60:63]\n"
    "ds_read_b64_tr_b16 a[178:179], v148 offset:21504\n"
    "v_cvt_pk_bf16_f32 v123, v126, v127\n"
    "v_mfma_f32_16x16x32_bf16 a[52:55], a[60:63], v[112:115], a[52:55]\n"
    "ds_read_b64_tr_b16 a[180:181], v149 offset:17408\n"
    "v_mfma_f32_16x16x32_bf16 v[104:107], a[96:99], a[16:19], v[132:135]\n"
    "v_exp_f32_e64 v96, v96\n"
    "v_mfma_f32_16x16x32_bf16 v[104:107], a[100:103], a[20:23], v[104:107]\n"
    "ds_read_b128 a[72:75], v140 offset:33792\n"
    "buffer_load_dwordx4 v153, s[36:39], s68 offen offset:1024 nt lds\n"
    "v_mfma_f32_16x16x32_bf16 v[104:107], a[104:107], a[24:27], v[104:107]\n"
    "v_exp_f32_e64 v97, v97\n"
    "v_mfma_f32_16x16x32_bf16 v[104:107], a[108:111], a[28:31], v[104:107]\n"
    "ds_read_b128 a[88:91], v140 offset:37888\n"
    "v_cvt_pk_bf16_f32 v96, v96, v97\n"
    "v_mfma_f32_16x16x32_bf16 v[64:67], a[128:131], v[120:123], v[64:67]\n"
    "v_exp_f32_e64 v98, v98\n"
    "v_mfma_f32_16x16x32_bf16 v[68:71], a[132:135], v[120:123], v[68:71]\n"
    "ds_read_b128 a[76:79], v141 offset:33792\n"
    "v_mfma_f32_16x16x32_bf16 v[72:75], a[136:139], v[120:123], v[72:75]\n"
    "v_exp_f32_e64 v99, v99\n"
    "v_mfma_f32_16x16x32_bf16 v[76:79], a[140:143], v[120:123], v[76:79]\n"
    "ds_read_b128 a[92:95], v141 offset:37888\n"
    "v_cvt_pk_bf16_f32 v97, v98, v99\n"
    "v_mfma_f32_16x16x32_bf16 v[108:111], a[112:115], a[16:19], v[132:135]\n"
    "v_exp_f32_e64 v100, v100\n"
    "v_mfma_f32_16x16x32_bf16 v[108:111], a[116:119], a[20:23], v[108:111]\n"
    "ds_read_b64_tr_b16 a[182:183], v149 offset:21504\n"
    "v_mfma_f32_16x16x32_bf16 v[108:111], a[120:123], a[24:27], v[108:111]\n"
    "v_exp_f32_e64 v101, v101\n"
    "v_mfma_f32_16x16x32_bf16 v[108:111], a[124:127], a[28:31], v[108:111]\n"
    "ds_read_b64_tr_b16 a[184:185], v150 offset:17408\n"
    "v_cvt_pk_bf16_f32 v98, v100, v101\n"
    "v_mfma_f32_16x16x32_bf16 v[80:83], a[144:147], v[120:123], v[80:83]\n"
    "v_exp_f32_e64 v102, v102\n"
    "v_mfma_f32_16x16x32_bf16 v[84:87], a[148:151], v[120:123], v[84:87]\n"
    "ds_read_b64_tr_b16 a[186:187], v150 offset:21504\n"
    "v_mfma_f32_16x16x32_bf16 v[88:91], a[152:155], v[120:123], v[88:91]\n"
    "v_exp_f32_e64 v103, v103\n"
    "v_mfma_f32_16x16x32_bf16 v[92:95], a[156:159], v[120:123], v[92:95]\n"
    "s_waitcnt lgkmcnt(3)\n"
    "s_nop 0\n"
    "ds_read_b64_tr_b16 a[188:189], v151 offset:17408\n"
    "v_cvt_pk_bf16_f32 v99, v102, v103\n"
    "v_mfma_f32_16x16x32_bf16 a[56:59], a[60:63], v[120:123], a[56:59]\n"
    "ds_read_b64_tr_b16 a[190:191], v151 offset:21504\n"
    "v_mfma_f32_16x16x32_bf16 v[112:115], a[64:67], a[0:3], v[128:131]\n"
    "v_exp_f32_e64 v104, v104\n"
    "v_mfma_f32_16x16x32_bf16 v[112:115], a[68:71], a[4:7], v[112:115]\n"
    "ds_read_b64_tr_b16 a[128:129], v144 offset:32768\n"
    "buffer_load_dwordx4 v152, s[36:39], s68 offen offset:2048 nt lds\n"
    "v_mfma_f32_16x16x32_bf16 v[112:115], a[72:75], a[8:11], v[112:115]\n"
    "v_exp_f32_e64 v105, v105\n"
    "v_mfma_f32_16x16x32_bf16 v[112:115], a[76:79], a[12:15], v[112:115]\n"
    "ds_read_b64_tr_b16 a[130:131], v144 offset:36864\n"
    "v_cvt_pk_bf16_f32 v104, v104, v105\n"
    "v_mfma_f32_16x16x32_bf16 v[0:3], a[160:163], v[96:99], v[0:3]\n"
    "v_exp_f32_e64 v106, v106\n"
    "v_mfma_f32_16x16x32_bf16 v[4:7], a[164:167], v[96:99], v[4:7]\n"
    "ds_read_b64_tr_b16 a[132:133], v145 offset:32768\n"
    "v_mfma_f32_16x16x32_bf16 v[8:11], a[168:171], v[96:99], v[8:11]\n"
    "v_exp_f32_e64 v107, v107\n"
    "v_mfma_f32_16x16x32_bf16 v[12:15], a[172:175], v[96:99], v[12:15]\n"
    "ds_read_b64_tr_b16 a[134:135], v145 offset:36864\n"
    "v_cvt_pk_bf16_f32 v105, v106, v107\n"
    "v_mfma_f32_16x16x32_bf16 v[116:119], a[80:83], a[0:3], v[128:131]\n"
    "v_exp_f32_e64 v108, v108\n"
    "v_mfma_f32_16x16x32_bf16 v[116:119], a[84:87], a[4:7], v[116:119]\n"
    "ds_read_b64_tr_b16 a[136:137], v146 offset:32768\n"
    "ds_read_b64_tr_b16 a[138:139], v146 offset:36864\n"
    "v_mfma_f32_16x16x32_bf16 v[116:119], a[88:91], a[8:11], v[116:119]\n"
    "v_exp_f32_e64 v109, v109\n"
    "v_mfma_f32_16x16x32_bf16 v[116:119], a[92:95], a[12:15], v[116:119]\n"
    "ds_read_b64_tr_b16 a[140:141], v147 offset:32768\n"
    "v_cvt_pk_bf16_f32 v106, v108, v109\n"
    "v_mfma_f32_16x16x32_bf16 v[16:19], a[176:179], v[96:99], v[16:19]\n"
    "v_exp_f32_e32 v110, v110\n"
    "s_waitcnt lgkmcnt(4)\n"
    "v_mfma_f32_16x16x32_bf16 v[20:23], a[180:183], v[96:99], v[20:23]\n"
    "ds_read_b64_tr_b16 a[142:143], v147 offset:36864\n"
    "v_mfma_f32_16x16x32_bf16 v[24:27], a[184:187], v[96:99], v[24:27]\n"
    "v_exp_f32_e64 v111, v111\n"
    "v_mfma_f32_16x16x32_bf16 v[28:31], a[188:191], v[96:99], v[28:31]\n"
    "ds_read_b128 a[96:99], v142 offset:32768\n"
    "v_cvt_pk_bf16_f32 v107, v110, v111\n"
    "v_mfma_f32_16x16x32_bf16 a[48:51], a[60:63], v[96:99], a[48:51]\n"
    "ds_read_b128 a[112:115], v142 offset:36864\n"
    "v_mfma_f32_16x16x32_bf16 v[120:123], a[64:67], a[16:19], v[132:135]\n"
    "v_exp_f32_e64 v112, v112\n"
    "v_mfma_f32_16x16x32_bf16 v[120:123], a[68:71], a[20:23], v[120:123]\n"
    "ds_read_b64_tr_b16 a[144:145], v144 offset:33792\n"
    "buffer_load_dwordx4 v153, s[36:39], s68 offen offset:3072 nt lds\n"
    "v_mfma_f32_16x16x32_bf16 v[120:123], a[72:75], a[24:27], v[120:123]\n"
    "v_exp_f32_e64 v113, v113\n"
    "v_mfma_f32_16x16x32_bf16 v[120:123], a[76:79], a[28:31], v[120:123]\n"
    "ds_read_b64_tr_b16 a[146:147], v144 offset:37888\n"
    "v_cvt_pk_bf16_f32 v112, v112, v113\n"
    "v_mfma_f32_16x16x32_bf16 v[32:35], a[160:163], v[104:107], v[32:35]\n"
    "v_exp_f32_e64 v114, v114\n"
    "v_mfma_f32_16x16x32_bf16 v[36:39], a[164:167], v[104:107], v[36:39]\n"
    "ds_read_b64_tr_b16 a[148:149], v145 offset:33792\n"
    "v_mfma_f32_16x16x32_bf16 v[40:43], a[168:171], v[104:107], v[40:43]\n"
    "v_exp_f32_e64 v115, v115\n"
    "v_mfma_f32_16x16x32_bf16 v[44:47], a[172:175], v[104:107], v[44:47]\n"
    "ds_read_b64_tr_b16 a[150:151], v145 offset:37888\n"
    "v_cvt_pk_bf16_f32 v113, v114, v115\n"
    "v_mfma_f32_16x16x32_bf16 v[124:127], a[80:83], a[16:19], v[132:135]\n"
    "v_exp_f32_e64 v116, v116\n"
    "v_mfma_f32_16x16x32_bf16 v[124:127], a[84:87], a[20:23], v[124:127]\n"
    "ds_read_b64_tr_b16 a[152:153], v146 offset:33792\n"
    "ds_read_b64_tr_b16 a[154:155], v146 offset:37888\n"
    "v_mfma_f32_16x16x32_bf16 v[124:127], a[88:91], a[24:27], v[124:127]\n"
    "v_exp_f32_e64 v117, v117\n"
    "v_mfma_f32_16x16x32_bf16 v[124:127], a[92:95], a[28:31], v[124:127]\n"
    "ds_read_b64_tr_b16 a[156:157], v147 offset:33792\n"
    "v_cvt_pk_bf16_f32 v114, v116, v117\n"
    "v_mfma_f32_16x16x32_bf16 v[48:51], a[176:179], v[104:107], v[48:51]\n"
    "v_exp_f32_e64 v118, v118\n"
    "v_mfma_f32_16x16x32_bf16 v[52:55], a[180:183], v[104:107], v[52:55]\n"
    "ds_read_b64_tr_b16 a[158:159], v147 offset:37888\n"
    "v_mfma_f32_16x16x32_bf16 v[56:59], a[184:187], v[104:107], v[56:59]\n"
    "v_exp_f32_e64 v119, v119\n"
    "v_mfma_f32_16x16x32_bf16 v[60:63], a[188:191], v[104:107], v[60:63]\n"
    "s_waitcnt lgkmcnt(4)\n"
    "s_nop 0\n"
    "ds_read_b128 a[100:103], v143 offset:32768\n"
    "v_cvt_pk_bf16_f32 v115, v118, v119\n"
    "v_mfma_f32_16x16x32_bf16 a[52:55], a[60:63], v[104:107], a[52:55]\n"
    "ds_read_b128 a[116:119], v143 offset:36864\n"
    "s_mov_b64 s[44:45], %11\n"
    "s_mov_b32 s46, 0x16800\n"
    "s_mov_b32 s47, 0x20000\n"
    "s_and_b32 s67, %7, 15\n"
    "s_bfe_u32 s68, %7, 0x40004\n"
    "s_mul_i32 s74, s68, 0x180000\n"
    "s_lshl_b32 s73, s67, 15\n"
    "s_add_u32 s73, s73, s74\n"
    "s_lshl_b32 s68, s68, 13\n"
    "s_lshl_b32 s75, s67, 9\n"
    "s_add_u32 s75, s75, s68\n"
    "s_add_u32 s75, s75, 0x1800000\n"
    "v_lshrrev_b32_e32 v156, 2, v154\n"
    "v_lshl_add_u32 v250, v156, 9, s68\n"
    "s_lshl_b32 s68, s60, 10\n"
    "v_add_u32_e32 v248, s68, v155\n"
    "s_lshl_b32 s67, s67, 2\n"
    "s_add_u32 s67, s67, s60\n"
    "s_lshl_b32 s68, s67, 9\n"
    "v_and_b32_e32 v156, 0x1ff, v155\n"
    "v_lshrrev_b32_e32 v157, 5, v154\n"
    "v_lshl_add_u32 v156, v157, 15, v156\n"
    "v_add_u32_e64 v249, s68, v156\n"
    "s_lshr_b32 s68, s67, 3\n"
    "s_lshl_b32 s68, s68, 1\n"
    "v_bfe_u32 v156, v154, 4, 1\n"
    "v_add_u32_e64 v156, s68, v156\n"
    "v_mul_u32_u24_e64 v158, 52, v156\n"
    "v_lshrrev_b32_e64 v158, 8, v158\n"
    "v_mul_u32_u24_e64 v159, 5, v158\n"
    "v_sub_u32_e64 v159, v156, v159\n"
    "v_cmp_eq_u32_e32 vcc, 15, v156\n"
    "v_and_b32_e32 v156, 15, v154\n"
    "s_nop 0\n"
    "v_cndmask_b32_e64 v159, v159, 5, vcc\n"
    "v_lshl_add_u32 v156, v159, 4, v156\n"
    "s_and_b32 s68, s67, 7\n"
    "s_lshl_b32 s68, s68, 4\n"
    "s_nop 0\n"
    "v_lshl_add_u32 v157, v157, 3, s68\n"
    "s_mov_b32 s68, 0x480\n"
    "v_mad_u32_u24 v156, v156, s68, v157\n"
    "s_bfe_u32 s68, %7, 0x40008\n"
    "v_add_u32_e64 v158, s68, v158\n"
    "v_cmp_le_u32_e32 vcc, 9, v158\n"
    "v_add_u32_e32 v157, -9, v158\n"
    "s_nop 0\n"
    "v_cndmask_b32_e64 v158, v158, v157, vcc\n"
    "v_lshl_add_u32 v251, v158, 7, v156\n"
    "v_add_u32_e32 v158, 3, v158\n"
    "v_cmp_le_u32_e32 vcc, 9, v158\n"
    "v_add_u32_e32 v157, -9, v158\n"
    "s_nop 0\n"
    "v_cndmask_b32_e64 v158, v158, v157, vcc\n"
    "v_lshl_add_u32 v252, v158, 7, v156\n"
    "v_add_u32_e32 v158, 3, v158\n"
    "v_cmp_le_u32_e32 vcc, 9, v158\n"
    "v_add_u32_e32 v157, -9, v158\n"
    "s_nop 0\n"
    "s_nop 0\n"
    "v_cndmask_b32_e64 v158, v158, v157, vcc\n"
    "v_lshl_add_u32 v253, v158, 7, v156\n"
    "v_xor_b32_e64 v224, -1, s96\n"
    "v_xor_b32_e64 v225, -1, s97\n"
    "v_xor_b32_e64 v226, -1, s96\n"
    "v_xor_b32_e64 v227, -1, s97\n"
    "s_cmp_lg_u32 s60, 0\n"
    "s_cbranch_scc1 L_noinit\n"
    "s_mov_b64 exec, 0xffffff\n"
    "buffer_store_dwordx4 v[224:227], v155, s[52:55], s75 offen sc1\n"
    "s_mov_b64 exec, -1\n"
    "L_noinit:\n"
    "v_and_b32_e32 v156, 1, v154\n"
    "v_lshlrev_b32_e64 v156, 4, v156\n"
    "s_mov_b32 s68, 0x3f80\n"
    "v_lshlrev_b32_e64 v156, v156, s68\n"
    "v_lshrrev_b32_e64 v157, 1, v154\n"
    "v_lshrrev_b32_e64 v158, 2, v154\n"
    "v_and_b32_e64 v158, 4, v158\n"
    "v_xor_b32_e64 v157, v157, v158\n"
    "v_and_b32_e64 v157, 7, v157\n"
    "v_cmp_eq_u32_e32 vcc, 0, v157\n"
    "s_nop 1\n"
    "v_cndmask_b32_e64 v224, 0, v156, vcc\n"
    "v_cmp_eq_u32_e32 vcc, 1, v157\n"
    "s_nop 1\n"
    "v_cndmask_b32_e64 v225, 0, v156, vcc\n"
    "v_cmp_eq_u32_e32 vcc, 2, v157\n"
    "s_nop 1\n"
    "v_cndmask_b32_e64 v226, 0, v156, vcc\n"
    "v_cmp_eq_u32_e32 vcc, 3, v157\n"
    "s_nop 1\n"
    "v_cndmask_b32_e64 v227, 0, v156, vcc\n"
    "v_mfma_f32_16x16x32_bf16 v[96:99], a[64:67], a[32:35], v[136:139]\n"
    "v_exp_f32_e64 v120, v120\n"
    "v_mfma_f32_16x16x32_bf16 v[96:99], a[68:71], a[36:39], v[96:99]\n"
    "ds_read_b128 a[104:107], v142 offset:33792\n"
    "s_add_u32 m0, s63, 0x10000\n"
    "v_mfma_f32_16x16x32_bf16 v[96:99], a[72:75], a[40:43], v[96:99]\n"
    "v_exp_f32_e64 v121, v121\n"
    "v_mfma_f32_16x16x32_bf16 v[96:99], a[76:79], a[44:47], v[96:99]\n"
    "ds_read_b128 a[120:123], v142 offset:37888\n"
    "v_cvt_pk_bf16_f32 v120, v120, v121\n"
    "v_mfma_f32_16x16x32_bf16 v[0:3], a[128:131], v[112:115], v[0:3]\n"
    "v_exp_f32_e64 v122, v122\n"
    "v_mfma_f32_16x16x32_bf16 v[4:7], a[132:135], v[112:115], v[4:7]\n"
    "ds_read_b128 a[108:111], v143 offset:33792\n"
    "ds_read_b128 a[124:127], v143 offset:37888\n"
    "v_mfma_f32_16x16x32_bf16 v[8:11], a[136:139], v[112:115], v[8:11]\n"
    "v_exp_f32_e64 v123, v123\n"
    "v_mfma_f32_16x16x32_bf16 v[12:15], a[140:143], v[112:115], v[12:15]\n"
    "s_add_u32 s68, s63, 0x30000\n"
    "v_cvt_pk_bf16_f32 v121, v122, v123\n"
    "v_mfma_f32_16x16x32_bf16 v[100:103], a[80:83], a[32:35], v[136:139]\n"
    "v_exp_f32_e64 v124, v124\n"
    "v_mfma_f32_16x16x32_bf16 v[100:103], a[84:87], a[36:39], v[100:103]\n"
    "ds_read_b64_tr_b16 a[160:161], v148 offset:32768\n"
    "ds_read_b64_tr_b16 a[162:163], v148 offset:36864\n"
    "v_mfma_f32_16x16x32_bf16 v[100:103], a[88:91], a[40:43], v[100:103]\n"
    "v_exp_f32_e64 v125, v125\n"
    "v_mfma_f32_16x16x32_bf16 v[100:103], a[92:95], a[44:47], v[100:103]\n"
    "ds_read_b64_tr_b16 a[164:165], v149 offset:32768\n"
    "v_cvt_pk_bf16_f32 v122, v124, v125\n"
    "v_mfma_f32_16x16x32_bf16 v[16:19], a[144:147], v[112:115], v[16:19]\n"
    "v_exp_f32_e64 v126, v126\n"
    "v_mfma_f32_16x16x32_bf16 v[20:23], a[148:151], v[112:115], v[20:23]\n"
    "ds_read_b64_tr_b16 a[166:167], v149 offset:36864\n"
    "buffer_load_dwordx4 v152, s[40:43], s68 offen nt lds\n"
    "s_waitcnt lgkmcnt(4)\n"
    "v_mfma_f32_16x16x32_bf16 v[24:27], a[152:155], v[112:115], v[24:27]\n"
    "v_exp_f32_e32 v127, v127\n"
    "v_mfma_f32_16x16x32_bf16 v[28:31], a[156:159], v[112:115], v[28:31]\n"
    "ds_read_b64_tr_b16 a[168:169], v150 offset:32768\n"
    "v_cvt_pk_bf16_f32 v123, v126, v127\n"
    "v_mfma_f32_16x16x32_bf16 a[48:51], a[60:63], v[112:115], a[48:51]\n"
    "ds_read_b64_tr_b16 a[170:171], v150 offset:36864\n"
    "ds_read_b64_tr_b16 a[172:173], v151 offset:32768\n"
    "buffer_load_dwordx4 v153, s[40:43], s68 offen offset:1024 nt lds\n"
    "buffer_load_dwordx4 v152, s[40:43], s68 offen offset:2048 nt lds\n"
    "buffer_load_dwordx4 v153, s[40:43], s68 offen offset:3072 nt lds\n"
    "s_memrealtime s[78:79]\n"
    "s_add_u32 s99, s99, s62\n"
    "s_waitcnt lgkmcnt(0)\n"
    "s_sub_u32 s67, s78, s99\n"
    "s_cmp_lt_u32 s67, 0x80000000\n"
    "s_cbranch_scc1 L_rt10x\n"
    "L_rt10:\n"
    "s_sleep 1\n"
    "s_add_u32 s67, s67, 4\n"
    "s_cbranch_scc0 L_rt10\n"
    "L_rt10x:\n"
    "s_waitcnt vmcnt(8)\n"
    "s_barrier\n"
    "v_mfma_f32_16x16x32_bf16 v[104:107], a[96:99], a[0:3], v[128:131]\n"
    "v_exp_f32_e32 v96, v96\n"
    "v_mfma_f32_16x16x32_bf16 v[104:107], a[100:103], a[4:7], v[104:107]\n"
    "ds_read_b128 a[64:67], v140 offset:49152\n"
    "s_add_u32 m0, s63, 0x4000\n"
    "v_mfma_f32_16x16x32_bf16 v[104:107], a[104:107], a[8:11], v[104:107]\n"
    "v_exp_f32_e64 v97, v97\n"
    "v_mfma_f32_16x16x32_bf16 v[104:107], a[108:111], a[12:15], v[104:107]\n"
    "ds_read_b128 a[80:83], v140 offset:53248\n"
    "v_cvt_pk_bf16_f32 v96, v96, v97\n"
    "v_mfma_f32_16x16x32_bf16 v[32:35], a[128:131], v[120:123], v[32:35]\n"
    "v_exp_f32_e64 v98, v98\n"
    "v_mfma_f32_16x16x32_bf16 v[36:39], a[132:135], v[120:123], v[36:39]\n"
    "ds_read_b128 a[68:71], v141 offset:49152\n"
    "v_mfma_f32_16x16x32_bf16 v[40:43], a[136:139], v[120:123], v[40:43]\n"
    "v_exp_f32_e64 v99, v99\n"
    "v_mfma_f32_16x16x32_bf16 v[44:47], a[140:143], v[120:123], v[44:47]\n"
    "s_add_u32 s68, s63, 0x34000\n"
    "v_cvt_pk_bf16_f32 v97, v98, v99\n"
    "v_mfma_f32_16x16x32_bf16 v[108:111], a[112:115], a[0:3], v[128:131]\n"
    "v_exp_f32_e64 v100, v100\n"
    "v_mfma_f32_16x16x32_bf16 v[108:111], a[116:119], a[4:7], v[108:111]\n"
    "ds_read_b128 a[84:87], v141 offset:53248\n"
    "ds_read_b64_tr_b16 a[174:175], v151 offset:36864\n"
    "v_mfma_f32_16x16x32_bf16 v[108:111], a[120:123], a[8:11], v[108:111]\n"
    "v_exp_f32_e64 v101, v101\n"
    "v_mfma_f32_16x16x32_bf16 v[108:111], a[124:127], a[12:15], v[108:111]\n"
    "ds_read_b64_tr_b16 a[176:177], v148 offset:33792\n"
    "v_cvt_pk_bf16_f32 v98, v100, v101\n"
    "v_mfma_f32_16x16x32_bf16 v[48:51], a[144:147], v[120:123], v[48:51]\n"
    "v_exp_f32_e64 v102, v102\n"
    "v_mfma_f32_16x16x32_bf16 v[52:55], a[148:151], v[120:123], v[52:55]\n"
    "buffer_load_dwordx4 v152, s[36:39], s68 offen nt lds\n"
    "v_mfma_f32_16x16x32_bf16 v[56:59], a[152:155], v[120:123], v[56:59]\n"
    "v_exp_f32_e64 v103, v103\n"
    "v_mfma_f32_16x16x32_bf16 v[60:63], a[156:159], v[120:123], v[60:63]\n"
    "ds_read_b64_tr_b16 a[178:179], v148 offset:37888\n"
    "v_cvt_pk_bf16_f32 v99, v102, v103\n"
    "v_mfma_f32_16x16x32_bf16 a[52:55], a[60:63], v[120:123], a[52:55]\n"
    "ds_read_b64_tr_b16 a[180:181], v149 offset:33792\n"
    "v_mfma_f32_16x16x32_bf16 v[112:115], a[96:99], a[16:19], v[132:135]\n"
    "v_exp_f32_e64 v104, v104\n"
    "v_mfma_f32_16x16x32_bf16 v[112:115], a[100:103], a[20:23], v[112:115]\n"
    "ds_read_b128 a[72:75], v140 offset:50176\n"
    "buffer_load_dwordx4 v153, s[36:39], s68 offen offset:1024 nt lds\n"
    "v_mfma_f32_16x16x32_bf16 v[112:115], a[104:107], a[24:27], v[112:115]\n"
    "v_exp_f32_e64 v105, v105\n"
    "v_mfma_f32_16x16x32_bf16 v[112:115], a[108:111], a[28:31], v[112:115]\n"
    "ds_read_b128 a[88:91], v140 offset:54272\n"
    "v_cvt_pk_bf16_f32 v104, v104, v105\n"
    "v_mfma_f32_16x16x32_bf16 v[64:67], a[128:131], v[96:99], v[64:67]\n"
    "v_exp_f32_e64 v106, v106\n"
    "v_mfma_f32_16x16x32_bf16 v[68:71], a[132:135], v[96:99], v[68:71]\n"
    "ds_read_b128 a[76:79], v141 offset:50176\n"
    "v_mfma_f32_16x16x32_bf16 v[72:75], a[136:139], v[96:99], v[72:75]\n"
    "v_exp_f32_e64 v107, v107\n"
    "v_mfma_f32_16x16x32_bf16 v[76:79], a[140:143], v[96:99], v[76:79]\n"
    "ds_read_b128 a[92:95], v141 offset:54272\n"
    "v_cvt_pk_bf16_f32 v105, v106, v107\n"
    "v_mfma_f32_16x16x32_bf16 v[116:119], a[112:115], a[16:19], v[132:135]\n"
    "v_exp_f32_e64 v108, v108\n"
    "v_mfma_f32_16x16x32_bf16 v[116:119], a[116:119], a[20:23], v[116:119]\n"
    "ds_read_b64_tr_b16 a[182:183], v149 offset:37888\n"
    "v_mfma_f32_16x16x32_bf16 v[116:119], a[120:123], a[24:27], v[116:119]\n"
    "v_exp_f32_e64 v109, v109\n"
    "v_mfma_f32_16x16x32_bf16 v[116:119], a[124:127], a[28:31], v[116:119]\n"
    "ds_read_b64_tr_b16 a[184:185], v150 offset:33792\n"
    "v_cvt_pk_bf16_f32 v106, v108, v109\n"
    "v_mfma_f32_16x16x32_bf16 v[80:83], a[144:147], v[96:99], v[80:83]\n"
    "v_exp_f32_e64 v110, v110\n"
    "v_mfma_f32_16x16x32_bf16 v[84:87], a[148:151], v[96:99], v[84:87]\n"
    "ds_read_b64_tr_b16 a[186:187], v150 offset:37888\n"
    "v_mfma_f32_16x16x32_bf16 v[88:91], a[152:155], v[96:99], v[88:91]\n"
    "v_exp_f32_e64 v111, v111\n"
    "v_mfma_f32_16x16x32_bf16 v[92:95], a[156:159], v[96:99], v[92:95]\n"
    "s_waitcnt lgkmcnt(3)\n"
    "s_nop 0\n"
    "ds_read_b64_tr_b16 a[188:189], v151 offset:33792\n"
    "v_cvt_pk_bf16_f32 v107, v110, v111\n"
    "v_mfma_f32_16x16x32_bf16 a[56:59], a[60:63], v[96:99], a[56:59]\n"
    "ds_read_b64_tr_b16 a[190:191], v151 offset:37888\n"
    "v_mfma_f32_16x16x32_bf16 v[120:123], a[64:67], a[0:3], v[128:131]\n"
    "v_exp_f32_e64 v112, v112\n"
    "v_mfma_f32_16x16x32_bf16 v[120:123], a[68:71], a[4:7], v[120:123]\n"
    "ds_read_b64_tr_b16 a[128:129], v144 offset:49152\n"
    "buffer_load_dwordx4 v152, s[36:39], s68 offen offset:2048 nt lds\n"
    "v_mfma_f32_16x16x32_bf16 v[120:123], a[72:75], a[8:11], v[120:123]\n"
    "v_exp_f32_e64 v113, v113\n"
    "v_mfma_f32_16x16x32_bf16 v[120:123], a[76:79], a[12:15], v[120:123]\n"
    "ds_read_b64_tr_b16 a[130:131], v144 offset:53248\n"
    "v_cvt_pk_bf16_f32 v112, v112, v113\n"
    "v_mfma_f32_16x16x32_bf16 v[0:3], a[160:163], v[104:107], v[0:3]\n"
    "v_exp_f32_e64 v114, v114\n"
    "v_mfma_f32_16x16x32_bf16 v[4:7], a[164:167], v[104:107], v[4:7]\n"
    "ds_read_b64_tr_b16 a[132:133], v145 offset:49152\n"
    "v_mfma_f32_16x16x32_bf16 v[8:11], a[168:171], v[104:107], v[8:11]\n"
    "v_exp_f32_e64 v115, v115\n"
    "v_mfma_f32_16x16x32_bf16 v[12:15], a[172:175], v[104:107], v[12:15]\n"
    "ds_read_b64_tr_b16 a[134:135], v145 offset:53248\n"
    "v_cvt_pk_bf16_f32 v113, v114, v115\n"
    "v_mfma_f32_16x16x32_bf16 v[124:127], a[80:83], a[0:3], v[128:131]\n"
    "v_exp_f32_e64 v116, v116\n"
    "v_mfma_f32_16x16x32_bf16 v[124:127], a[84:87], a[4:7], v[124:127]\n"
    "ds_read_b64_tr_b16 a[136:137], v146 offset:49152\n"
    "ds_read_b64_tr_b16 a[138:139], v146 offset:53248\n"
    "v_mfma_f32_16x16x32_bf16 v[124:127], a[88:91], a[8:11], v[124:127]\n"
    "v_exp_f32_e64 v117, v117\n"
    "v_mfma_f32_16x16x32_bf16 v[124:127], a[92:95], a[12:15], v[124:127]\n"
    "ds_read_b64_tr_b16 a[140:141], v147 offset:49152\n"
    "v_cvt_pk_bf16_f32 v114, v116, v117\n"
    "v_mfma_f32_16x16x32_bf16 v[16:19], a[176:179], v[104:107], v[16:19]\n"
    "v_exp_f32_e32 v118, v118\n"
    "s_waitcnt lgkmcnt(4)\n"
    "v_mfma_f32_16x16x32_bf16 v[20:23], a[180:183], v[104:107], v[20:23]\n"
    "ds_read_b64_tr_b16 a[142:143], v147 offset:53248\n"
    "v_mfma_f32_16x16x32_bf16 v[24:27], a[184:187], v[104:107], v[24:27]\n"
    "v_exp_f32_e64 v119, v119\n"
    "v_mfma_f32_16x16x32_bf16 v[28:31], a[188:191], v[104:107], v[28:31]\n"
    "ds_read_b128 a[96:99], v142 offset:49152\n"
    "v_cvt_pk_bf16_f32 v115, v118, v119\n"
    "v_mfma_f32_16x16x32_bf16 a[48:51], a[60:63], v[104:107], a[48:51]\n"
    "ds_read_b128 a[112:115], v142 offset:53248\n"
    "v_mfma_f32_16x16x32_bf16 v[96:99], a[64:67], a[16:19], v[132:135]\n"
    "v_exp_f32_e64 v120, v120\n"
    "v_mfma_f32_16x16x32_bf16 v[96:99], a[68:71], a[20:23], v[96:99]\n"
    "ds_read_b64_tr_b16 a[144:145], v144 offset:50176\n"
    "buffer_load_dwordx4 v153, s[36:39], s68 offen offset:3072 nt lds\n"
    "v_mfma_f32_16x16x32_bf16 v[96:99], a[72:75], a[24:27], v[96:99]\n"
    "v_exp_f32_e64 v121, v121\n"
    "v_mfma_f32_16x16x32_bf16 v[96:99], a[76:79], a[28:31], v[96:99]\n"
    "ds_read_b64_tr_b16 a[146:147], v144 offset:54272\n"
    "v_cvt_pk_bf16_f32 v120, v120, v121\n"
    "v_mfma_f32_16x16x32_bf16 v[32:35], a[160:163], v[112:115], v[32:35]\n"
    "v_exp_f32_e64 v122, v122\n"
    "v_mfma_f32_16x16x32_bf16 v[36:39], a[164:167], v[112:115], v[36:39]\n"
    "ds_read_b64_tr_b16 a[148:149], v145 offset:50176\n"
    "v_mfma_f32_16x16x32_bf16 v[40:43], a[168:171], v[112:115], v[40:43]\n"
    "v_exp_f32_e64 v123, v123\n"
    "v_mfma_f32_16x16x32_bf16 v[44:47], a[172:175], v[112:115], v[44:47]\n"
    "ds_read_b64_tr_b16 a[150:151], v145 offset:54272\n"
    "v_cvt_pk_bf16_f32 v121, v122, v123\n"
    "v_mfma_f32_16x16x32_bf16 v[100:103], a[80:83], a[16:19], v[132:135]\n"
    "v_exp_f32_e64 v124, v124\n"
    "v_mfma_f32_16x16x32_bf16 v[100:103], a[84:87], a[20:23], v[100:103]\n"
    "ds_read_b64_tr_b16 a[152:153], v146 offset:50176\n"
    "ds_read_b64_tr_b16 a[154:155], v146 offset:54272\n"
    "v_mfma_f32_16x16x32_bf16 v[100:103], a[88:91], a[24:27], v[100:103]\n"
    "v_exp_f32_e64 v125, v125\n"
    "v_mfma_f32_16x16x32_bf16 v[100:103], a[92:95], a[28:31], v[100:103]\n"
    "ds_read_b64_tr_b16 a[156:157], v147 offset:50176\n"
    "v_cvt_pk_bf16_f32 v122, v124, v125\n"
    "v_mfma_f32_16x16x32_bf16 v[48:51], a[176:179], v[112:115], v[48:51]\n"
    "v_exp_f32_e64 v126, v126\n"
    "v_mfma_f32_16x16x32_bf16 v[52:55], a[180:183], v[112:115], v[52:55]\n"
    "ds_read_b64_tr_b16 a[158:159], v147 offset:54272\n"
    "v_mfma_f32_16x16x32_bf16 v[56:59], a[184:187], v[112:115], v[56:59]\n"
    "v_exp_f32_e64 v127, v127\n"
    "v_mfma_f32_16x16x32_bf16 v[60:63], a[188:191], v[112:115], v[60:63]\n"
    "s_waitcnt lgkmcnt(4)\n"
    "s_nop 0\n"
    "ds_read_b128 a[100:103], v143 offset:49152\n"
    "v_cvt_pk_bf16_f32 v123, v126, v127\n"
    "v_mfma_f32_16x16x32_bf16 a[52:55], a[60:63], v[112:115], a[52:55]\n"
    "ds_read_b128 a[116:119], v143 offset:53248\n"
    "v_mfma_f32_16x16x32_bf16 v[104:107], a[64:67], a[32:35], v[136:139]\n"
    "v_exp_f32_e64 v96, v96\n"
    "v_mfma_f32_16x16x32_bf16 v[104:107], a[68:71], a[36:39], v[104:107]\n"
    "ds_read_b128 a[104:107], v142 offset:50176\n"
    "s_add_u32 m0, s63, 0x14000\n"
    "v_mfma_f32_16x16x32_bf16 v[104:107], a[72:75], a[40:43], v[104:107]\n"
    "v_exp_f32_e64 v97, v97\n"
    "v_mfma_f32_16x16x32_bf16 v[104:107], a[76:79], a[44:47], v[104:107]\n"
    "ds_read_b128 a[120:123], v142 offset:54272\n"
    "v_cvt_pk_bf16_f32 v96, v96, v97\n"
    "v_mfma_f32_16x16x32_bf16 v[0:3], a[128:131], v[120:123], v[0:3]\n"
    "v_exp_f32_e64 v98, v98\n"
    "v_mfma_f32_16x16x32_bf16 v[4:7], a[132:135], v[120:123], v[4:7]\n"
    "ds_read_b128 a[108:111], v143 offset:50176\n"
    "ds_read_b128 a[124:127], v143 offset:54272\n"
    "v_mfma_f32_16x16x32_bf16 v[8:11], a[136:139], v[120:123], v[8:11]\n"
    "v_exp_f32_e64 v99, v99\n"
    "v_mfma_f32_16x16x32_bf16 v[12:15], a[140:143], v[120:123], v[12:15]\n"
    "s_add_u32 s68, s63, 0x34000\n"
    "v_cvt_pk_bf16_f32 v97, v98, v99\n"
    "v_mfma_f32_16x16x32_bf16 v[108:111], a[80:83], a[32:35], v[136:139]\n"
    "v_exp_f32_e64 v100, v100\n"
    "v_mfma_f32_16x16x32_bf16 v[108:111], a[84:87], a[36:39], v[108:111]\n"
    "ds_read_b64_tr_b16 a[160:161], v148 offset:49152\n"
    "ds_read_b64_tr_b16 a[162:163], v148 offset:53248\n"
    "v_mfma_f32_16x16x32_bf16 v[108:111], a[88:91], a[40:43], v[108:111]\n"
    "v_exp_f32_e64 v101, v101\n"
    "v_mfma_f32_16x16x32_bf16 v[108:111], a[92:95], a[44:47], v[108:111]\n"
    "ds_read_b64_tr_b16 a[164:165], v149 offset:49152\n"
    "v_cvt_pk_bf16_f32 v98, v100, v101\n"
    "v_mfma_f32_16x16x32_bf16 v[16:19], a[144:147], v[120:123], v[16:19]\n"
    "v_exp_f32_e64 v102, v102\n"
    "v_mfma_f32_16x16x32_bf16 v[20:23], a[148:151], v[120:123], v[20:23]\n"
    "ds_read_b64_tr_b16 a[166:167], v149 offset:53248\n"
    "buffer_load_dwordx4 v152, s[40:43], s68 offen nt lds\n"
    "s_waitcnt lgkmcnt(4)\n"
    "v_mfma_f32_16x16x32_bf16 v[24:27], a[152:155], v[120:123], v[24:27]\n"
    "v_exp_f32_e32 v103, v103\n"
    "v_mfma_f32_16x16x32_bf16 v[28:31], a[156:159], v[120:123], v[28:31]\n"
    "ds_read_b64_tr_b16 a[168:169], v150 offset:49152\n"
    "v_cvt_pk_bf16_f32 v99, v102, v103\n"
    "v_mfma_f32_16x16x32_bf16 a[48:51], a[60:63], v[120:123], a[48:51]\n"
    "ds_read_b64_tr_b16 a[170:171], v150 offset:53248\n"
    "ds_read_b64_tr_b16 a[172:173], v151 offset:49152\n"
    "buffer_load_dwordx4 v153, s[40:43], s68 offen offset:1024 nt lds\n"
    "buffer_load_dwordx4 v152, s[40:43], s68 offen offset:2048 nt lds\n"
    "buffer_load_dwordx4 v153, s[40:43], s68 offen offset:3072 nt lds\n"
    "s_memrealtime s[78:79]\n"
    "s_add_u32 s99, s99, s62\n"
    "s_waitcnt lgkmcnt(0)\n"
    "s_sub_u32 s67, s78, s99\n"
    "s_cmp_lt_u32 s67, 0x80000000\n"
    "s_cbranch_scc1 L_rt11x\n"
    "L_rt11:\n"
    "s_sleep 1\n"
    "s_add_u32 s67, s67, 4\n"
    "s_cbranch_scc0 L_rt11\n"
    "L_rt11x:\n"
    "s_waitcnt vmcnt(8)\n"
    "s_barrier\n"
    "v_mfma_f32_16x16x32_bf16 v[112:115], a[96:99], a[0:3], v[128:131]\n"
    "v_exp_f32_e32 v104, v104\n"
    "v_mfma_f32_16x16x32_bf16 v[112:115], a[100:103], a[4:7], v[112:115]\n"
    "ds_read_b128 a[64:67], v140\n"
    "s_add_u32 m0, s63, 0x8000\n"
    "v_mfma_f32_16x16x32_bf16 v[112:115], a[104:107], a[8:11], v[112:115]\n"
    "v_exp_f32_e64 v105, v105\n"
    "v_mfma_f32_16x16x32_bf16 v[112:115], a[108:111], a[12:15], v[112:115]\n"
    "ds_read_b128 a[80:83], v140 offset:4096\n"
    "v_cvt_pk_bf16_f32 v104, v104, v105\n"
    "v_mfma_f32_16x16x32_bf16 v[32:35], a[128:131], v[96:99], v[32:35]\n"
    "v_exp_f32_e64 v106, v106\n"
    "v_mfma_f32_16x16x32_bf16 v[36:39], a[132:135], v[96:99], v[36:39]\n"
    "ds_read_b128 a[68:71], v141\n"
    "v_mfma_f32_16x16x32_bf16 v[40:43], a[136:139], v[96:99], v[40:43]\n"
    "v_exp_f32_e64 v107, v107\n"
    "v_mfma_f32_16x16x32_bf16 v[44:47], a[140:143], v[96:99], v[44:47]\n"
    "s_add_u32 s68, s63, 0x38000\n"
    "v_cvt_pk_bf16_f32 v105, v106, v107\n"
    "v_mfma_f32_16x16x32_bf16 v[116:119], a[112:115], a[0:3], v[128:131]\n"
    "v_exp_f32_e64 v108, v108\n"
    "v_mfma_f32_16x16x32_bf16 v[116:119], a[116:119], a[4:7], v[116:119]\n"
    "ds_read_b128 a[84:87], v141 offset:4096\n"
    "ds_read_b64_tr_b16 a[174:175], v151 offset:53248\n"
    "v_mfma_f32_16x16x32_bf16 v[116:119], a[120:123], a[8:11], v[116:119]\n"
    "v_exp_f32_e64 v109, v109\n"
    "v_mfma_f32_16x16x32_bf16 v[116:119], a[124:127], a[12:15], v[116:119]\n"
    "ds_read_b64_tr_b16 a[176:177], v148 offset:50176\n"
    "v_cvt_pk_bf16_f32 v106, v108, v109\n"
    "v_mfma_f32_16x16x32_bf16 v[48:51], a[144:147], v[96:99], v[48:51]\n"
    "v_exp_f32_e64 v110, v110\n"
    "v_mfma_f32_16x16x32_bf16 v[52:55], a[148:151], v[96:99], v[52:55]\n"
    "buffer_load_dwordx4 v152, s[36:39], s68 offen nt lds\n"
    "v_mfma_f32_16x16x32_bf16 v[56:59], a[152:155], v[96:99], v[56:59]\n"
    "v_exp_f32_e64 v111, v111\n"
    "v_mfma_f32_16x16x32_bf16 v[60:63], a[156:159], v[96:99], v[60:63]\n"
    "ds_read_b64_tr_b16 a[178:179], v148 offset:54272\n"
    "v_cvt_pk_bf16_f32 v107, v110, v111\n"
    "v_mfma_f32_16x16x32_bf16 a[52:55], a[60:63], v[96:99], a[52:55]\n"
    "ds_read_b64_tr_b16 a[180:181], v149 offset:50176\n"
    "v_mfma_f32_16x16x32_bf16 v[120:123], a[96:99], a[16:19], v[132:135]\n"
    "v_exp_f32_e64 v112, v112\n"
    "v_mfma_f32_16x16x32_bf16 v[120:123], a[100:103], a[20:23], v[120:123]\n"
    "ds_read_b128 a[72:75], v140 offset:1024\n"
    "buffer_load_dwordx4 v153, s[36:39], s68 offen offset:1024 nt lds\n"
    "v_mfma_f32_16x16x32_bf16 v[120:123], a[104:107], a[24:27], v[120:123]\n"
    "v_exp_f32_e64 v113, v113\n"
    "v_mfma_f32_16x16x32_bf16 v[120:123], a[108:111], a[28:31], v[120:123]\n"
    "ds_read_b128 a[88:91], v140 offset:5120\n"
    "v_cvt_pk_bf16_f32 v112, v112, v113\n"
    "v_mfma_f32_16x16x32_bf16 v[64:67], a[128:131], v[104:107], v[64:67]\n"
    "v_exp_f32_e64 v114, v114\n"
    "v_mfma_f32_16x16x32_bf16 v[68:71], a[132:135], v[104:107], v[68:71]\n"
    "ds_read_b128 a[76:79], v141 offset:1024\n"
    "v_mfma_f32_16x16x32_bf16 v[72:75], a[136:139], v[104:107], v[72:75]\n"
    "v_exp_f32_e64 v115, v115\n"
    "v_mfma_f32_16x16x32_bf16 v[76:79], a[140:143], v[104:107], v[76:79]\n"
    "ds_read_b128 a[92:95], v141 offset:5120\n"
    "v_cvt_pk_bf16_f32 v113, v114, v115\n"
    "v_mfma_f32_16x16x32_bf16 v[124:127], a[112:115], a[16:19], v[132:135]\n"
    "v_exp_f32_e64 v116, v116\n"
    "v_mfma_f32_16x16x32_bf16 v[124:127], a[116:119], a[20:23], v[124:127]\n"
    "ds_read_b64_tr_b16 a[182:183], v149 offset:54272\n"
    "v_mfma_f32_16x16x32_bf16 v[124:127], a[120:123], a[24:27], v[124:127]\n"
    "v_exp_f32_e64 v117, v117\n"
    "v_mfma_f32_16x16x32_bf16 v[124:127], a[124:127], a[28:31], v[124:127]\n"
    "ds_read_b64_tr_b16 a[184:185], v150 offset:50176\n"
    "v_cvt_pk_bf16_f32 v114, v116, v117\n"
    "v_mfma_f32_16x16x32_bf16 v[80:83], a[144:147], v[104:107], v[80:83]\n"
    "v_exp_f32_e64 v118, v118\n"
    "v_mfma_f32_16x16x32_bf16 v[84:87], a[148:151], v[104:107], v[84:87]\n"
    "ds_read_b64_tr_b16 a[186:187], v150 offset:54272\n"
    "v_mfma_f32_16x16x32_bf16 v[88:91], a[152:155], v[104:107], v[88:91]\n"
    "v_exp_f32_e64 v119, v119\n"
    "v_mfma_f32_16x16x32_bf16 v[92:95], a[156:159], v[104:107], v[92:95]\n"
    "s_waitcnt lgkmcnt(3)\n"
    "s_nop 0\n"
    "ds_read_b64_tr_b16 a[188:189], v151 offset:50176\n"
    "v_cvt_pk_bf16_f32 v115, v118, v119\n"
    "v_mfma_f32_16x16x32_bf16 a[56:59], a[60:63], v[104:107], a[56:59]\n"
    "ds_read_b64_tr_b16 a[190:191], v151 offset:54272\n"
    "v_mfma_f32_16x16x32_bf16 v[96:99], a[64:67], a[0:3], v[128:131]\n"
    "v_exp_f32_e64 v120, v120\n"
    "v_mfma_f32_16x16x32_bf16 v[96:99], a[68:71], a[4:7], v[96:99]\n"
    "ds_read_b64_tr_b16 a[128:129], v144\n"
    "buffer_load_dwordx4 v152, s[36:39], s68 offen offset:2048 nt lds\n"
    "v_mfma_f32_16x16x32_bf16 v[96:99], a[72:75], a[8:11], v[96:99]\n"
    "v_exp_f32_e64 v121, v121\n"
    "v_mfma_f32_16x16x32_bf16 v[96:99], a[76:79], a[12:15], v[96:99]\n"
    "ds_read_b64_tr_b16 a[130:131], v144 offset:4096\n"
    "v_cvt_pk_bf16_f32 v120, v120, v121\n"
    "v_mfma_f32_16x16x32_bf16 v[0:3], a[160:163], v[112:115], v[0:3]\n"
    "v_exp_f32_e64 v122, v122\n"
    "v_mfma_f32_16x16x32_bf16 v[4:7], a[164:167], v[112:115], v[4:7]\n"
    "ds_read_b64_tr_b16 a[132:133], v145\n"
    "v_mfma_f32_16x16x32_bf16 v[8:11], a[168:171], v[112:115], v[8:11]\n"
    "v_exp_f32_e64 v123, v123\n"
    "v_mfma_f32_16x16x32_bf16 v[12:15], a[172:175], v[112:115], v[12:15]\n"
    "ds_read_b64_tr_b16 a[134:135], v145 offset:4096\n"
    "v_cvt_pk_bf16_f32 v121, v122, v123\n"
    "v_mfma_f32_16x16x32_bf16 v[100:103], a[80:83], a[0:3], v[128:131]\n"
    "v_exp_f32_e64 v124, v124\n"
    "v_mfma_f32_16x16x32_bf16 v[100:103], a[84:87], a[4:7], v[100:103]\n"
    "ds_read_b64_tr_b16 a[136:137], v146\n"
    "ds_read_b64_tr_b16 a[138:139], v146 offset:4096\n"
    "v_mfma_f32_16x16x32_bf16 v[100:103], a[88:91], a[8:11], v[100:103]\n"
    "v_exp_f32_e64 v125, v125\n"
    "v_mfma_f32_16x16x32_bf16 v[100:103], a[92:95], a[12:15], v[100:103]\n"
    "ds_read_b64_tr_b16 a[140:141], v147\n"
    "v_cvt_pk_bf16_f32 v122, v124, v125\n"
    "v_mfma_f32_16x16x32_bf16 v[16:19], a[176:179], v[112:115], v[16:19]\n"
    "v_exp_f32_e32 v126, v126\n"
    "s_waitcnt lgkmcnt(4)\n"
    "v_mfma_f32_16x16x32_bf16 v[20:23], a[180:183], v[112:115], v[20:23]\n"
    "ds_read_b64_tr_b16 a[142:143], v147 offset:4096\n"
    "v_mfma_f32_16x16x32_bf16 v[24:27], a[184:187], v[112:115], v[24:27]\n"
    "v_exp_f32_e64 v127, v127\n"
    "v_mfma_f32_16x16x32_bf16 v[28:31], a[188:191], v[112:115], v[28:31]\n"
    "ds_read_b128 a[96:99], v142\n"
    "v_cvt_pk_bf16_f32 v123, v126, v127\n"
    "v_mfma_f32_16x16x32_bf16 a[48:51], a[60:63], v[112:115], a[48:51]\n"
    "ds_read_b128 a[112:115], v142 offset:4096\n"
    "v_mfma_f32_16x16x32_bf16 v[104:107], a[64:67], a[16:19], v[132:135]\n"
    "v_exp_f32_e64 v96, v96\n"
    "v_mfma_f32_16x16x32_bf16 v[104:107], a[68:71], a[20:23], v[104:107]\n"
    "ds_read_b64_tr_b16 a[144:145], v144 offset:1024\n"
    "buffer_load_dwordx4 v153, s[36:39], s68 offen offset:3072 nt lds\n"
    "v_mfma_f32_16x16x32_bf16 v[104:107], a[72:75], a[24:27], v[104:107]\n"
    "v_exp_f32_e64 v97, v97\n"
    "v_mfma_f32_16x16x32_bf16 v[104:107], a[76:79], a[28:31], v[104:107]\n"
    "ds_read_b64_tr_b16 a[146:147], v144 offset:5120\n"
    "v_cvt_pk_bf16_f32 v96, v96, v97\n"
    "v_mfma_f32_16x16x32_bf16 v[32:35], a[160:163], v[120:123], v[32:35]\n"
    "v_exp_f32_e64 v98, v98\n"
    "v_mfma_f32_16x16x32_bf16 v[36:39], a[164:167], v[120:123], v[36:39]\n"
    "ds_read_b64_tr_b16 a[148:149], v145 offset:1024\n"
    "v_mfma_f32_16x16x32_bf16 v[40:43], a[168:171], v[120:123], v[40:43]\n"
    "v_exp_f32_e64 v99, v99\n"
    "v_mfma_f32_16x16x32_bf16 v[44:47], a[172:175], v[120:123], v[44:47]\n"
    "ds_read_b64_tr_b16 a[150:151], v145 offset:5120\n"
    "v_cvt_pk_bf16_f32 v97, v98, v99\n"
    "v_mfma_f32_16x16x32_bf16 v[108:111], a[80:83], a[16:19], v[132:135]\n"
    "v_exp_f32_e64 v100, v100\n"
    "v_mfma_f32_16x16x32_bf16 v[108:111], a[84:87], a[20:23], v[108:111]\n"
    "ds_read_b64_tr_b16 a[152:153], v146 offset:1024\n"
    "ds_read_b64_tr_b16 a[154:155], v146 offset:5120\n"
    "v_mfma_f32_16x16x32_bf16 v[108:111], a[88:91], a[24:27], v[108:111]\n"
    "v_exp_f32_e64 v101, v101\n"
    "v_mfma_f32_16x16x32_bf16 v[108:111], a[92:95], a[28:31], v[108:111]\n"
    "ds_read_b64_tr_b16 a[156:157], v147 offset:1024\n"
    "v_cvt_pk_bf16_f32 v98, v100, v101\n"
    "v_mfma_f32_16x16x32_bf16 v[48:51], a[176:179], v[120:123], v[48:51]\n"
    "v_exp_f32_e64 v102, v102\n"
    "v_mfma_f32_16x16x32_bf16 v[52:55], a[180:183], v[120:123], v[52:55]\n"
    "ds_read_b64_tr_b16 a[158:159], v147 offset:5120\n"
    "v_mfma_f32_16x16x32_bf16 v[56:59], a[184:187], v[120:123], v[56:59]\n"
    "v_exp_f32_e64 v103, v103\n"
    "v_mfma_f32_16x16x32_bf16 v[60:63], a[188:191], v[120:123], v[60:63]\n"
    "s_waitcnt lgkmcnt(4)\n"
    "s_nop 0\n"
    "ds_read_b128 a[100:103], v143\n"
    "v_cvt_pk_bf16_f32 v99, v102, v103\n"
    "v_mfma_f32_16x16x32_bf16 a[52:55], a[60:63], v[120:123], a[52:55]\n"
    "ds_read_b128 a[116:119], v143 offset:4096\n"
    "v_mfma_f32_16x16x32_bf16 v[112:115], a[64:67], a[32:35], v[136:139]\n"
    "v_exp_f32_e64 v104, v104\n"
    "v_mfma_f32_16x16x32_bf16 v[112:115], a[68:71], a[36:39], v[112:115]\n"
    "ds_read_b128 a[104:107], v142 offset:1024\n"
    "s_add_u32 m0, s63, 0x18000\n"
    "v_mfma_f32_16x16x32_bf16 v[112:115], a[72:75], a[40:43], v[112:115]\n"
    "v_exp_f32_e64 v105, v105\n"
    "v_mfma_f32_16x16x32_bf16 v[112:115], a[76:79], a[44:47], v[112:115]\n"
    "ds_read_b128 a[120:123], v142 offset:5120\n"
    "v_cvt_pk_bf16_f32 v104, v104, v105\n"
    "v_mfma_f32_16x16x32_bf16 v[0:3], a[128:131], v[96:99], v[0:3]\n"
    "v_exp_f32_e64 v106, v106\n"
    "v_mfma_f32_16x16x32_bf16 v[4:7], a[132:135], v[96:99], v[4:7]\n"
    "ds_read_b128 a[108:111], v143 offset:1024\n"
    "ds_read_b128 a[124:127], v143 offset:5120\n"
    "v_mfma_f32_16x16x32_bf16 v[8:11], a[136:139], v[96:99], v[8:11]\n"
    "v_exp_f32_e64 v107, v107\n"
    "v_mfma_f32_16x16x32_bf16 v[12:15], a[140:143], v[96:99], v[12:15]\n"
    "s_add_u32 s68, s63, 0x38000\n"
    "v_cvt_pk_bf16_f32 v105, v106, v107\n"
    "v_mfma_f32_16x16x32_bf16 v[116:119], a[80:83], a[32:35], v[136:139]\n"
    "v_exp_f32_e64 v108, v108\n"
    "v_mfma_f32_16x16x32_bf16 v[116:119], a[84:87], a[36:39], v[116:119]\n"
    "ds_read_b64_tr_b16 a[160:161], v148\n"
    "ds_read_b64_tr_b16 a[162:163], v148 offset:4096\n"
    "v_mfma_f32_16x16x32_bf16 v[116:119], a[88:91], a[40:43], v[116:119]\n"
    "v_exp_f32_e64 v109, v109\n"
    "v_mfma_f32_16x16x32_bf16 v[116:119], a[92:95], a[44:47], v[116:119]\n"
    "ds_read_b64_tr_b16 a[164:165], v149\n"
    "v_cvt_pk_bf16_f32 v106, v108, v109\n"
    "v_mfma_f32_16x16x32_bf16 v[16:19], a[144:147], v[96:99], v[16:19]\n"
    "v_exp_f32_e64 v110, v110\n"
    "v_mfma_f32_16x16x32_bf16 v[20:23], a[148:151], v[96:99], v[20:23]\n"
    "ds_read_b64_tr_b16 a[166:167], v149 offset:4096\n"
    "buffer_load_dwordx4 v152, s[40:43], s68 offen nt lds\n"
    "s_waitcnt lgkmcnt(4)\n"
    "v_mfma_f32_16x16x32_bf16 v[24:27], a[152:155], v[96:99], v[24:27]\n"
    "v_exp_f32_e32 v111, v111\n"
    "v_mfma_f32_16x16x32_bf16 v[28:31], a[156:159], v[96:99], v[28:31]\n"
    "ds_read_b64_tr_b16 a[168:169], v150\n"
    "v_cvt_pk_bf16_f32 v107, v110, v111\n"
    "v_mfma_f32_16x16x32_bf16 a[48:51], a[60:63], v[96:99], a[48:51]\n"
    "ds_read_b64_tr_b16 a[170:171], v150 offset:4096\n"
    "ds_read_b64_tr_b16 a[172:173], v151\n"
    "buffer_load_dwordx4 v153, s[40:43], s68 offen offset:1024 nt lds\n"
    "buffer_load_dwordx4 v152, s[40:43], s68 offen offset:2048 nt lds\n"
    "buffer_load_dwordx4 v153, s[40:43], s68 offen offset:3072 nt lds\n"
    "s_memrealtime s[78:79]\n"
    "s_add_u32 s99, s99, s62\n"
    "s_waitcnt lgkmcnt(0)\n"
    "s_sub_u32 s67, s78, s99\n"
    "s_cmp_lt_u32 s67, 0x80000000\n"
    "s_cbranch_scc1 L_rt12x\n"
    "L_rt12:\n"
    "s_sleep 1\n"
    "s_add_u32 s67, s67, 4\n"
    "s_cbranch_scc0 L_rt12\n"
    "L_rt12x:\n"
    "s_waitcnt vmcnt(8)\n"
    "s_barrier\n"
    "v_mfma_f32_16x16x32_bf16 v[120:123], a[96:99], a[0:3], v[128:131]\n"
    "v_exp_f32_e32 v112, v112\n"
    "v_mfma_f32_16x16x32_bf16 v[120:123], a[100:103], a[4:7], v[120:123]\n"
    "ds_read_b128 a[64:67], v140 offset:16384\n"
    "s_add_u32 m0, s63, 0xc000\n"
    "v_mfma_f32_16x16x32_bf16 v[120:123], a[104:107], a[8:11], v[120:123]\n"
    "v_exp_f32_e64 v113, v113\n"
    "v_mfma_f32_16x16x32_bf16 v[120:123], a[108:111], a[12:15], v[120:123]\n"
    "ds_read_b128 a[80:83], v140 offset:20480\n"
    "v_cvt_pk_bf16_f32 v112, v112, v113\n"
    "v_mfma_f32_16x16x32_bf16 v[32:35], a[128:131], v[104:107], v[32:35]\n"
    "v_exp_f32_e64 v114, v114\n"
    "v_mfma_f32_16x16x32_bf16 v[36:39], a[132:135], v[104:107], v[36:39]\n"
    "ds_read_b128 a[68:71], v141 offset:16384\n"
    "v_mfma_f32_16x16x32_bf16 v[40:43], a[136:139], v[104:107], v[40:43]\n"
    "v_exp_f32_e64 v115, v115\n"
    "v_mfma_f32_16x16x32_bf16 v[44:47], a[140:143], v[104:107], v[44:47]\n"
    "s_add_u32 s68, s63, 0x3c000\n"
    "v_cvt_pk_bf16_f32 v113, v114, v115\n"
    "v_mfma_f32_16x16x32_bf16 v[124:127], a[112:115], a[0:3], v[128:131]\n"
    "v_exp_f32_e64 v116, v116\n"
    "v_mfma_f32_16x16x32_bf16 v[124:127], a[116:119], a[4:7], v[124:127]\n"
    "ds_read_b128 a[84:87], v141 offset:20480\n"
    "ds_read_b64_tr_b16 a[174:175], v151 offset:4096\n"
    "v_mfma_f32_16x16x32_bf16 v[124:127], a[120:123], a[8:11], v[124:127]\n"
    "v_exp_f32_e64 v117, v117\n"
    "v_mfma_f32_16x16x32_bf16 v[124:127], a[124:127], a[12:15], v[124:127]\n"
    "ds_read_b64_tr_b16 a[176:177], v148 offset:1024\n"
    "v_cvt_pk_bf16_f32 v114, v116, v117\n"
    "v_mfma_f32_16x16x32_bf16 v[48:51], a[144:147], v[104:107], v[48:51]\n"
    "v_exp_f32_e64 v118, v118\n"
    "v_mfma_f32_16x16x32_bf16 v[52:55], a[148:151], v[104:107], v[52:55]\n"
    "buffer_load_dwordx4 v152, s[36:39], s68 offen nt lds\n"
    "v_mfma_f32_16x16x32_bf16 v[56:59], a[152:155], v[104:107], v[56:59]\n"
    "v_exp_f32_e64 v119, v119\n"
    "v_mfma_f32_16x16x32_bf16 v[60:63], a[156:159], v[104:107], v[60:63]\n"
    "ds_read_b64_tr_b16 a[178:179], v148 offset:5120\n"
    "v_cvt_pk_bf16_f32 v115, v118, v119\n"
    "v_mfma_f32_16x16x32_bf16 a[52:55], a[60:63], v[104:107], a[52:55]\n"
    "ds_read_b64_tr_b16 a[180:181], v149 offset:1024\n"
    "v_mfma_f32_16x16x32_bf16 v[96:99], a[96:99], a[16:19], v[132:135]\n"
    "v_exp_f32_e64 v120, v120\n"
    "v_mfma_f32_16x16x32_bf16 v[96:99], a[100:103], a[20:23], v[96:99]\n"
    "ds_read_b128 a[72:75], v140 offset:17408\n"
    "buffer_load_dwordx4 v153, s[36:39], s68 offen offset:1024 nt lds\n"
    "v_mfma_f32_16x16x32_bf16 v[96:99], a[104:107], a[24:27], v[96:99]\n"
    "v_exp_f32_e64 v121, v121\n"
    "v_mfma_f32_16x16x32_bf16 v[96:99], a[108:111], a[28:31], v[96:99]\n"
    "ds_read_b128 a[88:91], v140 offset:21504\n"
    "v_cvt_pk_bf16_f32 v120, v120, v121\n"
    "v_mfma_f32_16x16x32_bf16 v[64:67], a[128:131], v[112:115], v[64:67]\n"
    "v_exp_f32_e64 v122, v122\n"
    "v_mfma_f32_16x16x32_bf16 v[68:71], a[132:135], v[112:115], v[68:71]\n"
    "ds_read_b128 a[76:79], v141 offset:17408\n"
    "v_mfma_f32_16x16x32_bf16 v[72:75], a[136:139], v[112:115], v[72:75]\n"
    "v_exp_f32_e64 v123, v123\n"
    "v_mfma_f32_16x16x32_bf16 v[76:79], a[140:143], v[112:115], v[76:79]\n"
    "ds_read_b128 a[92:95], v141 offset:21504\n"
    "v_cvt_pk_bf16_f32 v121, v122, v123\n"
    "v_mfma_f32_16x16x32_bf16 v[100:103], a[112:115], a[16:19], v[132:135]\n"
    "v_exp_f32_e64 v124, v124\n"
    "v_mfma_f32_16x16x32_bf16 v[100:103], a[116:119], a[20:23], v[100:103]\n"
    "ds_read_b64_tr_b16 a[182:183], v149 offset:5120\n"
    "v_mfma_f32_16x16x32_bf16 v[100:103], a[120:123], a[24:27], v[100:103]\n"
    "v_exp_f32_e64 v125, v125\n"
    "v_mfma_f32_16x16x32_bf16 v[100:103], a[124:127], a[28:31], v[100:103]\n"
    "ds_read_b64_tr_b16 a[184:185], v150 offset:1024\n"
    "v_cvt_pk_bf16_f32 v122, v124, v125\n"
    "v_mfma_f32_16x16x32_bf16 v[80:83], a[144:147], v[112:115], v[80:83]\n"
    "v_exp_f32_e64 v126, v126\n"
    "v_mfma_f32_16x16x32_bf16 v[84:87], a[148:151], v[112:115], v[84:87]\n"
    "ds_read_b64_tr_b16 a[186:187], v150 offset:5120\n"
    "v_mfma_f32_16x16x32_bf16 v[88:91], a[152:155], v[112:115], v[88:91]\n"
    "v_exp_f32_e64 v127, v127\n"
    "v_mfma_f32_16x16x32_bf16 v[92:95], a[156:159], v[112:115], v[92:95]\n"
    "s_waitcnt lgkmcnt(3)\n"
    "s_nop 0\n"
    "ds_read_b64_tr_b16 a[188:189], v151 offset:1024\n"
    "v_cvt_pk_bf16_f32 v123, v126, v127\n"
    "v_mfma_f32_16x16x32_bf16 a[56:59], a[60:63], v[112:115], a[56:59]\n"
    "ds_read_b64_tr_b16 a[190:191], v151 offset:5120\n"
    "v_mfma_f32_16x16x32_bf16 v[104:107], a[64:67], a[0:3], v[128:131]\n"
    "v_exp_f32_e64 v96, v96\n"
    "v_mfma_f32_16x16x32_bf16 v[104:107], a[68:71], a[4:7], v[104:107]\n"
    "ds_read_b64_tr_b16 a[128:129], v144 offset:16384\n"
    "buffer_load_dwordx4 v152, s[36:39], s68 offen offset:2048 nt lds\n"
    "v_mfma_f32_16x16x32_bf16 v[104:107], a[72:75], a[8:11], v[104:107]\n"
    "v_exp_f32_e64 v97, v97\n"
    "v_mfma_f32_16x16x32_bf16 v[104:107], a[76:79], a[12:15], v[104:107]\n"
    "ds_read_b64_tr_b16 a[130:131], v144 offset:20480\n"
    "v_cvt_pk_bf16_f32 v96, v96, v97\n"
    "v_mfma_f32_16x16x32_bf16 v[0:3], a[160:163], v[120:123], v[0:3]\n"
    "v_exp_f32_e64 v98, v98\n"
    "v_mfma_f32_16x16x32_bf16 v[4:7], a[164:167], v[120:123], v[4:7]\n"
    "ds_read_b64_tr_b16 a[132:133], v145 offset:16384\n"
    "v_mfma_f32_16x16x32_bf16 v[8:11], a[168:171], v[120:123], v[8:11]\n"
    "v_exp_f32_e64 v99, v99\n"
    "v_mfma_f32_16x16x32_bf16 v[12:15], a[172:175], v[120:123], v[12:15]\n"
    "ds_read_b64_tr_b16 a[134:135], v145 offset:20480\n"
    "v_cvt_pk_bf16_f32 v97, v98, v99\n"
    "v_mfma_f32_16x16x32_bf16 v[108:111], a[80:83], a[0:3], v[128:131]\n"
    "v_exp_f32_e64 v100, v100\n"
    "v_mfma_f32_16x16x32_bf16 v[108:111], a[84:87], a[4:7], v[108:111]\n"
    "ds_read_b64_tr_b16 a[136:137], v146 offset:16384\n"
    "ds_read_b64_tr_b16 a[138:139], v146 offset:20480\n"
    "v_mfma_f32_16x16x32_bf16 v[108:111], a[88:91], a[8:11], v[108:111]\n"
    "v_exp_f32_e64 v101, v101\n"
    "v_mfma_f32_16x16x32_bf16 v[108:111], a[92:95], a[12:15], v[108:111]\n"
    "ds_read_b64_tr_b16 a[140:141], v147 offset:16384\n"
    "v_cvt_pk_bf16_f32 v98, v100, v101\n"
    "v_mfma_f32_16x16x32_bf16 v[16:19], a[176:179], v[120:123], v[16:19]\n"
    "v_exp_f32_e32 v102, v102\n"
    "s_waitcnt lgkmcnt(4)\n"
    "v_mfma_f32_16x16x32_bf16 v[20:23], a[180:183], v[120:123], v[20:23]\n"
    "ds_read_b64_tr_b16 a[142:143], v147 offset:20480\n"
    "v_mfma_f32_16x16x32_bf16 v[24:27], a[184:187], v[120:123], v[24:27]\n"
    "v_exp_f32_e64 v103, v103\n"
    "v_mfma_f32_16x16x32_bf16 v[28:31], a[188:191], v[120:123], v[28:31]\n"
    "ds_read_b128 a[96:99], v142 offset:16384\n"
    "v_cvt_pk_bf16_f32 v99, v102, v103\n"
    "v_mfma_f32_16x16x32_bf16 a[48:51], a[60:63], v[120:123], a[48:51]\n"
    "ds_read_b128 a[112:115], v142 offset:20480\n"
    "v_mfma_f32_16x16x32_bf16 v[112:115], a[64:67], a[16:19], v[132:135]\n"
    "v_exp_f32_e64 v104, v104\n"
    "v_mfma_f32_16x16x32_bf16 v[112:115], a[68:71], a[20:23], v[112:115]\n"
    "ds_read_b64_tr_b16 a[144:145], v144 offset:17408\n"
    "buffer_load_dwordx4 v153, s[36:39], s68 offen offset:3072 nt lds\n"
    "v_mfma_f32_16x16x32_bf16 v[112:115], a[72:75], a[24:27], v[112:115]\n"
    "v_exp_f32_e64 v105, v105\n"
    "v_mfma_f32_16x16x32_bf16 v[112:115], a[76:79], a[28:31], v[112:115]\n"
    "ds_read_b64_tr_b16 a[146:147], v144 offset:21504\n"
    "v_cvt_pk_bf16_f32 v104, v104, v105\n"
    "v_mfma_f32_16x16x32_bf16 v[32:35], a[160:163], v[96:99], v[32:35]\n"
    "v_exp_f32_e64 v106, v106\n"
    "v_mfma_f32_16x16x32_bf16 v[36:39], a[164:167], v[96:99], v[36:39]\n"
    "ds_read_b64_tr_b16 a[148:149], v145 offset:17408\n"
    "v_mfma_f32_16x16x32_bf16 v[40:43], a[168:171], v[96:99], v[40:43]\n"
    "v_exp_f32_e64 v107, v107\n"
    "v_mfma_f32_16x16x32_bf16 v[44:47], a[172:175], v[96:99], v[44:47]\n"
    "ds_read_b64_tr_b16 a[150:151], v145 offset:21504\n"
    "v_cvt_pk_bf16_f32 v105, v106, v107\n"
    "v_mfma_f32_16x16x32_bf16 v[116:119], a[80:83], a[16:19], v[132:135]\n"
    "v_exp_f32_e64 v108, v108\n"
    "v_mfma_f32_16x16x32_bf16 v[116:119], a[84:87], a[20:23], v[116:119]\n"
    "ds_read_b64_tr_b16 a[152:153], v146 offset:17408\n"
    "ds_read_b64_tr_b16 a[154:155], v146 offset:21504\n"
    "v_mfma_f32_16x16x32_bf16 v[116:119], a[88:91], a[24:27], v[116:119]\n"
    "v_exp_f32_e64 v109, v109\n"
    "v_mfma_f32_16x16x32_bf16 v[116:119], a[92:95], a[28:31], v[116:119]\n"
    "ds_read_b64_tr_b16 a[156:157], v147 offset:17408\n"
    "v_cvt_pk_bf16_f32 v106, v108, v109\n"
    "v_mfma_f32_16x16x32_bf16 v[48:51], a[176:179], v[96:99], v[48:51]\n"
    "v_exp_f32_e64 v110, v110\n"
    "v_mfma_f32_16x16x32_bf16 v[52:55], a[180:183], v[96:99], v[52:55]\n"
    "ds_read_b64_tr_b16 a[158:159], v147 offset:21504\n"
    "v_mfma_f32_16x16x32_bf16 v[56:59], a[184:187], v[96:99], v[56:59]\n"
    "v_exp_f32_e64 v111, v111\n"
    "v_mfma_f32_16x16x32_bf16 v[60:63], a[188:191], v[96:99], v[60:63]\n"
    "s_waitcnt lgkmcnt(4)\n"
    "s_nop 0\n"
    "ds_read_b128 a[100:103], v143 offset:16384\n"
    "v_cvt_pk_bf16_f32 v107, v110, v111\n"
    "v_mfma_f32_16x16x32_bf16 a[52:55], a[60:63], v[96:99], a[52:55]\n"
    "ds_read_b128 a[116:119], v143 offset:20480\n"
    "v_mfma_f32_16x16x32_bf16 v[120:123], a[64:67], a[32:35], v[136:139]\n"
    "v_exp_f32_e64 v112, v112\n"
    "v_mfma_f32_16x16x32_bf16 v[120:123], a[68:71], a[36:39], v[120:123]\n"
    "ds_read_b128 a[104:107], v142 offset:17408\n"
    "s_add_u32 m0, s63, 0x1c000\n"
    "v_mfma_f32_16x16x32_bf16 v[120:123], a[72:75], a[40:43], v[120:123]\n"
    "ds_read_b128 a[120:123], v142 offset:21504\n"
    "v_exp_f32_e64 v113, v113\n"
    "v_mfma_f32_16x16x32_bf16 v[120:123], a[76:79], a[44:47], v[120:123]\n"
    "ds_read_b128 a[108:111], v143 offset:17408\n"
    "v_cvt_pk_bf16_f32 v112, v112, v113\n"
    "v_mfma_f32_16x16x32_bf16 v[0:3], a[128:131], v[104:107], v[0:3]\n"
    "v_exp_f32_e64 v114, v114\n"
    "v_mfma_f32_16x16x32_bf16 v[4:7], a[132:135], v[104:107], v[4:7]\n"
    "s_add_u32 s68, s63, 0x3c000\n"
    "ds_read_b128 a[124:127], v143 offset:21504\n"
    "v_mfma_f32_16x16x32_bf16 v[8:11], a[136:139], v[104:107], v[8:11]\n"
    "v_exp_f32_e64 v115, v115\n"
    "v_mfma_f32_16x16x32_bf16 v[12:15], a[140:143], v[104:107], v[12:15]\n"
    "ds_read_b64_tr_b16 a[160:161], v148 offset:16384\n"
    "v_cvt_pk_bf16_f32 v113, v114, v115\n"
    "v_mfma_f32_16x16x32_bf16 v[124:127], a[80:83], a[32:35], v[136:139]\n"
    "v_exp_f32_e64 v116, v116\n"
    "v_mfma_f32_16x16x32_bf16 v[124:127], a[84:87], a[36:39], v[124:127]\n"
    "ds_read_b64_tr_b16 a[162:163], v148 offset:20480\n"
    "buffer_load_dwordx4 v152, s[40:43], s68 offen nt lds\n"
    "v_mfma_f32_16x16x32_bf16 v[124:127], a[88:91], a[40:43], v[124:127]\n"
    "v_exp_f32_e64 v117, v117\n"
    "v_mfma_f32_16x16x32_bf16 v[124:127], a[92:95], a[44:47], v[124:127]\n"
    "ds_read_b64_tr_b16 a[164:165], v149 offset:16384\n"
    "v_cvt_pk_bf16_f32 v114, v116, v117\n"
    "v_mfma_f32_16x16x32_bf16 v[16:19], a[144:147], v[104:107], v[16:19]\n"
    "v_exp_f32_e64 v118, v118\n"
    "v_mfma_f32_16x16x32_bf16 v[20:23], a[148:151], v[104:107], v[20:23]\n"
    "ds_read_b64_tr_b16 a[166:167], v149 offset:20480\n"
    "ds_read_b64_tr_b16 a[168:169], v150 offset:16384\n"
    "s_waitcnt lgkmcnt(5)\n"
    "v_mfma_f32_16x16x32_bf16 v[24:27], a[152:155], v[104:107], v[24:27]\n"
    "v_exp_f32_e32 v119, v119\n"
    "v_mfma_f32_16x16x32_bf16 v[28:31], a[156:159], v[104:107], v[28:31]\n"
    "buffer_load_dwordx4 v153, s[40:43], s68 offen offset:1024 nt lds\n"
    "v_cvt_pk_bf16_f32 v115, v118, v119\n"
    "v_mfma_f32_16x16x32_bf16 a[48:51], a[60:63], v[104:107], a[48:51]\n"
    "ds_read_b64_tr_b16 a[170:171], v150 offset:20480\n"
    "ds_read_b64_tr_b16 a[172:173], v151 offset:16384\n"
    "v_mfma_f32_16x16x32_bf16 v[96:99], a[96:99], a[0:3], v[128:131]\n"
    "v_exp_f32_e64 v120, v120\n"
    "v_mfma_f32_16x16x32_bf16 v[96:99], a[100:103], a[4:7], v[96:99]\n"
    "ds_read_b64_tr_b16 a[174:175], v151 offset:20480\n"
    "buffer_load_dwordx4 v152, s[40:43], s68 offen offset:2048 nt lds\n"
    "v_mfma_f32_16x16x32_bf16 v[96:99], a[104:107], a[8:11], v[96:99]\n"
    "v_exp_f32_e64 v121, v121\n"
    "v_mfma_f32_16x16x32_bf16 v[96:99], a[108:111], a[12:15], v[96:99]\n"
    "ds_read_b64_tr_b16 a[176:177], v148 offset:17408\n"
    "v_cvt_pk_bf16_f32 v120, v120, v121\n"
    "v_mfma_f32_16x16x32_bf16 v[32:35], a[128:131], v[112:115], v[32:35]\n"
    "v_exp_f32_e64 v122, v122\n"
    "v_mfma_f32_16x16x32_bf16 v[36:39], a[132:135], v[112:115], v[36:39]\n"
    "ds_read_b64_tr_b16 a[178:179], v148 offset:21504\n"
    "v_mfma_f32_16x16x32_bf16 v[40:43], a[136:139], v[112:115], v[40:43]\n"
    "v_exp_f32_e64 v123, v123\n"
    "v_mfma_f32_16x16x32_bf16 v[44:47], a[140:143], v[112:115], v[44:47]\n"
    "ds_read_b64_tr_b16 a[180:181], v149 offset:17408\n"
    "v_cvt_pk_bf16_f32 v121, v122, v123\n"
    "v_mfma_f32_16x16x32_bf16 v[100:103], a[112:115], a[0:3], v[128:131]\n"
    "v_exp_f32_e64 v124, v124\n"
    "v_mfma_f32_16x16x32_bf16 v[100:103], a[116:119], a[4:7], v[100:103]\n"
    "ds_read_b64_tr_b16 a[182:183], v149 offset:21504\n"
    "buffer_load_dwordx4 v153, s[40:43], s68 offen offset:3072 nt lds\n"
    "v_mfma_f32_16x16x32_bf16 v[100:103], a[120:123], a[8:11], v[100:103]\n"
    "v_exp_f32_e64 v125, v125\n"
    "v_mfma_f32_16x16x32_bf16 v[100:103], a[124:127], a[12:15], v[100:103]\n"
    "ds_read_b64_tr_b16 a[184:185], v150 offset:17408\n"
    "v_cvt_pk_bf16_f32 v122, v124, v125\n"
    "v_mfma_f32_16x16x32_bf16 v[48:51], a[144:147], v[112:115], v[48:51]\n"
    "v_exp_f32_e64 v126, v126\n"
    "v_mfma_f32_16x16x32_bf16 v[52:55], a[148:151], v[112:115], v[52:55]\n"
    "ds_read_b64_tr_b16 a[186:187], v150 offset:21504\n"
    "v_mfma_f32_16x16x32_bf16 v[56:59], a[152:155], v[112:115], v[56:59]\n"
    "v_exp_f32_e64 v127, v127\n"
    "v_mfma_f32_16x16x32_bf16 v[60:63], a[156:159], v[112:115], v[60:63]\n"
    "ds_read_b64_tr_b16 a[188:189], v151 offset:17408\n"
    "v_cvt_pk_bf16_f32 v123, v126, v127\n"
    "v_mfma_f32_16x16x32_bf16 a[52:55], a[60:63], v[112:115], a[52:55]\n"
    "s_waitcnt lgkmcnt(4)\n"
    "ds_read_b64_tr_b16 a[190:191], v151 offset:21504\n"
    "s_memrealtime s[78:79]\n"
    "s_mul_i32 s67, s62, 19\n"
    "s_lshr_b32 s67, s67, 4\n"
    "s_add_u32 s99, s99, s67\n"
    "s_waitcnt lgkmcnt(0)\n"
    "s_sub_u32 s67, s78, s99\n"
    "s_cmp_lt_u32 s67, 0x80000000\n"
    "s_cbranch_scc1 L_rt20x\n"
    "L_rt20:\n"
    "s_sleep 1\n"
    "s_add_u32 s67, s67, 4\n"
    "s_cbranch_scc0 L_rt20\n"
    "L_rt20x:\n"
    "s_waitcnt vmcnt(12)\n"
    "s_barrier\n"
    "v_mfma_f32_16x16x32_bf16 v[104:107], a[96:99], a[16:19], v[132:135]\n"
    "v_exp_f32_e64 v96, v96\n"
    "v_mfma_f32_16x16x32_bf16 v[104:107], a[100:103], a[20:23], v[104:107]\n"
    "ds_read_b128 a[64:67], v140 offset:32768\n"
    "s_add_u32 m0, s63, 0x20000\n"
    "v_mfma_f32_16x16x32_bf16 v[104:107], a[104:107], a[24:27], v[104:107]\n"
    "v_exp_f32_e64 v97, v97\n"
    "v_mfma_f32_16x16x32_bf16 v[104:107], a[108:111], a[28:31], v[104:107]\n"
    "ds_read_b128 a[68:71], v141 offset:32768\n"
    "v_cvt_pk_bf16_f32 v96, v96, v97\n"
    "v_mfma_f32_16x16x32_bf16 v[64:67], a[128:131], v[120:123], v[64:67]\n"
    "v_exp_f32_e64 v98, v98\n"
    "v_mfma_f32_16x16x32_bf16 v[68:71], a[132:135], v[120:123], v[68:71]\n"
    "ds_read_b128 a[72:75], v140 offset:33792\n"
    "v_mfma_f32_16x16x32_bf16 v[72:75], a[136:139], v[120:123], v[72:75]\n"
    "v_exp_f32_e64 v99, v99\n"
    "v_mfma_f32_16x16x32_bf16 v[76:79], a[140:143], v[120:123], v[76:79]\n"
    "s_mov_b32 s70, s61\n"
    "v_cvt_pk_bf16_f32 v97, v98, v99\n"
    "v_mfma_f32_16x16x32_bf16 v[108:111], a[112:115], a[16:19], v[132:135]\n"
    "v_exp_f32_e32 v100, v100\n"
    "v_mfma_f32_16x16x32_bf16 v[108:111], a[116:119], a[20:23], v[108:111]\n"
    "ds_read_b128 a[76:79], v141 offset:33792\n"
    "ds_read_b128 a[80:83], v140 offset:36864\n"
    "v_mfma_f32_16x16x32_bf16 v[108:111], a[120:123], a[24:27], v[108:111]\n"
    "v_exp_f32_e64 v101, v101\n"
    "v_mfma_f32_16x16x32_bf16 v[108:111], a[124:127], a[28:31], v[108:111]\n"
    "ds_read_b128 a[84:87], v141 offset:36864\n"
    "v_cvt_pk_bf16_f32 v98, v100, v101\n"
    "v_mfma_f32_16x16x32_bf16 v[80:83], a[144:147], v[120:123], v[80:83]\n"
    "v_exp_f32_e64 v102, v102\n"
    "v_mfma_f32_16x16x32_bf16 v[84:87], a[148:151], v[120:123], v[84:87]\n"
    "buffer_load_dwordx4 v240, s[48:51], s70 offen lds\n"
    "v_mfma_f32_16x16x32_bf16 v[88:91], a[152:155], v[120:123], v[88:91]\n"
    "v_exp_f32_e64 v103, v103\n"
    "v_mfma_f32_16x16x32_bf16 v[92:95], a[156:159], v[120:123], v[92:95]\n"
    "ds_read_b128 a[88:91], v140 offset:37888\n"
    "v_cvt_pk_bf16_f32 v99, v102, v103\n"
    "v_mfma_f32_16x16x32_bf16 a[56:59], a[60:63], v[120:123], a[56:59]\n"
    "ds_read_b128 a[92:95], v141 offset:37888\n"
    "s_waitcnt vmcnt(9)\n"
    "s_barrier\n"
    "s_waitcnt lgkmcnt(4)\n"
    "v_mfma_f32_16x16x32_bf16 v[112:115], a[64:67], a[0:3], v[128:131]\n"
    "v_exp_f32_e32 v104, v104\n"
    "v_mfma_f32_16x16x32_bf16 v[112:115], a[68:71], a[4:7], v[112:115]\n"
    "ds_read_b64_tr_b16 a[128:129], v144 offset:32768\n"
    "buffer_load_dwordx4 v241, s[48:51], s70 offen offset:1024 lds\n"
    "v_mfma_f32_16x16x32_bf16 v[112:115], a[72:75], a[8:11], v[112:115]\n"
    "v_exp_f32_e64 v105, v105\n"
    "v_mfma_f32_16x16x32_bf16 v[112:115], a[76:79], a[12:15], v[112:115]\n"
    "ds_read_b64_tr_b16 a[130:131], v144 offset:36864\n"
    "v_cvt_pk_bf16_f32 v104, v104, v105\n"
    "v_mfma_f32_16x16x32_bf16 v[0:3], a[160:163], v[96:99], v[0:3]\n"
    "v_exp_f32_e64 v106, v106\n"
    "v_mfma_f32_16x16x32_bf16 v[4:7], a[164:167], v[96:99], v[4:7]\n"
    "ds_read_b64_tr_b16 a[132:133], v145 offset:32768\n"
    "v_mfma_f32_16x16x32_bf16 v[8:11], a[168:171], v[96:99], v[8:11]\n"
    "v_exp_f32_e64 v107, v107\n"
    "v_mfma_f32_16x16x32_bf16 v[12:15], a[172:175], v[96:99], v[12:15]\n"
    "ds_read_b64_tr_b16 a[134:135], v145 offset:36864\n"
    "v_cvt_pk_bf16_f32 v105, v106, v107\n"
    "s_waitcnt lgkmcnt(4)\n"
    "v_mfma_f32_16x16x32_bf16 v[116:119], a[80:83], a[0:3], v[128:131]\n"
    "v_exp_f32_e32 v108, v108\n"
    "v_mfma_f32_16x16x32_bf16 v[116:119], a[84:87], a[4:7], v[116:119]\n"
    "ds_read_b64_tr_b16 a[136:137], v146 offset:32768\n"
    "ds_read_b64_tr_b16 a[138:139], v146 offset:36864\n"
    "v_mfma_f32_16x16x32_bf16 v[116:119], a[88:91], a[8:11], v[116:119]\n"
    "v_exp_f32_e64 v109, v109\n"
    "v_mfma_f32_16x16x32_bf16 v[116:119], a[92:95], a[12:15], v[116:119]\n"
    "ds_read_b64_tr_b16 a[140:141], v147 offset:32768\n"
    "v_cvt_pk_bf16_f32 v106, v108, v109\n"
    "v_mfma_f32_16x16x32_bf16 v[16:19], a[176:179], v[96:99], v[16:19]\n"
    "v_exp_f32_e64 v110, v110\n"
    "v_mfma_f32_16x16x32_bf16 v[20:23], a[180:183], v[96:99], v[20:23]\n"
    "ds_read_b64_tr_b16 a[142:143], v147 offset:36864\n"
    "v_mfma_f32_16x16x32_bf16 v[24:27], a[184:187], v[96:99], v[24:27]\n"
    "v_exp_f32_e64 v111, v111\n"
    "v_mfma_f32_16x16x32_bf16 v[28:31], a[188:191], v[96:99], v[28:31]\n"
    "ds_read_b128 a[96:99], v142 offset:32768\n"
    "v_cvt_pk_bf16_f32 v107, v110, v111\n"
    "v_mfma_f32_16x16x32_bf16 a[48:51], a[60:63], v[96:99], a[48:51]\n"
    "ds_read_b128 a[112:115], v142 offset:36864\n"
    "v_mfma_f32_16x16x32_bf16 v[120:123], a[64:67], a[16:19], v[132:135]\n"
    "v_exp_f32_e64 v112, v112\n"
    "v_mfma_f32_16x16x32_bf16 v[120:123], a[68:71], a[20:23], v[120:123]\n"
    "ds_read_b64_tr_b16 a[144:145], v144 offset:33792\n"
    "buffer_load_dwordx4 v242, s[48:51], s70 offen offset:2048 lds\n"
    "v_mfma_f32_16x16x32_bf16 v[120:123], a[72:75], a[24:27], v[120:123]\n"
    "v_exp_f32_e64 v113, v113\n"
    "v_mfma_f32_16x16x32_bf16 v[120:123], a[76:79], a[28:31], v[120:123]\n"
    "ds_read_b64_tr_b16 a[146:147], v144 offset:37888\n"
    "v_cvt_pk_bf16_f32 v112, v112, v113\n"
    "v_mfma_f32_16x16x32_bf16 v[32:35], a[160:163], v[104:107], v[32:35]\n"
    "v_exp_f32_e64 v114, v114\n"
    "v_mfma_f32_16x16x32_bf16 v[36:39], a[164:167], v[104:107], v[36:39]\n"
    "ds_read_b64_tr_b16 a[148:149], v145 offset:33792\n"
    "v_mfma_f32_16x16x32_bf16 v[40:43], a[168:171], v[104:107], v[40:43]\n"
    "v_exp_f32_e64 v115, v115\n"
    "v_mfma_f32_16x16x32_bf16 v[44:47], a[172:175], v[104:107], v[44:47]\n"
    "ds_read_b64_tr_b16 a[150:151], v145 offset:37888\n"
    "v_cvt_pk_bf16_f32 v113, v114, v115\n"
    "v_mfma_f32_16x16x32_bf16 v[124:127], a[80:83], a[16:19], v[132:135]\n"
    "v_exp_f32_e64 v116, v116\n"
    "v_mfma_f32_16x16x32_bf16 v[124:127], a[84:87], a[20:23], v[124:127]\n"
    "ds_read_b64_tr_b16 a[152:153], v146 offset:33792\n"
    "s_waitcnt lgkmcnt(4)\n"
    "ds_read_b64_tr_b16 a[154:155], v146 offset:37888\n"
    "v_mfma_f32_16x16x32_bf16 v[124:127], a[88:91], a[24:27], v[124:127]\n"
    "v_exp_f32_e32 v117, v117\n"
    "v_mfma_f32_16x16x32_bf16 v[124:127], a[92:95], a[28:31], v[124:127]\n"
    "ds_read_b64_tr_b16 a[156:157], v147 offset:33792\n"
    "v_cvt_pk_bf16_f32 v114, v116, v117\n"
    "v_mfma_f32_16x16x32_bf16 v[48:51], a[176:179], v[104:107], v[48:51]\n"
    "v_exp_f32_e64 v118, v118\n"
    "v_mfma_f32_16x16x32_bf16 v[52:55], a[180:183], v[104:107], v[52:55]\n"
    "ds_read_b64_tr_b16 a[158:159], v147 offset:37888\n"
    "v_mfma_f32_16x16x32_bf16 v[56:59], a[184:187], v[104:107], v[56:59]\n"
    "v_exp_f32_e64 v119, v119\n"
    "v_mfma_f32_16x16x32_bf16 v[60:63], a[188:191], v[104:107], v[60:63]\n"
    "ds_read_b128 a[100:103], v143 offset:32768\n"
    "v_cvt_pk_bf16_f32 v115, v118, v119\n"
    "v_mfma_f32_16x16x32_bf16 a[52:55], a[60:63], v[104:107], a[52:55]\n"
    "ds_read_b128 a[116:119], v143 offset:36864\n"
    "v_mfma_f32_16x16x32_bf16 v[96:99], a[64:67], a[32:35], v[136:139]\n"
    "v_exp_f32_e64 v120, v120\n"
    "v_mfma_f32_16x16x32_bf16 v[96:99], a[68:71], a[36:39], v[96:99]\n"
    "ds_read_b128 a[104:107], v142 offset:33792\n"
    "buffer_load_dwordx4 v243, s[48:51], s70 offen offset:3072 lds\n"
    "v_mfma_f32_16x16x32_bf16 v[96:99], a[72:75], a[40:43], v[96:99]\n"
    "v_exp_f32_e64 v121, v121\n"
    "v_mfma_f32_16x16x32_bf16 v[96:99], a[76:79], a[44:47], v[96:99]\n"
    "ds_read_b128 a[120:123], v142 offset:37888\n"
    "v_cvt_pk_bf16_f32 v120, v120, v121\n"
    "v_mfma_f32_16x16x32_bf16 v[0:3], a[128:131], v[112:115], v[0:3]\n"
    "v_exp_f32_e64 v122, v122\n"
    "v_mfma_f32_16x16x32_bf16 v[4:7], a[132:135], v[112:115], v[4:7]\n"
    "ds_read_b128 a[108:111], v143 offset:33792\n"
    "ds_read_b128 a[124:127], v143 offset:37888\n"
    "v_mfma_f32_16x16x32_bf16 v[8:11], a[136:139], v[112:115], v[8:11]\n"
    "v_exp_f32_e64 v123, v123\n"
    "v_mfma_f32_16x16x32_bf16 v[12:15], a[140:143], v[112:115], v[12:15]\n"
    "ds_read_b64_tr_b16 a[160:161], v148 offset:32768\n"
    "v_cvt_pk_bf16_f32 v121, v122, v123\n"
    "v_mfma_f32_16x16x32_bf16 v[100:103], a[80:83], a[32:35], v[136:139]\n"
    "v_exp_f32_e64 v124, v124\n"
    "v_mfma_f32_16x16x32_bf16 v[100:103], a[84:87], a[36:39], v[100:103]\n"
    "ds_read_b64_tr_b16 a[162:163], v148 offset:36864\n"
    "v_mfma_f32_16x16x32_bf16 v[100:103], a[88:91], a[40:43], v[100:103]\n"
    "v_exp_f32_e64 v125, v125\n"
    "v_mfma_f32_16x16x32_bf16 v[100:103], a[92:95], a[44:47], v[100:103]\n"
    "s_waitcnt lgkmcnt(4)\n"
    "ds_read_b64_tr_b16 a[164:165], v149 offset:32768\n"
    "v_cvt_pk_bf16_f32 v122, v124, v125\n"
    "v_mfma_f32_16x16x32_bf16 v[16:19], a[144:147], v[112:115], v[16:19]\n"
    "v_exp_f32_e32 v126, v126\n"
    "v_mfma_f32_16x16x32_bf16 v[20:23], a[148:151], v[112:115], v[20:23]\n"
    "ds_read_b64_tr_b16 a[166:167], v149 offset:36864\n"
    "ds_read_b64_tr_b16 a[168:169], v150 offset:32768\n"
    "v_mfma_f32_16x16x32_bf16 v[24:27], a[152:155], v[112:115], v[24:27]\n"
    "v_exp_f32_e64 v127, v127\n"
    "v_mfma_f32_16x16x32_bf16 v[28:31], a[156:159], v[112:115], v[28:31]\n"
    "ds_read_b64_tr_b16 a[170:171], v150 offset:36864\n"
    "v_cvt_pk_bf16_f32 v123, v126, v127\n"
    "v_mfma_f32_16x16x32_bf16 a[48:51], a[60:63], v[112:115], a[48:51]\n"
    "ds_read_b64_tr_b16 a[172:173], v151 offset:32768\n"
    "v_mfma_f32_16x16x32_bf16 v[104:107], a[96:99], a[0:3], v[128:131]\n"
    "v_exp_f32_e64 v96, v96\n"
    "v_mfma_f32_16x16x32_bf16 v[104:107], a[100:103], a[4:7], v[104:107]\n"
    "ds_read_b64_tr_b16 a[174:175], v151 offset:36864\n"
    "s_add_u32 m0, s63, 0x24000\n"
    "v_mfma_f32_16x16x32_bf16 v[104:107], a[104:107], a[8:11], v[104:107]\n"
    "v_exp_f32_e32 v97, v97\n"
    "s_waitcnt lgkmcnt(5)\n"
    "v_mfma_f32_16x16x32_bf16 v[104:107], a[108:111], a[12:15], v[104:107]\n"
    "ds_read_b64_tr_b16 a[176:177], v148 offset:33792\n"
    "v_cvt_pk_bf16_f32 v96, v96, v97\n"
    "v_mfma_f32_16x16x32_bf16 v[32:35], a[128:131], v[120:123], v[32:35]\n"
    "v_exp_f32_e64 v98, v98\n"
    "v_mfma_f32_16x16x32_bf16 v[36:39], a[132:135], v[120:123], v[36:39]\n"
    "ds_read_b64_tr_b16 a[178:179], v148 offset:37888\n"
    "ds_read_b64_tr_b16 a[180:181], v149 offset:33792\n"
    "v_mfma_f32_16x16x32_bf16 v[40:43], a[136:139], v[120:123], v[40:43]\n"
    "v_exp_f32_e64 v99, v99\n"
    "v_mfma_f32_16x16x32_bf16 v[44:47], a[140:143], v[120:123], v[44:47]\n"
    "s_add_u32 s64, s70, 0x9000\n"
    "v_cvt_pk_bf16_f32 v97, v98, v99\n"
    "v_mfma_f32_16x16x32_bf16 v[108:111], a[112:115], a[0:3], v[128:131]\n"
    "v_exp_f32_e64 v100, v100\n"
    "v_mfma_f32_16x16x32_bf16 v[108:111], a[116:119], a[4:7], v[108:111]\n"
    "ds_read_b64_tr_b16 a[182:183], v149 offset:37888\n"
    "v_mfma_f32_16x16x32_bf16 v[108:111], a[120:123], a[8:11], v[108:111]\n"
    "v_exp_f32_e64 v101, v101\n"
    "v_mfma_f32_16x16x32_bf16 v[108:111], a[124:127], a[12:15], v[108:111]\n"
    "ds_read_b64_tr_b16 a[184:185], v150 offset:33792\n"
    "v_cvt_pk_bf16_f32 v98, v100, v101\n"
    "v_mfma_f32_16x16x32_bf16 v[48:51], a[144:147], v[120:123], v[48:51]\n"
    "v_exp_f32_e64 v102, v102\n"
    "v_mfma_f32_16x16x32_bf16 v[52:55], a[148:151], v[120:123], v[52:55]\n"
    "ds_read_b64_tr_b16 a[186:187], v150 offset:37888\n"
    "buffer_load_dwordx4 v240, s[48:51], s64 offen lds\n"
    "v_mfma_f32_16x16x32_bf16 v[56:59], a[152:155], v[120:123], v[56:59]\n"
    "v_exp_f32_e64 v103, v103\n"
    "v_mfma_f32_16x16x32_bf16 v[60:63], a[156:159], v[120:123], v[60:63]\n"
    "ds_read_b64_tr_b16 a[188:189], v151 offset:33792\n"
    "v_cvt_pk_bf16_f32 v99, v102, v103\n"
    "v_mfma_f32_16x16x32_bf16 a[52:55], a[60:63], v[120:123], a[52:55]\n"
    "ds_read_b64_tr_b16 a[190:191], v151 offset:37888\n"
    "s_memrealtime s[78:79]\n"
    "s_add_u32 s99, s99, s62\n"
    "s_waitcnt lgkmcnt(0)\n"
    "s_sub_u32 s67, s78, s99\n"
    "s_cmp_lt_u32 s67, 0x80000000\n"
    "s_cbranch_scc1 L_rt22x\n"
    "L_rt22:\n"
    "s_sleep 1\n"
    "s_add_u32 s67, s67, 4\n"
    "s_cbranch_scc0 L_rt22\n"
    "L_rt22x:\n"
    "s_waitcnt vmcnt(9)\n"
    "s_barrier\n"
    "v_mfma_f32_16x16x32_bf16 v[112:115], a[96:99], a[16:19], v[132:135]\n"
    "v_exp_f32_e32 v104, v104\n"
    "v_mfma_f32_16x16x32_bf16 v[112:115], a[100:103], a[20:23], v[112:115]\n"
    "ds_read_b128 a[64:67], v140 offset:49152\n"
    "buffer_load_dwordx4 v241, s[48:51], s64 offen offset:1024 lds\n"
    "v_mfma_f32_16x16x32_bf16 v[112:115], a[104:107], a[24:27], v[112:115]\n"
    "v_exp_f32_e64 v105, v105\n"
    "v_mfma_f32_16x16x32_bf16 v[112:115], a[108:111], a[28:31], v[112:115]\n"
    "v_cvt_pk_bf16_f32 v104, v104, v105\n"
    "v_mfma_f32_16x16x32_bf16 v[64:67], a[128:131], v[96:99], v[64:67]\n"
    "v_exp_f32_e64 v106, v106\n"
    "v_mfma_f32_16x16x32_bf16 v[68:71], a[132:135], v[96:99], v[68:71]\n"
    "ds_read_b128 a[68:71], v141 offset:49152\n"
    "ds_read_b128 a[72:75], v140 offset:50176\n"
    "v_mfma_f32_16x16x32_bf16 v[72:75], a[136:139], v[96:99], v[72:75]\n"
    "v_exp_f32_e64 v107, v107\n"
    "v_mfma_f32_16x16x32_bf16 v[76:79], a[140:143], v[96:99], v[76:79]\n"
    "v_cvt_pk_bf16_f32 v105, v106, v107\n"
    "v_mfma_f32_16x16x32_bf16 v[116:119], a[112:115], a[16:19], v[132:135]\n"
    "v_exp_f32_e64 v108, v108\n"
    "v_mfma_f32_16x16x32_bf16 v[116:119], a[116:119], a[20:23], v[116:119]\n"
    "ds_read_b128 a[76:79], v141 offset:50176\n"
    "ds_read_b128 a[80:83], v140 offset:53248\n"
    "v_mfma_f32_16x16x32_bf16 v[116:119], a[120:123], a[24:27], v[116:119]\n"
    "v_exp_f32_e64 v109, v109\n"
    "v_mfma_f32_16x16x32_bf16 v[116:119], a[124:127], a[28:31], v[116:119]\n"
    "v_cvt_pk_bf16_f32 v106, v108, v109\n"
    "v_mfma_f32_16x16x32_bf16 v[80:83], a[144:147], v[96:99], v[80:83]\n"
    "v_exp_f32_e64 v110, v110\n"
    "v_mfma_f32_16x16x32_bf16 v[84:87], a[148:151], v[96:99], v[84:87]\n"
    "ds_read_b128 a[84:87], v141 offset:53248\n"
    "ds_read_b128 a[88:91], v140 offset:54272\n"
    "v_mfma_f32_16x16x32_bf16 v[88:91], a[152:155], v[96:99], v[88:91]\n"
    "v_exp_f32_e64 v111, v111\n"
    "v_mfma_f32_16x16x32_bf16 v[92:95], a[156:159], v[96:99], v[92:95]\n"
    "v_cvt_pk_bf16_f32 v107, v110, v111\n"
    "v_mfma_f32_16x16x32_bf16 a[56:59], a[60:63], v[96:99], a[56:59]\n"
    "ds_read_b128 a[92:95], v141 offset:54272\n"
    "s_waitcnt lgkmcnt(5)\n"
    "v_mfma_f32_16x16x32_bf16 v[120:123], a[64:67], a[0:3], v[128:131]\n"
    "v_exp_f32_e32 v112, v112\n"
    "v_mfma_f32_16x16x32_bf16 v[120:123], a[68:71], a[4:7], v[120:123]\n"
    "ds_read_b128 a[96:99], v142 offset:49152\n"
    "v_mfma_f32_16x16x32_bf16 v[120:123], a[72:75], a[8:11], v[120:123]\n"
    "v_exp_f32_e32 v113, v113\n"
    "s_waitcnt lgkmcnt(4)\n"
    "v_mfma_f32_16x16x32_bf16 v[120:123], a[76:79], a[12:15], v[120:123]\n"
    "buffer_load_dwordx4 v242, s[48:51], s64 offen offset:2048 lds\n"
    "v_cvt_pk_bf16_f32 v112, v112, v113\n"
    "v_mfma_f32_16x16x32_bf16 v[0:3], a[160:163], v[104:107], v[0:3]\n"
    "v_exp_f32_e64 v114, v114\n"
    "v_mfma_f32_16x16x32_bf16 v[4:7], a[164:167], v[104:107], v[4:7]\n"
    "v_mfma_f32_16x16x32_bf16 v[8:11], a[168:171], v[104:107], v[8:11]\n"
    "v_exp_f32_e64 v115, v115\n"
    "v_mfma_f32_16x16x32_bf16 v[12:15], a[172:175], v[104:107], v[12:15]\n"
    "ds_read_b128 a[100:103], v143 offset:49152\n"
    "v_cvt_pk_bf16_f32 v113, v114, v115\n"
    "v_mfma_f32_16x16x32_bf16 v[124:127], a[80:83], a[0:3], v[128:131]\n"
    "v_exp_f32_e32 v116, v116\n"
    "s_waitcnt lgkmcnt(2)\n"
    "v_mfma_f32_16x16x32_bf16 v[124:127], a[84:87], a[4:7], v[124:127]\n"
    "v_mfma_f32_16x16x32_bf16 v[124:127], a[88:91], a[8:11], v[124:127]\n"
    "v_exp_f32_e64 v117, v117\n"
    "v_mfma_f32_16x16x32_bf16 v[124:127], a[92:95], a[12:15], v[124:127]\n"
    "ds_read_b128 a[104:107], v142 offset:50176\n"
    "v_cvt_pk_bf16_f32 v114, v116, v117\n"
    "v_mfma_f32_16x16x32_bf16 v[16:19], a[176:179], v[104:107], v[16:19]\n"
    "v_exp_f32_e64 v118, v118\n"
    "v_mfma_f32_16x16x32_bf16 v[20:23], a[180:183], v[104:107], v[20:23]\n"
    "v_mfma_f32_16x16x32_bf16 v[24:27], a[184:187], v[104:107], v[24:27]\n"
    "v_exp_f32_e64 v119, v119\n"
    "v_mfma_f32_16x16x32_bf16 v[28:31], a[188:191], v[104:107], v[28:31]\n"
    "ds_read_b128 a[108:111], v143 offset:50176\n"
    "v_cvt_pk_bf16_f32 v115, v118, v119\n"
    "v_mfma_f32_16x16x32_bf16 a[48:51], a[60:63], v[104:107], a[48:51]\n"
    "v_mfma_f32_16x16x32_bf16 v[96:99], a[64:67], a[16:19], v[132:135]\n"
    "v_exp_f32_e64 v120, v120\n"
    "v_mfma_f32_16x16x32_bf16 v[96:99], a[68:71], a[20:23], v[96:99]\n"
    "ds_read_b128 a[112:115], v142 offset:53248\n"
    "v_mfma_f32_16x16x32_bf16 v[96:99], a[72:75], a[24:27], v[96:99]\n"
    "v_exp_f32_e64 v121, v121\n"
    "v_mfma_f32_16x16x32_bf16 v[96:99], a[76:79], a[28:31], v[96:99]\n"
    "buffer_load_dwordx4 v243, s[48:51], s64 offen offset:3072 lds\n"
    "v_cvt_pk_bf16_f32 v120, v120, v121\n"
    "v_mfma_f32_16x16x32_bf16 v[32:35], a[160:163], v[112:115], v[32:35]\n"
    "v_exp_f32_e64 v122, v122\n"
    "v_mfma_f32_16x16x32_bf16 v[36:39], a[164:167], v[112:115], v[36:39]\n"
    "v_mfma_f32_16x16x32_bf16 v[40:43], a[168:171], v[112:115], v[40:43]\n"
    "v_exp_f32_e64 v123, v123\n"
    "v_mfma_f32_16x16x32_bf16 v[44:47], a[172:175], v[112:115], v[44:47]\n"
    "ds_read_b128 a[116:119], v143 offset:53248\n"
    "v_cvt_pk_bf16_f32 v121, v122, v123\n"
    "v_mfma_f32_16x16x32_bf16 v[100:103], a[80:83], a[16:19], v[132:135]\n"
    "v_exp_f32_e64 v124, v124\n"
    "v_mfma_f32_16x16x32_bf16 v[100:103], a[84:87], a[20:23], v[100:103]\n"
    "v_mfma_f32_16x16x32_bf16 v[100:103], a[88:91], a[24:27], v[100:103]\n"
    "v_exp_f32_e64 v125, v125\n"
    "v_mfma_f32_16x16x32_bf16 v[100:103], a[92:95], a[28:31], v[100:103]\n"
    "ds_read_b128 a[120:123], v142 offset:54272\n"
    "v_cvt_pk_bf16_f32 v122, v124, v125\n"
    "v_mfma_f32_16x16x32_bf16 v[48:51], a[176:179], v[112:115], v[48:51]\n"
    "v_exp_f32_e64 v126, v126\n"
    "v_mfma_f32_16x16x32_bf16 v[52:55], a[180:183], v[112:115], v[52:55]\n"
    "v_mfma_f32_16x16x32_bf16 v[56:59], a[184:187], v[112:115], v[56:59]\n"
    "v_exp_f32_e64 v127, v127\n"
    "v_mfma_f32_16x16x32_bf16 v[60:63], a[188:191], v[112:115], v[60:63]\n"
    "ds_read_b128 a[124:127], v143 offset:54272\n"
    "v_cvt_pk_bf16_f32 v123, v126, v127\n"
    "v_mfma_f32_16x16x32_bf16 a[52:55], a[60:63], v[112:115], a[52:55]\n"
    "v_mfma_f32_16x16x32_bf16 v[104:107], a[64:67], a[32:35], v[136:139]\n"
    "s_add_u32 s69, s63, 0xc000\n"
    "v_exp_f32_e64 v96, v96\n"
    "v_mfma_f32_16x16x32_bf16 v[104:107], a[68:71], a[36:39], v[104:107]\n"
    "v_exp_f32_e64 v97, v97\n"
    "v_mfma_f32_16x16x32_bf16 v[104:107], a[72:75], a[40:43], v[104:107]\n"
    "v_exp_f32_e64 v98, v98\n"
    "v_cvt_pk_bf16_f32 v96, v96, v97\n"
    "v_mfma_f32_16x16x32_bf16 v[104:107], a[76:79], a[44:47], v[104:107]\n"
    "v_add_u32_e64 v156, s69, v155\n"
    "v_exp_f32_e64 v99, v99\n"
    "v_mfma_f32_16x16x32_bf16 v[108:111], a[80:83], a[32:35], v[136:139]\n"
    "v_exp_f32_e64 v100, v100\n"
    "v_cvt_pk_bf16_f32 v97, v98, v99\n"
    "v_mfma_f32_16x16x32_bf16 v[108:111], a[84:87], a[36:39], v[108:111]\n"
    "v_exp_f32_e64 v101, v101\n"
    "v_lshl_add_u32 v157, v154, 3, s69\n"
    "v_mfma_f32_16x16x32_bf16 v[108:111], a[88:91], a[40:43], v[108:111]\n"
    "v_exp_f32_e64 v102, v102\n"
    "v_cvt_pk_bf16_f32 v98, v100, v101\n"
    "v_mfma_f32_16x16x32_bf16 v[108:111], a[92:95], a[44:47], v[108:111]\n"
    "v_exp_f32_e32 v103, v103\n"
    "s_nop 0\n"
    "v_cvt_pk_bf16_f32 v99, v102, v103\n"
    "s_waitcnt lgkmcnt(0)\n"
    "v_mfma_f32_16x16x32_bf16 v[112:115], a[96:99], a[0:3], v[128:131]\n"
    "s_mov_b32 s98, 0x5d800000\n"
    "v_exp_f32_e32 v104, v104\n"
    "v_mfma_f32_16x16x32_bf16 v[112:115], a[100:103], a[4:7], v[112:115]\n"
    "v_exp_f32_e64 v105, v105\n"
    "v_mfma_f32_16x16x32_bf16 v[112:115], a[104:107], a[8:11], v[112:115]\n"
    "v_exp_f32_e64 v106, v106\n"
    "v_cvt_pk_bf16_f32 v104, v104, v105\n"
    "v_mfma_f32_16x16x32_bf16 v[112:115], a[108:111], a[12:15], v[112:115]\n"
    "v_add_u32_e32 v254, 0xffffc000, v156\n"
    "v_exp_f32_e64 v107, v107\n"
    "v_mfma_f32_16x16x32_bf16 v[116:119], a[112:115], a[0:3], v[128:131]\n"
    "v_exp_f32_e64 v108, v108\n"
    "v_cvt_pk_bf16_f32 v105, v106, v107\n"
    "v_mfma_f32_16x16x32_bf16 v[116:119], a[116:119], a[4:7], v[116:119]\n"
    "v_exp_f32_e64 v109, v109\n"
    "v_add_u32_e32 v255, 0x10000, v156\n"
    "v_mfma_f32_16x16x32_bf16 v[116:119], a[120:123], a[8:11], v[116:119]\n"
    "v_exp_f32_e64 v110, v110\n"
    "v_cvt_pk_bf16_f32 v106, v108, v109\n"
    "v_mfma_f32_16x16x32_bf16 v[116:119], a[124:127], a[12:15], v[116:119]\n"
    "v_exp_f32_e32 v111, v111\n"
    "s_nop 0\n"
    "v_cvt_pk_bf16_f32 v107, v110, v111\n"
    "s_memrealtime s[78:79]\n"
    "s_mul_i32 s67, s62, 10\n"
    "s_lshr_b32 s67, s67, 4\n"
    "s_add_u32 s99, s99, s67\n"
    "s_waitcnt lgkmcnt(0)\n"
    "s_sub_u32 s67, s78, s99\n"
    "s_cmp_lt_u32 s67, 0x80000000\n"
    "s_cbranch_scc1 L_rt23x\n"
    "L_rt23:\n"
    "s_sleep 1\n"
    "s_add_u32 s67, s67, 4\n"
    "s_cbranch_scc0 L_rt23\n"
    "L_rt23x:\n"
    "s_waitcnt vmcnt(8)\n"
    "s_barrier\n"
    "s_nop 0\n"
    "ds_read_b64_tr_b16 a[128:129], v144 offset:49152\n"
    "ds_read_b64_tr_b16 a[130:131], v144 offset:53248\n"
    "ds_read_b64_tr_b16 a[132:133], v145 offset:49152\n"
    "ds_read_b64_tr_b16 a[134:135], v145 offset:53248\n"
    "v_mfma_f32_16x16x32_bf16 v[124:127], a[96:99], a[16:19], v[132:135]\n"
    "ds_read_b64_tr_b16 a[136:137], v146 offset:49152\n"
    "ds_read_b64_tr_b16 a[138:139], v146 offset:53248\n"
    "v_exp_f32_e64 v112, v112\n"
    "v_mfma_f32_16x16x32_bf16 v[124:127], a[100:103], a[20:23], v[124:127]\n"
    "ds_read_b64_tr_b16 a[140:141], v147 offset:49152\n"
    "ds_read_b64_tr_b16 a[142:143], v147 offset:53248\n"
    "v_exp_f32_e64 v113, v113\n"
    "v_mfma_f32_16x16x32_bf16 v[124:127], a[104:107], a[24:27], v[124:127]\n"
    "ds_read_b64_tr_b16 a[144:145], v144 offset:50176\n"
    "v_exp_f32_e64 v114, v114\n"
    "v_cvt_pk_bf16_f32 v112, v112, v113\n"
    "v_mfma_f32_16x16x32_bf16 v[124:127], a[108:111], a[28:31], v[124:127]\n"
    "ds_read_b64_tr_b16 a[146:147], v144 offset:54272\n"
    "ds_read_b64_tr_b16 a[148:149], v145 offset:50176\n"
    "v_exp_f32_e64 v115, v115\n"
    "v_mfma_f32_16x16x32_bf16 v[100:103], a[112:115], a[16:19], v[132:135]\n"
    "ds_read_b64_tr_b16 a[150:151], v145 offset:54272\n"
    "v_exp_f32_e64 v116, v116\n"
    "v_cvt_pk_bf16_f32 v113, v114, v115\n"
    "v_mfma_f32_16x16x32_bf16 v[100:103], a[116:119], a[20:23], v[100:103]\n"
    "ds_read_b64_tr_b16 a[152:153], v146 offset:50176\n"
    "ds_read_b64_tr_b16 a[154:155], v146 offset:54272\n"
    "v_exp_f32_e64 v117, v117\n"
    "v_mfma_f32_16x16x32_bf16 v[100:103], a[120:123], a[24:27], v[100:103]\n"
    "ds_read_b64_tr_b16 a[156:157], v147 offset:50176\n"
    "v_exp_f32_e64 v118, v118\n"
    "v_cvt_pk_bf16_f32 v114, v116, v117\n"
    "v_mfma_f32_16x16x32_bf16 v[100:103], a[124:127], a[28:31], v[100:103]\n"
    "s_waitcnt lgkmcnt(7)\n"
    "ds_read_b64_tr_b16 a[158:159], v147 offset:54272\n"
    "v_exp_f32_e32 v119, v119\n"
    "s_nop 0\n"
    "v_cvt_pk_bf16_f32 v115, v118, v119\n"
    "v_mfma_f32_16x16x32_bf16 a[56:59], a[60:63], v[104:107], a[56:59]\n"
    "ds_read_b64_tr_b16 a[160:161], v148 offset:49152\n"
    "s_add_u32 m0, s63, 0\n"
    "v_mfma_f32_16x16x32_bf16 v[64:67], a[128:131], v[104:107], v[64:67]\n"
    "ds_read_b64_tr_b16 a[162:163], v148 offset:53248\n"
    "v_mfma_f32_16x16x32_bf16 v[68:71], a[132:135], v[104:107], v[68:71]\n"
    "ds_read_b64_tr_b16 a[164:165], v149 offset:49152\n"
    "v_exp_f32_e64 v124, v124\n"
    "v_mfma_f32_16x16x32_bf16 v[72:75], a[136:139], v[104:107], v[72:75]\n"
    "ds_read_b64_tr_b16 a[166:167], v149 offset:53248\n"
    "v_exp_f32_e64 v125, v125\n"
    "v_mfma_f32_16x16x32_bf16 v[76:79], a[140:143], v[104:107], v[76:79]\n"
    "s_add_u32 s70, s70, 0x80\n"
    "s_cmp_ge_u32 s70, s76\n"
    "s_cselect_b32 s70, s77, s70\n"
    "s_waitcnt lgkmcnt(5)\n"
    "v_mfma_f32_16x16x32_bf16 v[80:83], a[144:147], v[104:107], v[80:83]\n"
    "ds_read_b64_tr_b16 a[168:169], v150 offset:49152\n"
    "v_exp_f32_e32 v126, v126\n"
    "v_mfma_f32_16x16x32_bf16 v[84:87], a[148:151], v[104:107], v[84:87]\n"
    "ds_read_b64_tr_b16 a[170:171], v150 offset:53248\n"
    "v_exp_f32_e64 v127, v127\n"
    "v_mfma_f32_16x16x32_bf16 v[88:91], a[152:155], v[104:107], v[88:91]\n"
    "ds_read_b64_tr_b16 a[172:173], v151 offset:49152\n"
    "v_exp_f32_e32 v100, v100\n"
    "s_waitcnt lgkmcnt(5)\n"
    "v_mfma_f32_16x16x32_bf16 v[92:95], a[156:159], v[104:107], v[92:95]\n"
    "ds_read_b64_tr_b16 a[174:175], v151 offset:53248\n"
    "ds_read_b64_tr_b16 a[176:177], v148 offset:50176\n"
    "buffer_load_dwordx4 v240, s[48:51], s70 offen lds\n"
    "v_mfma_f32_16x16x32_bf16 a[48:51], a[60:63], v[120:123], a[48:51]\n"
    "v_exp_f32_e64 v101, v101\n"
    "v_mfma_f32_16x16x32_bf16 v[0:3], a[128:131], v[120:123], v[0:3]\n"
    "ds_read_b64_tr_b16 a[178:179], v148 offset:54272\n"
    "v_exp_f32_e64 v102, v102\n"
    "v_mfma_f32_16x16x32_bf16 v[4:7], a[132:135], v[120:123], v[4:7]\n"
    "ds_read_b64_tr_b16 a[180:181], v149 offset:50176\n"
    "buffer_load_dwordx4 v241, s[48:51], s70 offen offset:1024 lds\n"
    "v_mfma_f32_16x16x32_bf16 v[8:11], a[136:139], v[120:123], v[8:11]\n"
    "ds_read_b64_tr_b16 a[182:183], v149 offset:54272\n"
    "v_exp_f32_e64 v103, v103\n"
    "v_mfma_f32_16x16x32_bf16 v[12:15], a[140:143], v[120:123], v[12:15]\n"
    "ds_read_b64_tr_b16 a[184:185], v150 offset:50176\n"
    "buffer_load_dwordx4 v242, s[48:51], s70 offen offset:2048 lds\n"
    "v_mfma_f32_16x16x32_bf16 v[16:19], a[144:147], v[120:123], v[16:19]\n"
    "ds_read_b64_tr_b16 a[186:187], v150 offset:54272\n"
    "v_cvt_pk_bf16_f32 v124, v124, v125\n"
    "v_mfma_f32_16x16x32_bf16 v[20:23], a[148:151], v[120:123], v[20:23]\n"
    "ds_read_b64_tr_b16 a[188:189], v151 offset:50176\n"
    "v_cvt_pk_bf16_f32 v125, v126, v127\n"
    "v_mfma_f32_16x16x32_bf16 v[24:27], a[152:155], v[120:123], v[24:27]\n"
    "buffer_load_dwordx4 v243, s[48:51], s70 offen offset:3072 lds\n"
    "v_cvt_pk_bf16_f32 v126, v100, v101\n"
    "v_mfma_f32_16x16x32_bf16 v[28:31], a[156:159], v[120:123], v[28:31]\n"
    "ds_read_b64_tr_b16 a[190:191], v151 offset:54272\n"
    "v_cvt_pk_bf16_f32 v127, v102, v103\n"
    "s_waitcnt lgkmcnt(0)\n"
    "s_cmp_lg_u32 s66, 0\n"
    "s_nop 0\n"
    "s_cbranch_scc1 L_rp1\n"
    "v_mfma_f32_16x16x32_bf16 a[48:51], a[60:63], v[112:115], a[48:51]\n"
    "v_accvgpr_read_b32 v136, a56\n"
    "ds_write_b128 v156, v[80:83] offset:49152\n"
    "v_mfma_f32_16x16x32_bf16 v[0:3], a[160:163], v[112:115], v[0:3]\n"
    "ds_write_b128 v156, v[84:87] offset:50176\n"
    "v_mfma_f32_16x16x32_bf16 v[4:7], a[164:167], v[112:115], v[4:7]\n"
    "ds_write_b128 v156, v[88:91] offset:51200\n"
    "v_mfma_f32_16x16x32_bf16 v[8:11], a[168:171], v[112:115], v[8:11]\n"
    "ds_write_b128 v156, v[92:95] offset:52224\n"
    "v_mfma_f32_16x16x32_bf16 v[12:15], a[172:175], v[112:115], v[12:15]\n"
    "ds_write_b64 v157, v[136:137]\n"
    "v_mfma_f32_16x16x32_bf16 v[16:19], a[176:179], v[112:115], v[16:19]\n"
    "v_cmp_nlt_f32_e64 s[56:57], v136, s98\n"
    "v_mfma_f32_16x16x32_bf16 v[20:23], a[180:183], v[112:115], v[20:23]\n"
    "v_xor_b32_e32 v156, 0x1000, v156\n"
    "v_mfma_f32_16x16x32_bf16 v[24:27], a[184:187], v[112:115], v[24:27]\n"
    "v_xor_b32_e32 v157, 0x1000, v157\n"
    "v_mfma_f32_16x16x32_bf16 v[28:31], a[188:191], v[112:115], v[28:31]\n"
    "s_waitcnt lgkmcnt(0)\n"
    "s_branch L_rpx\n"
    "L_rp1:\n"
    "v_mfma_f32_16x16x32_bf16 a[48:51], a[60:63], v[112:115], a[48:51]\n"
    "v_accvgpr_read_b32 v136, a56\n"
    "ds_write_b128 v156, v[64:67] offset:49152\n"
    "v_mfma_f32_16x16x32_bf16 v[0:3], a[160:163], v[112:115], v[0:3]\n"
    "ds_write_b128 v156, v[68:71] offset:50176\n"
    "ds_write_b128 v156, v[72:75] offset:51200\n"
    "v_mfma_f32_16x16x32_bf16 v[4:7], a[164:167], v[112:115], v[4:7]\n"
    "ds_write_b128 v156, v[76:79] offset:52224\n"
    "ds_write_b64 v157, v[136:137]\n"
    "v_mfma_f32_16x16x32_bf16 v[8:11], a[168:171], v[112:115], v[8:11]\n"
    "v_cmp_nlt_f32_e64 s[56:57], v136, s98\n"
    "v_xor_b32_e32 v156, 0x1000, v156\n"
    "v_mfma_f32_16x16x32_bf16 v[12:15], a[172:175], v[112:115], v[12:15]\n"
    "v_xor_b32_e32 v157, 0x1000, v157\n"
    "v_pk_mul_f32 v[64:65], v[80:81], 1.0 op_sel_hi:[1,0]\n"
    "v_mfma_f32_16x16x32_bf16 v[16:19], a[176:179], v[112:115], v[16:19]\n"
    "v_pk_mul_f32 v[66:67], v[82:83], 1.0 op_sel_hi:[1,0]\n"
    "v_pk_mul_f32 v[68:69], v[84:85], 1.0 op_sel_hi:[1,0]\n"
    "v_mfma_f32_16x16x32_bf16 v[20:23], a[180:183], v[112:115], v[20:23]\n"
    "v_pk_mul_f32 v[70:71], v[86:87], 1.0 op_sel_hi:[1,0]\n"
    "v_pk_mul_f32 v[72:73], v[88:89], 1.0 op_sel_hi:[1,0]\n"
    "v_mfma_f32_16x16x32_bf16 v[24:27], a[184:187], v[112:115], v[24:27]\n"
    "v_pk_mul_f32 v[74:75], v[90:91], 1.0 op_sel_hi:[1,0]\n"
    "v_pk_mul_f32 v[76:77], v[92:93], 1.0 op_sel_hi:[1,0]\n"
    "v_mfma_f32_16x16x32_bf16 v[28:31], a[188:191], v[112:115], v[28:31]\n"
    "v_pk_mul_f32 v[78:79], v[94:95], 1.0 op_sel_hi:[1,0]\n"
    "s_waitcnt lgkmcnt(0)\n"
    "L_rpx:\n"
    "s_waitcnt vmcnt(4)\n"
    "s_barrier\n"
    "s_nop 0\n"
    "v_mfma_f32_16x16x32_bf16 a[52:55], a[60:63], v[96:99], a[52:55]\n"
    "ds_read_b64 v[138:139], v157\n"
    "s_add_u32 m0, s63, 0x4000\n"
    "v_mfma_f32_16x16x32_bf16 v[32:35], a[128:131], v[96:99], v[32:35]\n"
    "v_accvgpr_read_b32 v158, a48\n"
    "v_cmp_nlt_f32_e64 vcc, v158, s98\n"
    "v_mfma_f32_16x16x32_bf16 v[36:39], a[132:135], v[96:99], v[36:39]\n"
    "v_rcp_f32_e64 v158, v158\n"
    "v_mfma_f32_16x16x32_bf16 v[40:43], a[136:139], v[96:99], v[40:43]\n"
    "ds_read_b128 v[80:83], v156 offset:49152\n"
    "s_or_b64 s[56:57], s[56:57], vcc\n"
    "v_mfma_f32_16x16x32_bf16 v[44:47], a[140:143], v[96:99], v[44:47]\n"
    "ds_read_b128 v[84:87], v156 offset:50176\n"
    "v_mul_f32_e32 v0, v158, v0\n"
    "v_mfma_f32_16x16x32_bf16 v[48:51], a[144:147], v[96:99], v[48:51]\n"
    "ds_read_b128 v[88:91], v156 offset:51200\n"
    "v_mul_f32_e64 v1, v158, v1\n"
    "v_mfma_f32_16x16x32_bf16 v[52:55], a[148:151], v[96:99], v[52:55]\n"
    "s_add_u32 s64, s70, 0x9000\n"
    "v_mul_f32_e64 v2, v158, v2\n"
    "v_mfma_f32_16x16x32_bf16 v[56:59], a[152:155], v[96:99], v[56:59]\n"
    "ds_read_b128 v[92:95], v156 offset:52224\n"
    "v_mul_f32_e64 v3, v158, v3\n"
    "v_mfma_f32_16x16x32_bf16 v[60:63], a[156:159], v[96:99], v[60:63]\n"
    "v_mul_f32_e64 v4, v158, v4\n"
    "v_mul_f32_e64 v5, v158, v5\n"
    "v_mfma_f32_16x16x32_bf16 a[52:55], a[60:63], v[124:127], a[52:55]\n"
    "ds_read_b64_tr_b16 v[160:161], v246\n"
    "v_mul_f32_e64 v6, v158, v6\n"
    "v_mfma_f32_16x16x32_bf16 v[32:35], a[160:163], v[124:127], v[32:35]\n"
    "ds_read_b64_tr_b16 v[162:163], v246 offset:2048\n"
    "v_mul_f32_e64 v7, v158, v7\n"
    "v_mfma_f32_16x16x32_bf16 v[36:39], a[164:167], v[124:127], v[36:39]\n"
    "v_cvt_pk_bf16_f32 v0, v0, v1\n"
    "v_cvt_pk_bf16_f32 v1, v2, v3\n"
    "v_mfma_f32_16x16x32_bf16 v[40:43], a[168:171], v[124:127], v[40:43]\n"
    "ds_read_b64_tr_b16 v[164:165], v246 offset:16384\n"
    "buffer_load_dwordx4 v240, s[48:51], s64 offen lds\n"
    "v_mfma_f32_16x16x32_bf16 v[44:47], a[172:175], v[124:127], v[44:47]\n"
    "v_cvt_pk_bf16_f32 v2, v4, v5\n"
    "v_cvt_pk_bf16_f32 v3, v6, v7\n"
    "v_mfma_f32_16x16x32_bf16 v[48:51], a[176:179], v[124:127], v[48:51]\n"
    "ds_write_b128 v254, v[0:3]\n"
    "ds_read_b64_tr_b16 v[166:167], v246 offset:18432\n"
    "v_mfma_f32_16x16x32_bf16 v[52:55], a[180:183], v[124:127], v[52:55]\n"
    "v_pk_mul_f32 v[8:9], v[8:9], v[158:159] op_sel_hi:[1,0]\n"
    "v_pk_mul_f32 v[10:11], v[10:11], v[158:159] op_sel_hi:[1,0]\n"
    "v_mfma_f32_16x16x32_bf16 v[56:59], a[184:187], v[124:127], v[56:59]\n"
    "ds_read_b64_tr_b16 v[168:169], v246 offset:4096\n"
    "v_pk_mul_f32 v[12:13], v[12:13], v[158:159] op_sel_hi:[1,0]\n"
    "v_mfma_f32_16x16x32_bf16 v[60:63], a[188:191], v[124:127], v[60:63]\n"
    "ds_read_b64_tr_b16 v[170:171], v246 offset:6144\n"
    "v_pk_mul_f32 v[14:15], v[14:15], v[158:159] op_sel_hi:[1,0]\n"
    "ds_read_b64_tr_b16 v[172:173], v246 offset:20480\n"
    "ds_read_b64_tr_b16 v[174:175], v246 offset:22528\n"
    "ds_read_b64_tr_b16 v[176:177], v246 offset:8192\n"
    "s_waitcnt lgkmcnt(8)\n"
    "s_nop 0\n"
    "ds_read_b64_tr_b16 v[178:179], v246 offset:10240\n"
    "ds_read_b64_tr_b16 v[180:181], v246 offset:24576\n"
    "ds_read_b64_tr_b16 v[182:183], v246 offset:26624\n"
    "ds_read_b64_tr_b16 v[184:185], v246 offset:12288\n"
    "ds_read_b64_tr_b16 v[186:187], v246 offset:14336\n"
    "ds_read_b64_tr_b16 v[188:189], v246 offset:28672\n"
    "ds_read_b64_tr_b16 v[190:191], v246 offset:30720\n"
    "buffer_load_dwordx4 v241, s[48:51], s64 offen offset:1024 lds\n"
    "buffer_load_dwordx4 v242, s[48:51], s64 offen offset:2048 lds\n"
    "buffer_load_dwordx4 v243, s[48:51], s64 offen offset:3072 lds\n"
    "v_cvt_pk_bf16_f32 v8, v8, v9\n"
    "v_cvt_pk_bf16_f32 v9, v10, v11\n"
    "v_cvt_pk_bf16_f32 v10, v12, v13\n"
    "v_cvt_pk_bf16_f32 v11, v14, v15\n"
    "s_waitcnt lgkmcnt(14)\n"
    "s_nop 0\n"
    "ds_write_b128 v254, v[8:11] offset:1024\n"
    "v_pk_mul_f32 v[16:17], v[16:17], v[158:159] op_sel_hi:[1,0]\n"
    "v_pk_mul_f32 v[18:19], v[18:19], v[158:159] op_sel_hi:[1,0]\n"
    "v_pk_mul_f32 v[20:21], v[20:21], v[158:159] op_sel_hi:[1,0]\n"
    "v_pk_mul_f32 v[22:23], v[22:23], v[158:159] op_sel_hi:[1,0]\n"
    "v_cvt_pk_bf16_f32 v16, v16, v17\n"
    "v_cvt_pk_bf16_f32 v17, v18, v19\n"
    "v_cvt_pk_bf16_f32 v18, v20, v21\n"
    "v_cvt_pk_bf16_f32 v19, v22, v23\n"
    "v_pk_mul_f32 v[24:25], v[24:25], v[158:159] op_sel_hi:[1,0]\n"
    "v_pk_mul_f32 v[26:27], v[26:27], v[158:159] op_sel_hi:[1,0]\n"
    "v_pk_mul_f32 v[28:29], v[28:29], v[158:159] op_sel_hi:[1,0]\n"
    "v_pk_mul_f32 v[30:31], v[30:31], v[158:159] op_sel_hi:[1,0]\n"
    "v_cvt_pk_bf16_f32 v24, v24, v25\n"
    "v_cvt_pk_bf16_f32 v25, v26, v27\n"
    "v_cvt_pk_bf16_f32 v26, v28, v29\n"
    "v_cvt_pk_bf16_f32 v27, v30, v31\n"
    "s_waitcnt lgkmcnt(11)\n"
    "s_nop 0\n"
    "ds_write_b128 v254, v[16:19] offset:2048\n"
    "ds_write_b128 v254, v[24:27] offset:3072\n"
    "s_add_u32 m0, s63, 0x10000\n"
    "s_add_u32 s70, s70, 0x80\n"
    "s_cmp_ge_u32 s70, s76\n"
    "s_cselect_b32 s70, s77, s70\n"
    "buffer_load_dwordx4 v240, s[48:51], s70 offen lds\n"
    "buffer_load_dwordx4 v241, s[48:51], s70 offen offset:1024 lds\n"
    "buffer_load_dwordx4 v242, s[48:51], s70 offen offset:2048 lds\n"
    "buffer_load_dwordx4 v243, s[48:51], s70 offen offset:3072 lds\n"
    "v_min_f32_e64 v128, v137, v139\n"
    "v_sub_f32_e64 v137, v128, v137\n"
    "v_sub_f32_e64 v139, v128, v139\n"
    "v_exp_f32_e64 v137, v137\n"
    "v_exp_f32_e64 v139, v139\n"
    "v_mul_f32_e64 v136, v136, v137\n"
    "v_fmac_f32_e64 v136, v138, v139\n"
    "v_rcp_f32_e64 v136, v136\n"
    "s_nop 0\n"
    "v_mul_f32_e32 v132, v137, v136\n"
    "v_mul_f32_e64 v134, v139, v136\n"
    "v_pk_mul_f32 v[64:65], v[64:65], v[132:133] op_sel_hi:[1,0]\n"
    "v_pk_mul_f32 v[66:67], v[66:67], v[132:133] op_sel_hi:[1,0]\n"
    "v_pk_mul_f32 v[68:69], v[68:69], v[132:133] op_sel_hi:[1,0]\n"
    "v_pk_mul_f32 v[70:71], v[70:71], v[132:133] op_sel_hi:[1,0]\n"
    "v_pk_mul_f32 v[72:73], v[72:73], v[132:133] op_sel_hi:[1,0]\n"
    "v_pk_mul_f32 v[74:75], v[74:75], v[132:133] op_sel_hi:[1,0]\n"
    "v_pk_mul_f32 v[76:77], v[76:77], v[132:133] op_sel_hi:[1,0]\n"
    "v_pk_mul_f32 v[78:79], v[78:79], v[132:133] op_sel_hi:[1,0]\n"
    "v_pk_fma_f32 v[64:65], v[80:81], v[134:135], v[64:65] op_sel_hi:[1,0,1]\n"
    "v_pk_fma_f32 v[66:67], v[82:83], v[134:135], v[66:67] op_sel_hi:[1,0,1]\n"
    "v_pk_fma_f32 v[68:69], v[84:85], v[134:135], v[68:69] op_sel_hi:[1,0,1]\n"
    "v_pk_fma_f32 v[70:71], v[86:87], v[134:135], v[70:71] op_sel_hi:[1,0,1]\n"
    "v_pk_fma_f32 v[72:73], v[88:89], v[134:135], v[72:73] op_sel_hi:[1,0,1]\n"
    "v_pk_fma_f32 v[74:75], v[90:91], v[134:135], v[74:75] op_sel_hi:[1,0,1]\n"
    "v_pk_fma_f32 v[76:77], v[92:93], v[134:135], v[76:77] op_sel_hi:[1,0,1]\n"
    "v_pk_fma_f32 v[78:79], v[94:95], v[134:135], v[78:79] op_sel_hi:[1,0,1]\n"
    "v_cvt_pk_bf16_f32 v64, v64, v65\n"
    "v_cvt_pk_bf16_f32 v65, v66, v67\n"
    "v_cvt_pk_bf16_f32 v66, v68, v69\n"
    "v_cvt_pk_bf16_f32 v67, v70, v71\n"
    "v_cvt_pk_bf16_f32 v72, v72, v73\n"
    "v_cvt_pk_bf16_f32 v73, v74, v75\n"
    "v_cvt_pk_bf16_f32 v74, v76, v77\n"
    "v_cvt_pk_bf16_f32 v75, v78, v79\n"
    "ds_write_b128 v254, v[64:67] offset:17408\n"
    "ds_write_b128 v254, v[72:75] offset:18432\n"
    "s_add_u32 m0, s63, 0x14000\n"
    "s_add_u32 s64, s70, 0x9000\n"
    "buffer_load_dwordx4 v240, s[48:51], s64 offen lds\n"
    "buffer_load_dwordx4 v241, s[48:51], s64 offen offset:1024 lds\n"
    "buffer_load_dwordx4 v242, s[48:51], s64 offen offset:2048 lds\n"
    "buffer_load_dwordx4 v243, s[48:51], s64 offen offset:3072 lds\n"
    "v_accvgpr_read_b32 v130, a52\n"
    "v_cmp_nlt_f32_e64 vcc, v130, s98\n"
    "v_rcp_f32_e64 v130, v130\n"
    "s_or_b64 s[56:57], s[56:57], vcc\n"
    "s_cmp_lg_u64 s[56:57], 0\n"
    "s_cselect_b32 s67, 1, 0\n"
    "v_mov_b32_e32 v98, s67\n"
    "v_mov_b32_e64 v99, s69\n"
    "v_and_b32_e64 v100, 3, v154\n"
    "v_lshlrev_b32_e64 v100, 12, v100\n"
    "v_pk_mul_f32 v[32:33], v[32:33], v[130:131] op_sel_hi:[1,0]\n"
    "v_pk_mul_f32 v[34:35], v[34:35], v[130:131] op_sel_hi:[1,0]\n"
    "v_pk_mul_f32 v[36:37], v[36:37], v[130:131] op_sel_hi:[1,0]\n"
    "v_pk_mul_f32 v[38:39], v[38:39], v[130:131] op_sel_hi:[1,0]\n"
    "v_cvt_pk_bf16_f32 v32, v32, v33\n"
    "v_cvt_pk_bf16_f32 v33, v34, v35\n"
    "v_cvt_pk_bf16_f32 v34, v36, v37\n"
    "v_cvt_pk_bf16_f32 v35, v38, v39\n"
    "v_pk_mul_f32 v[40:41], v[40:41], v[130:131] op_sel_hi:[1,0]\n"
    "v_pk_mul_f32 v[42:43], v[42:43], v[130:131] op_sel_hi:[1,0]\n"
    "v_pk_mul_f32 v[44:45], v[44:45], v[130:131] op_sel_hi:[1,0]\n"
    "v_pk_mul_f32 v[46:47], v[46:47], v[130:131] op_sel_hi:[1,0]\n"
    "v_cvt_pk_bf16_f32 v40, v40, v41\n"
    "v_cvt_pk_bf16_f32 v41, v42, v43\n"
    "v_cvt_pk_bf16_f32 v42, v44, v45\n"
    "v_cvt_pk_bf16_f32 v43, v46, v47\n"
    "s_waitcnt lgkmcnt(2)\n"
    "s_nop 0\n"
    "ds_write_b128 v255, v[32:35]\n"
    "ds_write_b128 v255, v[40:43] offset:1024\n"
    "v_pk_mul_f32 v[48:49], v[48:49], v[130:131] op_sel_hi:[1,0]\n"
    "v_pk_mul_f32 v[50:51], v[50:51], v[130:131] op_sel_hi:[1,0]\n"
    "v_pk_mul_f32 v[52:53], v[52:53], v[130:131] op_sel_hi:[1,0]\n"
    "v_pk_mul_f32 v[54:55], v[54:55], v[130:131] op_sel_hi:[1,0]\n"
    "v_cvt_pk_bf16_f32 v48, v48, v49\n"
    "v_cvt_pk_bf16_f32 v49, v50, v51\n"
    "v_cvt_pk_bf16_f32 v50, v52, v53\n"
    "v_cvt_pk_bf16_f32 v51, v54, v55\n"
    "v_pk_mul_f32 v[56:57], v[56:57], v[130:131] op_sel_hi:[1,0]\n"
    "v_pk_mul_f32 v[58:59], v[58:59], v[130:131] op_sel_hi:[1,0]\n"
    "v_pk_mul_f32 v[60:61], v[60:61], v[130:131] op_sel_hi:[1,0]\n"
    "v_pk_mul_f32 v[62:63], v[62:63], v[130:131] op_sel_hi:[1,0]\n"
    "v_cvt_pk_bf16_f32 v56, v56, v57\n"
    "v_cvt_pk_bf16_f32 v57, v58, v59\n"
    "v_cvt_pk_bf16_f32 v58, v60, v61\n"
    "v_cvt_pk_bf16_f32 v59, v62, v63\n"
    "ds_write_b128 v255, v[48:51] offset:2048\n"
    "ds_write_b128 v255, v[56:59] offset:3072\n"
    "ds_write_b32 v99, v98 offset:512\n"
    "s_waitcnt vmcnt(8) lgkmcnt(0)\n"
    "s_barrier\n"
    "ds_read_b32 v98, v100 offset:49664\n"
    "ds_read_b128 a[64:67], v155 offset:32768\n"
    "ds_read_b128 a[68:71], v155 offset:33792\n"
    "ds_read_b128 a[72:75], v155 offset:34816\n"
    "ds_read_b128 a[76:79], v155 offset:35840\n"
    "s_waitcnt lgkmcnt(4)\n"
    "v_cmp_lt_u32_e32 vcc, s72, v98\n"
    "v_mov_b32_e64 v56, s96\n"
    "v_mov_b32_e64 v57, s97\n"
    "v_mov_b32_e64 v58, s96\n"
    "v_mov_b32_e32 v59, s97\n"
    "s_cbranch_vccnz L_fallback\n"
    "ds_read_b128 a[80:83], v155 offset:40960\n"
    "ds_read_b128 a[84:87], v155 offset:41984\n"
    "ds_read_b128 a[88:91], v155 offset:43008\n"
    "ds_read_b128 a[92:95], v155 offset:44032\n"
    "ds_read_b128 a[96:99], v247 offset:49152\n"
    "ds_read_b128 a[100:103], v247 offset:50176\n"
    "ds_read_b128 a[104:107], v247 offset:51200\n"
    "ds_read_b128 a[108:111], v247 offset:52224\n"
    "ds_read_b128 a[112:115], v247 offset:57344\n"
    "ds_read_b128 a[116:119], v247 offset:58368\n"
    "s_waitcnt lgkmcnt(13)\n"
    "v_mfma_f32_16x16x32_bf16 v[0:3], v[160:163], a[64:67], 0\n"
    "ds_read_b128 a[120:123], v247 offset:59392\n"
    "s_add_u32 m0, s63, 0x20000\n"
    "s_waitcnt lgkmcnt(13)\n"
    "v_mfma_f32_16x16x32_bf16 v[0:3], v[164:167], a[68:71], v[0:3]\n"
    "ds_read_b128 a[124:127], v247 offset:60416\n"
    "ds_read_b128 a[128:131], v155 offset:50176\n"
    "s_waitcnt lgkmcnt(14)\n"
    "v_mfma_f32_16x16x32_bf16 v[0:3], v[168:171], a[72:75], v[0:3]\n"
    "ds_read_b128 a[132:135], v155 offset:51200\n"
    "s_waitcnt lgkmcnt(14)\n"
    "v_mfma_f32_16x16x32_bf16 v[0:3], v[172:175], a[76:79], v[0:3]\n"
    "s_add_u32 s70, s70, 0x80\n"
    "s_cmp_ge_u32 s70, s76\n"
    "s_cselect_b32 s70, s77, s70\n"
    "s_waitcnt lgkmcnt(13)\n"
    "v_mfma_f32_16x16x32_bf16 v[0:3], v[176:179], a[80:83], v[0:3]\n"
    "ds_read_b128 a[136:139], v155 offset:54272\n"
    "ds_read_b128 a[140:143], v155 offset:55296\n"
    "s_waitcnt lgkmcnt(14)\n"
    "v_mfma_f32_16x16x32_bf16 v[0:3], v[180:183], a[84:87], v[0:3]\n"
    "ds_read_b128 a[144:147], v155 offset:58368\n"
    "s_waitcnt lgkmcnt(13)\n"
    "s_nop 0\n"
    "v_mfma_f32_16x16x32_bf16 v[0:3], v[184:187], a[88:91], v[0:3]\n"
    "ds_read_b128 a[148:151], v155 offset:59392\n"
    "v_mfma_f32_16x16x32_bf16 v[0:3], v[188:191], a[92:95], v[0:3]\n"
    "ds_read_b128 a[152:155], v155 offset:62464\n"
    "s_waitcnt lgkmcnt(9)\n"
    "s_nop 0\n"
    "v_mfma_f32_16x16x32_bf16 v[4:7], v[160:163], a[96:99], 0\n"
    "ds_read_b128 a[156:159], v155 offset:63488\n"
    "v_mfma_f32_16x16x32_bf16 v[4:7], v[164:167], a[100:103], v[4:7]\n"
    "ds_read_b128 a[160:163], v155 offset:36864\n"
    "v_mfma_f32_16x16x32_bf16 v[4:7], v[168:171], a[104:107], v[4:7]\n"
    "ds_read_b128 a[164:167], v155 offset:37888\n"
    "v_mfma_f32_16x16x32_bf16 v[4:7], v[172:175], a[108:111], v[4:7]\n"
    "v_cvt_pk_bf16_f32 v64, v0, v1\n"
    "v_cvt_pk_bf16_f32 v65, v2, v3\n"
    "v_mfma_f32_16x16x32_bf16 v[4:7], v[176:179], a[112:115], v[4:7]\n"
    "ds_read_b128 a[168:171], v155 offset:38912\n"
    "buffer_load_dwordx4 v240, s[48:51], s70 offen lds\n"
    "v_mfma_f32_16x16x32_bf16 v[4:7], v[180:183], a[116:119], v[4:7]\n"
    "ds_read_b128 a[172:175], v155 offset:39936\n"
    "ds_read_b128 a[176:179], v155 offset:45056\n"
    "s_waitcnt lgkmcnt(8)\n"
    "s_nop 0\n"
    "v_mfma_f32_16x16x32_bf16 v[4:7], v[184:187], a[120:123], v[4:7]\n"
    "ds_read_b128 a[180:183], v155 offset:46080\n"
    "buffer_load_dwordx4 v241, s[48:51], s70 offen offset:1024 lds\n"
    "v_mfma_f32_16x16x32_bf16 v[4:7], v[188:191], a[124:127], v[4:7]\n"
    "ds_read_b128 a[184:187], v155 offset:47104\n"
    "v_mfma_f32_16x16x32_bf16 v[8:11], v[160:163], a[128:131], 0\n"
    "ds_read_b128 a[188:191], v155 offset:48128\n"
    "v_mfma_f32_16x16x32_bf16 v[8:11], v[164:167], a[132:135], v[8:11]\n"
    "ds_read_b128 a[192:195], v247 offset:53248\n"
    "buffer_load_dwordx4 v242, s[48:51], s70 offen offset:2048 lds\n"
    "v_mfma_f32_16x16x32_bf16 v[8:11], v[168:171], a[136:139], v[8:11]\n"
    "ds_read_b128 a[196:199], v247 offset:54272\n"
    "v_mfma_f32_16x16x32_bf16 v[8:11], v[172:175], a[140:143], v[8:11]\n"
    "v_cvt_pk_bf16_f32 v66, v4, v5\n"
    "v_cvt_pk_bf16_f32 v67, v6, v7\n"
    "v_mfma_f32_16x16x32_bf16 v[8:11], v[176:179], a[144:147], v[8:11]\n"
    "ds_read_b128 a[200:203], v247 offset:55296\n"
    "ds_read_b128 a[204:207], v247 offset:56320\n"
    "s_waitcnt lgkmcnt(9)\n"
    "v_mfma_f32_16x16x32_bf16 v[8:11], v[180:183], a[148:151], v[8:11]\n"
    "s_add_u32 s68, s73, 0\n"
    "buffer_store_dwordx4 v[64:67], v248, s[52:55], s68 offen sc1\n"
    "v_mfma_f32_16x16x32_bf16 v[8:11], v[184:187], a[152:155], v[8:11]\n"
    "buffer_load_dwordx4 v243, s[48:51], s70 offen offset:3072 lds\n"
    "ds_read_b128 a[208:211], v247 offset:61440\n"
    "v_mfma_f32_16x16x32_bf16 v[8:11], v[188:191], a[156:159], v[8:11]\n"
    "ds_read_b128 a[212:215], v247 offset:62464\n"
    "ds_read_b128 a[216:219], v247 offset:63488\n"
    "v_mfma_f32_16x16x32_bf16 v[12:15], v[160:163], a[160:163], 0\n"
    "s_add_u32 m0, s63, 0x24000\n"
    "ds_read_b128 a[220:223], v247 offset:64512\n"
    "v_mfma_f32_16x16x32_bf16 v[12:15], v[164:167], a[164:167], v[12:15]\n"
    "ds_read_b64_tr_b16 v[192:193], v244\n"
    "ds_read_b64_tr_b16 v[194:195], v244 offset:2048\n"
    "v_mfma_f32_16x16x32_bf16 v[12:15], v[168:171], a[168:171], v[12:15]\n"
    "s_add_u32 s64, s70, 0x9000\n"
    "s_waitcnt lgkmcnt(8)\n"
    "s_nop 0\n"
    "ds_read_b64_tr_b16 v[196:197], v244 offset:16384\n"
    "v_mfma_f32_16x16x32_bf16 v[12:15], v[172:175], a[172:175], v[12:15]\n"
    "v_cvt_pk_bf16_f32 v68, v8, v9\n"
    "v_cvt_pk_bf16_f32 v69, v10, v11\n"
    "v_mfma_f32_16x16x32_bf16 v[12:15], v[176:179], a[176:179], v[12:15]\n"
    "ds_read_b64_tr_b16 v[198:199], v244 offset:18432\n"
    "ds_read_b64_tr_b16 v[200:201], v244 offset:4096\n"
    "v_mfma_f32_16x16x32_bf16 v[12:15], v[180:183], a[180:183], v[12:15]\n"
    "buffer_load_dwordx4 v240, s[48:51], s64 offen lds\n"
    "ds_read_b64_tr_b16 v[202:203], v244 offset:6144\n"
    "v_mfma_f32_16x16x32_bf16 v[12:15], v[184:187], a[184:187], v[12:15]\n"
    "ds_read_b64_tr_b16 v[204:205], v244 offset:20480\n"
    "v_mfma_f32_16x16x32_bf16 v[12:15], v[188:191], a[188:191], v[12:15]\n"
    "ds_read_b64_tr_b16 v[206:207], v244 offset:22528\n"
    "buffer_load_dwordx4 v241, s[48:51], s64 offen offset:1024 lds\n"
    "v_mfma_f32_16x16x32_bf16 v[16:19], v[160:163], a[192:195], 0\n"
    "ds_read_b64_tr_b16 v[208:209], v244 offset:8192\n"
    "v_mfma_f32_16x16x32_bf16 v[16:19], v[164:167], a[196:199], v[16:19]\n"
    "s_waitcnt lgkmcnt(7)\n"
    "s_nop 0\n"
    "ds_read_b64_tr_b16 v[210:211], v244 offset:10240\n"
    "v_mfma_f32_16x16x32_bf16 v[16:19], v[168:171], a[200:203], v[16:19]\n"
    "ds_read_b64_tr_b16 v[212:213], v244 offset:24576\n"
    "buffer_load_dwordx4 v242, s[48:51], s64 offen offset:2048 lds\n"
    "v_mfma_f32_16x16x32_bf16 v[16:19], v[172:175], a[204:207], v[16:19]\n"
    "v_cvt_pk_bf16_f32 v70, v12, v13\n"
    "v_cvt_pk_bf16_f32 v71, v14, v15\n"
    "v_mfma_f32_16x16x32_bf16 v[16:19], v[176:179], a[208:211], v[16:19]\n"
    "ds_read_b64_tr_b16 v[214:215], v244 offset:26624\n"
    "ds_read_b64_tr_b16 v[216:217], v244 offset:12288\n"
    "v_mfma_f32_16x16x32_bf16 v[16:19], v[180:183], a[212:215], v[16:19]\n"
    "s_add_u32 s68, s73, 0x1000\n"
    "buffer_store_dwordx4 v[68:71], v248, s[52:55], s68 offen sc1\n"
    "v_mfma_f32_16x16x32_bf16 v[16:19], v[184:187], a[216:219], v[16:19]\n"
    "ds_read_b64_tr_b16 v[218:219], v244 offset:14336\n"
    "buffer_load_dwordx4 v243, s[48:51], s64 offen offset:3072 lds\n"
    "v_mfma_f32_16x16x32_bf16 v[16:19], v[188:191], a[220:223], v[16:19]\n"
    "ds_read_b64_tr_b16 v[220:221], v244 offset:28672\n"
    "ds_read_b64_tr_b16 v[222:223], v244 offset:30720\n"
    "s_waitcnt vmcnt(10) lgkmcnt(0)\n"
    "s_barrier\n"
    "v_mfma_f32_16x16x32_bf16 v[20:23], v[192:195], a[64:67], 0\n"
    "ds_read_b64_tr_b16 v[160:161], v245\n"
    "s_add_u32 m0, s63, 0x8000\n"
    "v_mfma_f32_16x16x32_bf16 v[20:23], v[196:199], a[68:71], v[20:23]\n"
    "s_add_u32 s70, s70, 0x80\n"
    "s_cmp_ge_u32 s70, s76\n"
    "s_cselect_b32 s70, s77, s70\n"
    "v_mfma_f32_16x16x32_bf16 v[20:23], v[200:203], a[72:75], v[20:23]\n"
    "ds_read_b64_tr_b16 v[162:163], v245 offset:2048\n"
    "v_mfma_f32_16x16x32_bf16 v[20:23], v[204:207], a[76:79], v[20:23]\n"
    "v_cvt_pk_bf16_f32 v72, v16, v17\n"
    "v_cvt_pk_bf16_f32 v73, v18, v19\n"
    "v_mfma_f32_16x16x32_bf16 v[20:23], v[208:211], a[80:83], v[20:23]\n"
    "ds_read_b64_tr_b16 v[164:165], v245 offset:16384\n"
    "v_mfma_f32_16x16x32_bf16 v[20:23], v[212:215], a[84:87], v[20:23]\n"
    "buffer_load_dwordx4 v240, s[48:51], s70 offen lds\n"
    "v_mfma_f32_16x16x32_bf16 v[20:23], v[216:219], a[88:91], v[20:23]\n"
    "v_mfma_f32_16x16x32_bf16 v[20:23], v[220:223], a[92:95], v[20:23]\n"
    "buffer_load_dwordx4 v241, s[48:51], s70 offen offset:1024 lds\n"
    "ds_read_b64_tr_b16 v[166:167], v245 offset:18432\n"
    "v_mfma_f32_16x16x32_bf16 v[24:27], v[192:195], a[96:99], 0\n"
    "buffer_load_dwordx4 v242, s[48:51], s70 offen offset:2048 lds\n"
    "v_mfma_f32_16x16x32_bf16 v[24:27], v[196:199], a[100:103], v[24:27]\n"
    "ds_read_b64_tr_b16 v[168:169], v245 offset:4096\n"
    "buffer_load_dwordx4 v243, s[48:51], s70 offen offset:3072 lds\n"
    "v_mfma_f32_16x16x32_bf16 v[24:27], v[200:203], a[104:107], v[24:27]\n"
    "v_mfma_f32_16x16x32_bf16 v[24:27], v[204:207], a[108:111], v[24:27]\n"
    "v_cvt_pk_bf16_f32 v74, v20, v21\n"
    "v_cvt_pk_bf16_f32 v75, v22, v23\n"
    "v_mfma_f32_16x16x32_bf16 v[24:27], v[208:211], a[112:115], v[24:27]\n"
    "s_add_u32 m0, s63, 0xc000\n"
    "ds_read_b64_tr_b16 v[170:171], v245 offset:6144\n"
    "v_mfma_f32_16x16x32_bf16 v[24:27], v[212:215], a[116:119], v[24:27]\n"
    "s_add_u32 s68, s73, 0x2000\n"
    "buffer_store_dwordx4 v[72:75], v248, s[52:55], s68 offen sc1\n"
    "v_mfma_f32_16x16x32_bf16 v[24:27], v[216:219], a[120:123], v[24:27]\n"
    "s_add_u32 s64, s70, 0x9000\n"
    "buffer_load_dwordx4 v240, s[48:51], s64 offen lds\n"
    "v_mfma_f32_16x16x32_bf16 v[24:27], v[220:223], a[124:127], v[24:27]\n"
    "ds_read_b64_tr_b16 v[172:173], v245 offset:20480\n"
    "buffer_load_dwordx4 v241, s[48:51], s64 offen offset:1024 lds\n"
    "v_mfma_f32_16x16x32_bf16 v[28:31], v[192:195], a[128:131], 0\n"
    "v_mfma_f32_16x16x32_bf16 v[28:31], v[196:199], a[132:135], v[28:31]\n"
    "buffer_load_dwordx4 v242, s[48:51], s64 offen offset:2048 lds\n"
    "ds_read_b64_tr_b16 v[174:175], v245 offset:22528\n"
    "v_mfma_f32_16x16x32_bf16 v[28:31], v[200:203], a[136:139], v[28:31]\n"
    "buffer_load_dwordx4 v243, s[48:51], s64 offen offset:3072 lds\n"
    "v_mfma_f32_16x16x32_bf16 v[28:31], v[204:207], a[140:143], v[28:31]\n"
    "ds_read_b64_tr_b16 v[176:177], v245 offset:8192\n"
    "v_cvt_pk_bf16_f32 v76, v24, v25\n"
    "v_mfma_f32_16x16x32_bf16 v[28:31], v[208:211], a[144:147], v[28:31]\n"
    "s_add_u32 m0, s63, 0x18000\n"
    "v_cvt_pk_bf16_f32 v77, v26, v27\n"
    "v_mfma_f32_16x16x32_bf16 v[28:31], v[212:215], a[148:151], v[28:31]\n"
    "s_add_u32 s70, s70, 0x80\n"
    "s_cmp_ge_u32 s70, s76\n"
    "s_cselect_b32 s70, s77, s70\n"
    "v_mfma_f32_16x16x32_bf16 v[28:31], v[216:219], a[152:155], v[28:31]\n"
    "ds_read_b64_tr_b16 v[178:179], v245 offset:10240\n"
    "v_mfma_f32_16x16x32_bf16 v[28:31], v[220:223], a[156:159], v[28:31]\n"
    "v_mfma_f32_16x16x32_bf16 v[32:35], v[192:195], a[160:163], 0\n"
    "ds_read_b64_tr_b16 v[180:181], v245 offset:24576\n"
    "v_mfma_f32_16x16x32_bf16 v[32:35], v[196:199], a[164:167], v[32:35]\n"
    "buffer_load_dwordx4 v240, s[48:51], s70 offen lds\n"
    "v_mfma_f32_16x16x32_bf16 v[32:35], v[200:203], a[168:171], v[32:35]\n"
    "v_mfma_f32_16x16x32_bf16 v[32:35], v[204:207], a[172:175], v[32:35]\n"
    "s_nop 1\n"
    "s_nop 0\n"
    "v_cvt_pk_bf16_f32 v78, v28, v29\n"
    "v_cvt_pk_bf16_f32 v79, v30, v31\n"
    "v_mfma_f32_16x16x32_bf16 v[32:35], v[208:211], a[176:179], v[32:35]\n"
    "buffer_load_dwordx4 v241, s[48:51], s70 offen offset:1024 lds\n"
    "ds_read_b64_tr_b16 v[182:183], v245 offset:26624\n"
    "v_mfma_f32_16x16x32_bf16 v[32:35], v[212:215], a[180:183], v[32:35]\n"
    "buffer_load_dwordx4 v242, s[48:51], s70 offen offset:2048 lds\n"
    "ds_read_b64_tr_b16 v[184:185], v245 offset:12288\n"
    "v_mfma_f32_16x16x32_bf16 v[32:35], v[216:219], a[184:187], v[32:35]\n"
    "s_add_u32 s68, s73, 0x3000\n"
    "buffer_store_dwordx4 v[76:79], v248, s[52:55], s68 offen sc1\n"
    "v_mfma_f32_16x16x32_bf16 v[32:35], v[220:223], a[188:191], v[32:35]\n"
    "buffer_load_dwordx4 v243, s[48:51], s70 offen offset:3072 lds\n"
    "s_add_u32 m0, s63, 0x1c000\n"
    "v_mfma_f32_16x16x32_bf16 v[36:39], v[192:195], a[192:195], 0\n"
    "ds_read_b64_tr_b16 v[186:187], v245 offset:14336\n"
    "s_add_u32 s64, s70, 0x9000\n"
    "v_mfma_f32_16x16x32_bf16 v[36:39], v[196:199], a[196:199], v[36:39]\n"
    "v_mfma_f32_16x16x32_bf16 v[36:39], v[200:203], a[200:203], v[36:39]\n"
    "buffer_load_dwordx4 v240, s[48:51], s64 offen lds\n"
    "ds_read_b64_tr_b16 v[188:189], v245 offset:28672\n"
    "v_mfma_f32_16x16x32_bf16 v[36:39], v[204:207], a[204:207], v[36:39]\n"
    "buffer_load_dwordx4 v241, s[48:51], s64 offen offset:1024 lds\n"
    "v_cvt_pk_bf16_f32 v80, v32, v33\n"
    "v_mfma_f32_16x16x32_bf16 v[36:39], v[208:211], a[208:211], v[36:39]\n"
    "v_mfma_f32_16x16x32_bf16 v[36:39], v[212:215], a[212:215], v[36:39]\n"
    "buffer_load_dwordx4 v242, s[48:51], s64 offen offset:2048 lds\n"
    "v_cvt_pk_bf16_f32 v81, v34, v35\n"
    "v_mfma_f32_16x16x32_bf16 v[36:39], v[216:219], a[216:219], v[36:39]\n"
    "s_waitcnt lgkmcnt(2)\n"
    "ds_read_b64_tr_b16 v[190:191], v245 offset:30720\n"
    "buffer_load_dwordx4 v243, s[48:51], s64 offen offset:3072 lds\n"
    "v_mfma_f32_16x16x32_bf16 v[36:39], v[220:223], a[220:223], v[36:39]\n"
    "s_waitcnt vmcnt(18) lgkmcnt(0)\n"
    "s_barrier\n"
    "v_mfma_f32_16x16x32_bf16 v[0:3], v[160:163], a[64:67], 0\n"
    "ds_read_b64_tr_b16 v[192:193], v246\n"
    "s_add_u32 m0, s63, 0\n"
    "v_mfma_f32_16x16x32_bf16 v[0:3], v[164:167], a[68:71], v[0:3]\n"
    "v_mfma_f32_16x16x32_bf16 v[0:3], v[168:171], a[72:75], v[0:3]\n"
    "ds_read_b64_tr_b16 v[194:195], v246 offset:2048\n"
    "v_mfma_f32_16x16x32_bf16 v[0:3], v[172:175], a[76:79], v[0:3]\n"
    "v_cvt_pk_bf16_f32 v82, v36, v37\n"
    "v_cvt_pk_bf16_f32 v83, v38, v39\n"
    "v_mfma_f32_16x16x32_bf16 v[0:3], v[176:179], a[80:83], v[0:3]\n"
    "s_add_u32 s70, s70, 0x80\n"
    "s_cmp_ge_u32 s70, s76\n"
    "s_cselect_b32 s70, s77, s70\n"
    "v_mfma_f32_16x16x32_bf16 v[0:3], v[180:183], a[84:87], v[0:3]\n"
    "ds_read_b64_tr_b16 v[196:197], v246 offset:16384\n"
    "v_mfma_f32_16x16x32_bf16 v[0:3], v[184:187], a[88:91], v[0:3]\n"
    "s_add_u32 s68, s73, 0x4000\n"
    "buffer_store_dwordx4 v[80:83], v248, s[52:55], s68 offen sc1\n"
    "v_mfma_f32_16x16x32_bf16 v[0:3], v[188:191], a[92:95], v[0:3]\n"
    "ds_read_b64_tr_b16 v[198:199], v246 offset:18432\n"
    "v_mfma_f32_16x16x32_bf16 v[4:7], v[160:163], a[96:99], 0\n"
    "v_mfma_f32_16x16x32_bf16 v[4:7], v[164:167], a[100:103], v[4:7]\n"
    "ds_read_b64_tr_b16 v[200:201], v246 offset:4096\n"
    "v_mfma_f32_16x16x32_bf16 v[4:7], v[168:171], a[104:107], v[4:7]\n"
    "v_mfma_f32_16x16x32_bf16 v[4:7], v[172:175], a[108:111], v[4:7]\n"
    "buffer_load_dwordx4 v240, s[48:51], s70 offen lds\n"
    "s_nop 0\n"
    "s_nop 0\n"
    "v_cvt_pk_bf16_f32 v84, v0, v1\n"
    "v_mfma_f32_16x16x32_bf16 v[4:7], v[176:179], a[112:115], v[4:7]\n"
    "ds_read_b64_tr_b16 v[202:203], v246 offset:6144\n"
    "v_cvt_pk_bf16_f32 v85, v2, v3\n"
    "v_mfma_f32_16x16x32_bf16 v[4:7], v[180:183], a[116:119], v[4:7]\n"
    "v_mfma_f32_16x16x32_bf16 v[4:7], v[184:187], a[120:123], v[4:7]\n"
    "buffer_load_dwordx4 v241, s[48:51], s70 offen offset:1024 lds\n"
    "ds_read_b64_tr_b16 v[204:205], v246 offset:20480\n"
    "v_mfma_f32_16x16x32_bf16 v[4:7], v[188:191], a[124:127], v[4:7]\n"
    "v_mfma_f32_16x16x32_bf16 v[8:11], v[160:163], a[128:131], 0\n"
    "v_mfma_f32_16x16x32_bf16 v[8:11], v[164:167], a[132:135], v[8:11]\n"
    "buffer_load_dwordx4 v242, s[48:51], s70 offen offset:2048 lds\n"
    "ds_read_b64_tr_b16 v[206:207], v246 offset:22528\n"
    "v_mfma_f32_16x16x32_bf16 v[8:11], v[168:171], a[136:139], v[8:11]\n"
    "v_mfma_f32_16x16x32_bf16 v[8:11], v[172:175], a[140:143], v[8:11]\n"
    "s_nop 1\n"
    "s_nop 0\n"
    "v_cvt_pk_bf16_f32 v86, v4, v5\n"
    "v_cvt_pk_bf16_f32 v87, v6, v7\n"
    "v_mfma_f32_16x16x32_bf16 v[8:11], v[176:179], a[144:147], v[8:11]\n"
    "ds_read_b64_tr_b16 v[208:209], v246 offset:8192\n"
    "buffer_load_dwordx4 v243, s[48:51], s70 offen offset:3072 lds\n"
    "v_mfma_f32_16x16x32_bf16 v[8:11], v[180:183], a[148:151], v[8:11]\n"
    "s_add_u32 s68, s73, 0x5000\n"
    "buffer_store_dwordx4 v[84:87], v248, s[52:55], s68 offen sc1\n"
    "v_mfma_f32_16x16x32_bf16 v[8:11], v[184:187], a[152:155], v[8:11]\n"
    "ds_read_b64_tr_b16 v[210:211], v246 offset:10240\n"
    "s_add_u32 m0, s63, 0x4000\n"
    "v_mfma_f32_16x16x32_bf16 v[8:11], v[188:191], a[156:159], v[8:11]\n"
    "v_mfma_f32_16x16x32_bf16 v[12:15], v[160:163], a[160:163], 0\n"
    "ds_read_b64_tr_b16 v[212:213], v246 offset:24576\n"
    "v_mfma_f32_16x16x32_bf16 v[12:15], v[164:167], a[164:167], v[12:15]\n"
    "s_add_u32 s64, s70, 0x9000\n"
    "v_mfma_f32_16x16x32_bf16 v[12:15], v[168:171], a[168:171], v[12:15]\n"
    "v_mfma_f32_16x16x32_bf16 v[12:15], v[172:175], a[172:175], v[12:15]\n"
    "ds_read_b64_tr_b16 v[214:215], v246 offset:26624\n"
    "s_nop 0\n"
    "s_nop 0\n"
    "v_cvt_pk_bf16_f32 v88, v8, v9\n"
    "v_mfma_f32_16x16x32_bf16 v[12:15], v[176:179], a[176:179], v[12:15]\n"
    "buffer_load_dwordx4 v240, s[48:51], s64 offen lds\n"
    "v_cvt_pk_bf16_f32 v89, v10, v11\n"
    "v_mfma_f32_16x16x32_bf16 v[12:15], v[180:183], a[180:183], v[12:15]\n"
    "ds_read_b64_tr_b16 v[216:217], v246 offset:12288\n"
    "v_mfma_f32_16x16x32_bf16 v[12:15], v[184:187], a[184:187], v[12:15]\n"
    "v_mfma_f32_16x16x32_bf16 v[12:15], v[188:191], a[188:191], v[12:15]\n"
    "buffer_load_dwordx4 v241, s[48:51], s64 offen offset:1024 lds\n"
    "v_mfma_f32_16x16x32_bf16 v[16:19], v[160:163], a[192:195], 0\n"
    "ds_read_b64_tr_b16 v[218:219], v246 offset:14336\n"
    "v_mfma_f32_16x16x32_bf16 v[16:19], v[164:167], a[196:199], v[16:19]\n"
    "v_mfma_f32_16x16x32_bf16 v[16:19], v[168:171], a[200:203], v[16:19]\n"
    "buffer_load_dwordx4 v242, s[48:51], s64 offen offset:2048 lds\n"
    "ds_read_b64_tr_b16 v[220:221], v246 offset:28672\n"
    "v_mfma_f32_16x16x32_bf16 v[16:19], v[172:175], a[204:207], v[16:19]\n"
    "v_cvt_pk_bf16_f32 v90, v12, v13\n"
    "v_cvt_pk_bf16_f32 v91, v14, v15\n"
    "v_mfma_f32_16x16x32_bf16 v[16:19], v[176:179], a[208:211], v[16:19]\n"
    "v_mfma_f32_16x16x32_bf16 v[16:19], v[180:183], a[212:215], v[16:19]\n"
    "s_add_u32 s68, s73, 0x6000\n"
    "buffer_store_dwordx4 v[88:91], v248, s[52:55], s68 offen sc1\n"
    "v_mfma_f32_16x16x32_bf16 v[16:19], v[184:187], a[216:219], v[16:19]\n"
    "buffer_load_dwordx4 v243, s[48:51], s64 offen offset:3072 lds\n"
    "s_waitcnt lgkmcnt(2)\n"
    "ds_read_b64_tr_b16 v[222:223], v246 offset:30720\n"
    "v_mfma_f32_16x16x32_bf16 v[16:19], v[188:191], a[220:223], v[16:19]\n"
    "s_waitcnt vmcnt(20) lgkmcnt(0)\n"
    "s_barrier\n"
    "s_nop 0\n"
    "v_mfma_f32_16x16x32_bf16 v[20:23], v[192:195], a[64:67], 0\n"
    "ds_read_b64_tr_b16 v[160:161], v244 offset:32768\n"
    "s_add_u32 m0, s63, 0x10000\n"
    "v_mfma_f32_16x16x32_bf16 v[20:23], v[196:199], a[68:71], v[20:23]\n"
    "v_mfma_f32_16x16x32_bf16 v[20:23], v[200:203], a[72:75], v[20:23]\n"
    "ds_read_b64_tr_b16 v[162:163], v244 offset:34816\n"
    "v_mfma_f32_16x16x32_bf16 v[20:23], v[204:207], a[76:79], v[20:23]\n"
    "v_cvt_pk_bf16_f32 v92, v16, v17\n"
    "v_cvt_pk_bf16_f32 v93, v18, v19\n"
    "v_mfma_f32_16x16x32_bf16 v[20:23], v[208:211], a[80:83], v[20:23]\n"
    "s_add_u32 s70, s70, 0x80\n"
    "s_cmp_ge_u32 s70, s76\n"
    "s_cselect_b32 s70, s77, s70\n"
    "v_mfma_f32_16x16x32_bf16 v[20:23], v[212:215], a[84:87], v[20:23]\n"
    "ds_read_b64_tr_b16 v[164:165], v244 offset:49152\n"
    "v_mov_b32_e64 v94, 0\n"
    "v_mfma_f32_16x16x32_bf16 v[20:23], v[216:219], a[88:91], v[20:23]\n"
    "v_mov_b32_e64 v95, 0\n"
    "v_mfma_f32_16x16x32_bf16 v[20:23], v[220:223], a[92:95], v[20:23]\n"
    "ds_read_b64_tr_b16 v[166:167], v244 offset:51200\n"
    "v_mfma_f32_16x16x32_bf16 v[24:27], v[192:195], a[96:99], 0\n"
    "s_add_u32 s68, s73, 0x7000\n"
    "buffer_store_dwordx4 v[92:95], v248, s[52:55], s68 offen sc1\n"
    "v_mfma_f32_16x16x32_bf16 v[24:27], v[196:199], a[100:103], v[24:27]\n"
    "ds_read_b64_tr_b16 v[168:169], v244 offset:36864\n"
    "v_mfma_f32_16x16x32_bf16 v[24:27], v[200:203], a[104:107], v[24:27]\n"
    "v_mfma_f32_16x16x32_bf16 v[24:27], v[204:207], a[108:111], v[24:27]\n"
    "buffer_load_dwordx4 v240, s[48:51], s70 offen lds\n"
    "v_mfma_f32_16x16x32_bf16 v[24:27], v[208:211], a[112:115], v[24:27]\n"
    "ds_read_b64_tr_b16 v[170:171], v244 offset:38912\n"
    "v_mfma_f32_16x16x32_bf16 v[24:27], v[212:215], a[116:119], v[24:27]\n"
    "v_cvt_pk_bf16_f32 v96, v20, v21\n"
    "v_mfma_f32_16x16x32_bf16 v[24:27], v[216:219], a[120:123], v[24:27]\n"
    "buffer_load_dwordx4 v241, s[48:51], s70 offen offset:1024 lds\n"
    "ds_read_b64_tr_b16 v[172:173], v244 offset:53248\n"
    "v_mfma_f32_16x16x32_bf16 v[24:27], v[220:223], a[124:127], v[24:27]\n"
    "v_cvt_pk_bf16_f32 v97, v22, v23\n"
    "v_mfma_f32_16x16x32_bf16 v[28:31], v[192:195], a[128:131], 0\n"
    "v_mfma_f32_16x16x32_bf16 v[28:31], v[196:199], a[132:135], v[28:31]\n"
    "buffer_load_dwordx4 v242, s[48:51], s70 offen offset:2048 lds\n"
    "ds_read_b64_tr_b16 v[174:175], v244 offset:55296\n"
    "v_mfma_f32_16x16x32_bf16 v[28:31], v[200:203], a[136:139], v[28:31]\n"
    "v_mfma_f32_16x16x32_bf16 v[28:31], v[204:207], a[140:143], v[28:31]\n"
    "ds_read_b64_tr_b16 v[176:177], v244 offset:40960\n"
    "v_cvt_pk_bf16_f32 v98, v24, v25\n"
    "v_mfma_f32_16x16x32_bf16 v[28:31], v[208:211], a[144:147], v[28:31]\n"
    "buffer_load_dwordx4 v243, s[48:51], s70 offen offset:3072 lds\n"
    "v_cvt_pk_bf16_f32 v99, v26, v27\n"
    "v_mfma_f32_16x16x32_bf16 v[28:31], v[212:215], a[148:151], v[28:31]\n"
    "v_mfma_f32_16x16x32_bf16 v[28:31], v[216:219], a[152:155], v[28:31]\n"
    "s_add_u32 s68, s73, 0x80000\n"
    "buffer_store_dwordx4 v[96:99], v248, s[52:55], s68 offen sc1\n"
    "v_mfma_f32_16x16x32_bf16 v[28:31], v[220:223], a[156:159], v[28:31]\n"
    "ds_read_b64_tr_b16 v[178:179], v244 offset:43008\n"
    "s_add_u32 m0, s63, 0x14000\n"
    "v_mfma_f32_16x16x32_bf16 v[32:35], v[192:195], a[160:163], 0\n"
    "ds_read_b64_tr_b16 v[180:181], v244 offset:57344\n"
    "v_mfma_f32_16x16x32_bf16 v[32:35], v[196:199], a[164:167], v[32:35]\n"
    "s_add_u32 s64, s70, 0x9000\n"
    "v_mfma_f32_16x16x32_bf16 v[32:35], v[200:203], a[168:171], v[32:35]\n"
    "v_mfma_f32_16x16x32_bf16 v[32:35], v[204:207], a[172:175], v[32:35]\n"
    "ds_read_b64_tr_b16 v[182:183], v244 offset:59392\n"
    "v_cvt_pk_bf16_f32 v100, v28, v29\n"
    "v_mfma_f32_16x16x32_bf16 v[32:35], v[208:211], a[176:179], v[32:35]\n"
    "buffer_load_dwordx4 v240, s[48:51], s64 offen lds\n"
    "v_cvt_pk_bf16_f32 v101, v30, v31\n"
    "v_mfma_f32_16x16x32_bf16 v[32:35], v[212:215], a[180:183], v[32:35]\n"
    "ds_read_b64_tr_b16 v[184:185], v244 offset:45056\n"
    "v_mfma_f32_16x16x32_bf16 v[32:35], v[216:219], a[184:187], v[32:35]\n"
    "v_mfma_f32_16x16x32_bf16 v[32:35], v[220:223], a[188:191], v[32:35]\n"
    "buffer_load_dwordx4 v241, s[48:51], s64 offen offset:1024 lds\n"
    "v_mfma_f32_16x16x32_bf16 v[36:39], v[192:195], a[192:195], 0\n"
    "ds_read_b64_tr_b16 v[186:187], v244 offset:47104\n"
    "v_mfma_f32_16x16x32_bf16 v[36:39], v[196:199], a[196:199], v[36:39]\n"
    "v_mfma_f32_16x16x32_bf16 v[36:39], v[200:203], a[200:203], v[36:39]\n"
    "buffer_load_dwordx4 v242, s[48:51], s64 offen offset:2048 lds\n"
    "ds_read_b64_tr_b16 v[188:189], v244 offset:61440\n"
    "v_mfma_f32_16x16x32_bf16 v[36:39], v[204:207], a[204:207], v[36:39]\n"
    "v_cvt_pk_bf16_f32 v102, v32, v33\n"
    "v_cvt_pk_bf16_f32 v103, v34, v35\n"
    "v_mfma_f32_16x16x32_bf16 v[36:39], v[208:211], a[208:211], v[36:39]\n"
    "v_mfma_f32_16x16x32_bf16 v[36:39], v[212:215], a[212:215], v[36:39]\n"
    "s_add_u32 s68, s73, 0x81000\n"
    "buffer_store_dwordx4 v[100:103], v248, s[52:55], s68 offen sc1\n"
    "v_mfma_f32_16x16x32_bf16 v[36:39], v[216:219], a[216:219], v[36:39]\n"
    "buffer_load_dwordx4 v243, s[48:51], s64 offen offset:3072 lds\n"
    "s_waitcnt lgkmcnt(2)\n"
    "ds_read_b64_tr_b16 v[190:191], v244 offset:63488\n"
    "v_mfma_f32_16x16x32_bf16 v[36:39], v[220:223], a[220:223], v[36:39]\n"
    "s_waitcnt vmcnt(22) lgkmcnt(0)\n"
    "s_barrier\n"
    "s_nop 0\n"
    "v_mfma_f32_16x16x32_bf16 v[0:3], v[160:163], a[64:67], 0\n"
    "ds_read_b64_tr_b16 v[192:193], v245 offset:32768\n"
    "s_add_u32 m0, s63, 0x20000\n"
    "v_mfma_f32_16x16x32_bf16 v[0:3], v[164:167], a[68:71], v[0:3]\n"
    "v_mfma_f32_16x16x32_bf16 v[0:3], v[168:171], a[72:75], v[0:3]\n"
    "ds_read_b64_tr_b16 v[194:195], v245 offset:34816\n"
    "v_mfma_f32_16x16x32_bf16 v[0:3], v[172:175], a[76:79], v[0:3]\n"
    "v_cvt_pk_bf16_f32 v104, v36, v37\n"
    "v_cvt_pk_bf16_f32 v105, v38, v39\n"
    "v_mfma_f32_16x16x32_bf16 v[0:3], v[176:179], a[80:83], v[0:3]\n"
    "s_add_u32 s70, s70, 0x80\n"
    "s_cmp_ge_u32 s70, s76\n"
    "s_cselect_b32 s70, s77, s70\n"
    "v_mfma_f32_16x16x32_bf16 v[0:3], v[180:183], a[84:87], v[0:3]\n"
    "ds_read_b64_tr_b16 v[196:197], v245 offset:49152\n"
    "v_mfma_f32_16x16x32_bf16 v[0:3], v[184:187], a[88:91], v[0:3]\n"
    "v_mfma_f32_16x16x32_bf16 v[0:3], v[188:191], a[92:95], v[0:3]\n"
    "ds_read_b64_tr_b16 v[198:199], v245 offset:51200\n"
    "v_mfma_f32_16x16x32_bf16 v[4:7], v[160:163], a[96:99], 0\n"
    "v_mfma_f32_16x16x32_bf16 v[4:7], v[164:167], a[100:103], v[4:7]\n"
    "ds_read_b64_tr_b16 v[200:201], v245 offset:36864\n"
    "v_mfma_f32_16x16x32_bf16 v[4:7], v[168:171], a[104:107], v[4:7]\n"
    "v_mfma_f32_16x16x32_bf16 v[4:7], v[172:175], a[108:111], v[4:7]\n"
    "s_nop 1\n"
    "s_nop 0\n"
    "v_cvt_pk_bf16_f32 v106, v0, v1\n"
    "v_cvt_pk_bf16_f32 v107, v2, v3\n"
    "v_mfma_f32_16x16x32_bf16 v[4:7], v[176:179], a[112:115], v[4:7]\n"
    "buffer_load_dwordx4 v240, s[48:51], s70 offen lds\n"
    "ds_read_b64_tr_b16 v[202:203], v245 offset:38912\n"
    "v_mfma_f32_16x16x32_bf16 v[4:7], v[180:183], a[116:119], v[4:7]\n"
    "s_add_u32 s68, s73, 0x82000\n"
    "buffer_store_dwordx4 v[104:107], v248, s[52:55], s68 offen sc1\n"
    "v_mfma_f32_16x16x32_bf16 v[4:7], v[184:187], a[120:123], v[4:7]\n"
    "buffer_load_dwordx4 v241, s[48:51], s70 offen offset:1024 lds\n"
    "ds_read_b64_tr_b16 v[204:205], v245 offset:53248\n"
    "v_mfma_f32_16x16x32_bf16 v[4:7], v[188:191], a[124:127], v[4:7]\n"
    "v_mfma_f32_16x16x32_bf16 v[8:11], v[160:163], a[128:131], 0\n"
    "v_mfma_f32_16x16x32_bf16 v[8:11], v[164:167], a[132:135], v[8:11]\n"
    "buffer_load_dwordx4 v242, s[48:51], s70 offen offset:2048 lds\n"
    "ds_read_b64_tr_b16 v[206:207], v245 offset:55296\n"
    "v_mfma_f32_16x16x32_bf16 v[8:11], v[168:171], a[136:139], v[8:11]\n"
    "v_mfma_f32_16x16x32_bf16 v[8:11], v[172:175], a[140:143], v[8:11]\n"
    "ds_read_b64_tr_b16 v[208:209], v245 offset:40960\n"
    "s_nop 0\n"
    "s_nop 0\n"
    "v_cvt_pk_bf16_f32 v108, v4, v5\n"
    "v_mfma_f32_16x16x32_bf16 v[8:11], v[176:179], a[144:147], v[8:11]\n"
    "buffer_load_dwordx4 v243, s[48:51], s70 offen offset:3072 lds\n"
    "v_cvt_pk_bf16_f32 v109, v6, v7\n"
    "v_mfma_f32_16x16x32_bf16 v[8:11], v[180:183], a[148:151], v[8:11]\n"
    "v_mfma_f32_16x16x32_bf16 v[8:11], v[184:187], a[152:155], v[8:11]\n"
    "ds_read_b64_tr_b16 v[210:211], v245 offset:43008\n"
    "s_add_u32 m0, s63, 0x24000\n"
    "v_mfma_f32_16x16x32_bf16 v[8:11], v[188:191], a[156:159], v[8:11]\n"
    "v_mfma_f32_16x16x32_bf16 v[12:15], v[160:163], a[160:163], 0\n"
    "ds_read_b64_tr_b16 v[212:213], v245 offset:57344\n"
    "v_mfma_f32_16x16x32_bf16 v[12:15], v[164:167], a[164:167], v[12:15]\n"
    "s_add_u32 s64, s70, 0x9000\n"
    "v_mfma_f32_16x16x32_bf16 v[12:15], v[168:171], a[168:171], v[12:15]\n"
    "v_mfma_f32_16x16x32_bf16 v[12:15], v[172:175], a[172:175], v[12:15]\n"
    "s_nop 1\n"
    "s_nop 0\n"
    "v_cvt_pk_bf16_f32 v110, v8, v9\n"
    "v_cvt_pk_bf16_f32 v111, v10, v11\n"
    "v_mfma_f32_16x16x32_bf16 v[12:15], v[176:179], a[176:179], v[12:15]\n"
    "ds_read_b64_tr_b16 v[214:215], v245 offset:59392\n"
    "buffer_load_dwordx4 v240, s[48:51], s64 offen lds\n"
    "v_mfma_f32_16x16x32_bf16 v[12:15], v[180:183], a[180:183], v[12:15]\n"
    "s_add_u32 s68, s73, 0x83000\n"
    "buffer_store_dwordx4 v[108:111], v248, s[52:55], s68 offen sc1\n"
    "v_mfma_f32_16x16x32_bf16 v[12:15], v[184:187], a[184:187], v[12:15]\n"
    "ds_read_b64_tr_b16 v[216:217], v245 offset:45056\n"
    "v_mfma_f32_16x16x32_bf16 v[12:15], v[188:191], a[188:191], v[12:15]\n"
    "buffer_load_dwordx4 v241, s[48:51], s64 offen offset:1024 lds\n"
    "v_mfma_f32_16x16x32_bf16 v[16:19], v[160:163], a[192:195], 0\n"
    "ds_read_b64_tr_b16 v[218:219], v245 offset:47104\n"
    "v_mfma_f32_16x16x32_bf16 v[16:19], v[164:167], a[196:199], v[16:19]\n"
    "v_mfma_f32_16x16x32_bf16 v[16:19], v[168:171], a[200:203], v[16:19]\n"
    "buffer_load_dwordx4 v242, s[48:51], s64 offen offset:2048 lds\n"
    "ds_read_b64_tr_b16 v[220:221], v245 offset:61440\n"
    "v_mfma_f32_16x16x32_bf16 v[16:19], v[172:175], a[204:207], v[16:19]\n"
    "v_cvt_pk_bf16_f32 v112, v12, v13\n"
    "v_mfma_f32_16x16x32_bf16 v[16:19], v[176:179], a[208:211], v[16:19]\n"
    "v_mfma_f32_16x16x32_bf16 v[16:19], v[180:183], a[212:215], v[16:19]\n"
    "buffer_load_dwordx4 v243, s[48:51], s64 offen offset:3072 lds\n"
    "v_cvt_pk_bf16_f32 v113, v14, v15\n"
    "v_mfma_f32_16x16x32_bf16 v[16:19], v[184:187], a[216:219], v[16:19]\n"
    "s_waitcnt lgkmcnt(3)\n"
    "ds_read_b64_tr_b16 v[222:223], v245 offset:63488\n"
    "v_mfma_f32_16x16x32_bf16 v[16:19], v[188:191], a[220:223], v[16:19]\n"
    "s_waitcnt vmcnt(21) lgkmcnt(0)\n"
    "s_barrier\n"
    "s_nop 0\n"
    "v_mfma_f32_16x16x32_bf16 v[20:23], v[192:195], a[64:67], 0\n"
    "ds_read_b64_tr_b16 v[160:161], v244\n"
    "v_mfma_f32_16x16x32_bf16 v[20:23], v[196:199], a[68:71], v[20:23]\n"
    "v_mfma_f32_16x16x32_bf16 v[20:23], v[200:203], a[72:75], v[20:23]\n"
    "ds_read_b64_tr_b16 v[162:163], v244 offset:2048\n"
    "v_mfma_f32_16x16x32_bf16 v[20:23], v[204:207], a[76:79], v[20:23]\n"
    "v_cvt_pk_bf16_f32 v114, v16, v17\n"
    "v_cvt_pk_bf16_f32 v115, v18, v19\n"
    "v_mfma_f32_16x16x32_bf16 v[20:23], v[208:211], a[80:83], v[20:23]\n"
    "ds_read_b64_tr_b16 v[164:165], v244 offset:16384\n"
    "v_mfma_f32_16x16x32_bf16 v[20:23], v[212:215], a[84:87], v[20:23]\n"
    "s_add_u32 s68, s73, 0x84000\n"
    "buffer_store_dwordx4 v[112:115], v248, s[52:55], s68 offen sc1\n"
    "v_mfma_f32_16x16x32_bf16 v[20:23], v[216:219], a[88:91], v[20:23]\n"
    "v_mfma_f32_16x16x32_bf16 v[20:23], v[220:223], a[92:95], v[20:23]\n"
    "ds_read_b64_tr_b16 v[166:167], v244 offset:18432\n"
    "v_mfma_f32_16x16x32_bf16 v[24:27], v[192:195], a[96:99], 0\n"
    "v_mfma_f32_16x16x32_bf16 v[24:27], v[196:199], a[100:103], v[24:27]\n"
    "ds_read_b64_tr_b16 v[168:169], v244 offset:4096\n"
    "v_mfma_f32_16x16x32_bf16 v[24:27], v[200:203], a[104:107], v[24:27]\n"
    "v_mfma_f32_16x16x32_bf16 v[24:27], v[204:207], a[108:111], v[24:27]\n"
    "s_nop 1\n"
    "s_nop 0\n"
    "v_cvt_pk_bf16_f32 v116, v20, v21\n"
    "v_mfma_f32_16x16x32_bf16 v[24:27], v[208:211], a[112:115], v[24:27]\n"
    "ds_read_b64_tr_b16 v[170:171], v244 offset:6144\n"
    "v_cvt_pk_bf16_f32 v117, v22, v23\n"
    "v_mfma_f32_16x16x32_bf16 v[24:27], v[212:215], a[116:119], v[24:27]\n"
    "v_mfma_f32_16x16x32_bf16 v[24:27], v[216:219], a[120:123], v[24:27]\n"
    "ds_read_b64_tr_b16 v[172:173], v244 offset:20480\n"
    "v_mfma_f32_16x16x32_bf16 v[24:27], v[220:223], a[124:127], v[24:27]\n"
    "v_mfma_f32_16x16x32_bf16 v[28:31], v[192:195], a[128:131], 0\n"
    "v_mfma_f32_16x16x32_bf16 v[28:31], v[196:199], a[132:135], v[28:31]\n"
    "ds_read_b64_tr_b16 v[174:175], v244 offset:22528\n"
    "v_mfma_f32_16x16x32_bf16 v[28:31], v[200:203], a[136:139], v[28:31]\n"
    "v_mfma_f32_16x16x32_bf16 v[28:31], v[204:207], a[140:143], v[28:31]\n"
    "s_nop 2\n"
    "s_nop 0\n"
    "v_cvt_pk_bf16_f32 v118, v24, v25\n"
    "v_cvt_pk_bf16_f32 v119, v26, v27\n"
    "v_mfma_f32_16x16x32_bf16 v[28:31], v[208:211], a[144:147], v[28:31]\n"
    "ds_read_b64_tr_b16 v[176:177], v244 offset:8192\n"
    "v_mfma_f32_16x16x32_bf16 v[28:31], v[212:215], a[148:151], v[28:31]\n"
    "s_add_u32 s68, s73, 0x85000\n"
    "buffer_store_dwordx4 v[116:119], v248, s[52:55], s68 offen sc1\n"
    "v_mfma_f32_16x16x32_bf16 v[28:31], v[216:219], a[152:155], v[28:31]\n"
    "ds_read_b64_tr_b16 v[178:179], v244 offset:10240\n"
    "v_mfma_f32_16x16x32_bf16 v[28:31], v[220:223], a[156:159], v[28:31]\n"
    "v_mfma_f32_16x16x32_bf16 v[32:35], v[192:195], a[160:163], 0\n"
    "ds_read_b64_tr_b16 v[180:181], v244 offset:24576\n"
    "v_mfma_f32_16x16x32_bf16 v[32:35], v[196:199], a[164:167], v[32:35]\n"
    "v_mfma_f32_16x16x32_bf16 v[32:35], v[200:203], a[168:171], v[32:35]\n"
    "v_mfma_f32_16x16x32_bf16 v[32:35], v[204:207], a[172:175], v[32:35]\n"
    "ds_read_b64_tr_b16 v[182:183], v244 offset:26624\n"
    "s_nop 1\n"
    "s_nop 0\n"
    "v_cvt_pk_bf16_f32 v120, v28, v29\n"
    "v_mfma_f32_16x16x32_bf16 v[32:35], v[208:211], a[176:179], v[32:35]\n"
    "v_cvt_pk_bf16_f32 v121, v30, v31\n"
    "v_mfma_f32_16x16x32_bf16 v[32:35], v[212:215], a[180:183], v[32:35]\n"
    "ds_read_b64_tr_b16 v[184:185], v244 offset:12288\n"
    "v_mfma_f32_16x16x32_bf16 v[32:35], v[216:219], a[184:187], v[32:35]\n"
    "v_mfma_f32_16x16x32_bf16 v[32:35], v[220:223], a[188:191], v[32:35]\n"
    "v_mfma_f32_16x16x32_bf16 v[36:39], v[192:195], a[192:195], 0\n"
    "ds_read_b64_tr_b16 v[186:187], v244 offset:14336\n"
    "v_mfma_f32_16x16x32_bf16 v[36:39], v[196:199], a[196:199], v[36:39]\n"
    "v_mfma_f32_16x16x32_bf16 v[36:39], v[200:203], a[200:203], v[36:39]\n"
    "ds_read_b64_tr_b16 v[188:189], v244 offset:28672\n"
    "v_mfma_f32_16x16x32_bf16 v[36:39], v[204:207], a[204:207], v[36:39]\n"
    "s_nop 1\n"
    "s_nop 0\n"
    "v_cvt_pk_bf16_f32 v122, v32, v33\n"
    "v_cvt_pk_bf16_f32 v123, v34, v35\n"
    "v_mfma_f32_16x16x32_bf16 v[36:39], v[208:211], a[208:211], v[36:39]\n"
    "v_mfma_f32_16x16x32_bf16 v[36:39], v[212:215], a[212:215], v[36:39]\n"
    "s_add_u32 s68, s73, 0x86000\n"
    "buffer_store_dwordx4 v[120:123], v248, s[52:55], s68 offen sc1\n"
    "v_mfma_f32_16x16x32_bf16 v[36:39], v[216:219], a[216:219], v[36:39]\n"
    "s_waitcnt lgkmcnt(3)\n"
    "ds_read_b64_tr_b16 v[190:191], v244 offset:30720\n"
    "v_mfma_f32_16x16x32_bf16 v[36:39], v[220:223], a[220:223], v[36:39]\n"
    "s_waitcnt vmcnt(13) lgkmcnt(0)\n"
    "s_barrier\n"
    "s_nop 0\n"
    "v_mfma_f32_16x16x32_bf16 v[0:3], v[160:163], a[64:67], 0\n"
    "ds_read_b64_tr_b16 v[192:193], v245\n"
    "v_mfma_f32_16x16x32_bf16 v[0:3], v[164:167], a[68:71], v[0:3]\n"
    "v_mfma_f32_16x16x32_bf16 v[0:3], v[168:171], a[72:75], v[0:3]\n"
    "ds_read_b64_tr_b16 v[194:195], v245 offset:2048\n"
    "v_mfma_f32_16x16x32_bf16 v[0:3], v[172:175], a[76:79], v[0:3]\n"
    "v_cvt_pk_bf16_f32 v124, v36, v37\n"
    "v_cvt_pk_bf16_f32 v125, v38, v39\n"
    "v_mfma_f32_16x16x32_bf16 v[0:3], v[176:179], a[80:83], v[0:3]\n"
    "ds_read_b64_tr_b16 v[196:197], v245 offset:16384\n"
    "v_mov_b32_e64 v126, 0\n"
    "v_mfma_f32_16x16x32_bf16 v[0:3], v[180:183], a[84:87], v[0:3]\n"
    "v_mfma_f32_16x16x32_bf16 v[0:3], v[184:187], a[88:91], v[0:3]\n"
    "v_mov_b32_e64 v127, 0\n"
    "v_mfma_f32_16x16x32_bf16 v[0:3], v[188:191], a[92:95], v[0:3]\n"
    "ds_read_b64_tr_b16 v[198:199], v245 offset:18432\n"
    "v_mfma_f32_16x16x32_bf16 v[4:7], v[160:163], a[96:99], 0\n"
    "s_add_u32 s68, s73, 0x87000\n"
    "buffer_store_dwordx4 v[124:127], v248, s[52:55], s68 offen sc1\n"
    "v_mfma_f32_16x16x32_bf16 v[4:7], v[164:167], a[100:103], v[4:7]\n"
    "ds_read_b64_tr_b16 v[200:201], v245 offset:4096\n"
    "v_mfma_f32_16x16x32_bf16 v[4:7], v[168:171], a[104:107], v[4:7]\n"
    "v_mfma_f32_16x16x32_bf16 v[4:7], v[172:175], a[108:111], v[4:7]\n"
    "v_mfma_f32_16x16x32_bf16 v[4:7], v[176:179], a[112:115], v[4:7]\n"
    "ds_read_b64_tr_b16 v[202:203], v245 offset:6144\n"
    "v_mfma_f32_16x16x32_bf16 v[4:7], v[180:183], a[116:119], v[4:7]\n"
    "v_cvt_pk_bf16_f32 v64, v0, v1\n"
    "v_mfma_f32_16x16x32_bf16 v[4:7], v[184:187], a[120:123], v[4:7]\n"
    "ds_read_b64_tr_b16 v[204:205], v245 offset:20480\n"
    "v_mfma_f32_16x16x32_bf16 v[4:7], v[188:191], a[124:127], v[4:7]\n"
    "v_cvt_pk_bf16_f32 v65, v2, v3\n"
    "v_mfma_f32_16x16x32_bf16 v[8:11], v[160:163], a[128:131], 0\n"
    "v_mfma_f32_16x16x32_bf16 v[8:11], v[164:167], a[132:135], v[8:11]\n"
    "ds_read_b64_tr_b16 v[206:207], v245 offset:22528\n"
    "v_mfma_f32_16x16x32_bf16 v[8:11], v[168:171], a[136:139], v[8:11]\n"
    "v_mfma_f32_16x16x32_bf16 v[8:11], v[172:175], a[140:143], v[8:11]\n"
    "ds_read_b64_tr_b16 v[208:209], v245 offset:8192\n"
    "s_nop 0\n"
    "s_nop 0\n"
    "v_cvt_pk_bf16_f32 v66, v4, v5\n"
    "v_mfma_f32_16x16x32_bf16 v[8:11], v[176:179], a[144:147], v[8:11]\n"
    "v_cvt_pk_bf16_f32 v67, v6, v7\n"
    "v_mfma_f32_16x16x32_bf16 v[8:11], v[180:183], a[148:151], v[8:11]\n"
    "v_mfma_f32_16x16x32_bf16 v[8:11], v[184:187], a[152:155], v[8:11]\n"
    "s_add_u32 s68, s73, 0x100000\n"
    "buffer_store_dwordx4 v[64:67], v248, s[52:55], s68 offen sc1\n"
    "v_mfma_f32_16x16x32_bf16 v[8:11], v[188:191], a[156:159], v[8:11]\n"
    "ds_read_b64_tr_b16 v[210:211], v245 offset:10240\n"
    "v_mfma_f32_16x16x32_bf16 v[12:15], v[160:163], a[160:163], 0\n"
    "ds_read_b64_tr_b16 v[212:213], v245 offset:24576\n"
    "v_mfma_f32_16x16x32_bf16 v[12:15], v[164:167], a[164:167], v[12:15]\n"
    "v_mfma_f32_16x16x32_bf16 v[12:15], v[168:171], a[168:171], v[12:15]\n"
    "v_mfma_f32_16x16x32_bf16 v[12:15], v[172:175], a[172:175], v[12:15]\n"
    "ds_read_b64_tr_b16 v[214:215], v245 offset:26624\n"
    "s_nop 0\n"
    "s_nop 0\n"
    "v_cvt_pk_bf16_f32 v68, v8, v9\n"
    "v_mfma_f32_16x16x32_bf16 v[12:15], v[176:179], a[176:179], v[12:15]\n"
    "v_cvt_pk_bf16_f32 v69, v10, v11\n"
    "v_mfma_f32_16x16x32_bf16 v[12:15], v[180:183], a[180:183], v[12:15]\n"
    "ds_read_b64_tr_b16 v[216:217], v245 offset:12288\n"
    "v_mfma_f32_16x16x32_bf16 v[12:15], v[184:187], a[184:187], v[12:15]\n"
    "v_mfma_f32_16x16x32_bf16 v[12:15], v[188:191], a[188:191], v[12:15]\n"
    "v_mfma_f32_16x16x32_bf16 v[16:19], v[160:163], a[192:195], 0\n"
    "ds_read_b64_tr_b16 v[218:219], v245 offset:14336\n"
    "v_mfma_f32_16x16x32_bf16 v[16:19], v[164:167], a[196:199], v[16:19]\n"
    "v_mfma_f32_16x16x32_bf16 v[16:19], v[168:171], a[200:203], v[16:19]\n"
    "ds_read_b64_tr_b16 v[220:221], v245 offset:28672\n"
    "v_mfma_f32_16x16x32_bf16 v[16:19], v[172:175], a[204:207], v[16:19]\n"
    "s_nop 1\n"
    "s_nop 0\n"
    "v_cvt_pk_bf16_f32 v70, v12, v13\n"
    "v_cvt_pk_bf16_f32 v71, v14, v15\n"
    "v_mfma_f32_16x16x32_bf16 v[16:19], v[176:179], a[208:211], v[16:19]\n"
    "v_mfma_f32_16x16x32_bf16 v[16:19], v[180:183], a[212:215], v[16:19]\n"
    "s_add_u32 s68, s73, 0x101000\n"
    "buffer_store_dwordx4 v[68:71], v248, s[52:55], s68 offen sc1\n"
    "v_mfma_f32_16x16x32_bf16 v[16:19], v[184:187], a[216:219], v[16:19]\n"
    "s_waitcnt lgkmcnt(3)\n"
    "ds_read_b64_tr_b16 v[222:223], v245 offset:30720\n"
    "v_mfma_f32_16x16x32_bf16 v[16:19], v[188:191], a[220:223], v[16:19]\n"
    "s_waitcnt vmcnt(6) lgkmcnt(0)\n"
    "s_barrier\n"
    "s_nop 0\n"
    "v_mfma_f32_16x16x32_bf16 v[20:23], v[192:195], a[64:67], 0\n"
    "ds_read_b64_tr_b16 v[160:161], v246\n"
    "v_mfma_f32_16x16x32_bf16 v[20:23], v[196:199], a[68:71], v[20:23]\n"
    "v_mfma_f32_16x16x32_bf16 v[20:23], v[200:203], a[72:75], v[20:23]\n"
    "ds_read_b64_tr_b16 v[162:163], v246 offset:2048\n"
    "v_mfma_f32_16x16x32_bf16 v[20:23], v[204:207], a[76:79], v[20:23]\n"
    "v_cvt_pk_bf16_f32 v72, v16, v17\n"
    "v_cvt_pk_bf16_f32 v73, v18, v19\n"
    "v_mfma_f32_16x16x32_bf16 v[20:23], v[208:211], a[80:83], v[20:23]\n"
    "ds_read_b64_tr_b16 v[164:165], v246 offset:16384\n"
    "v_mfma_f32_16x16x32_bf16 v[20:23], v[212:215], a[84:87], v[20:23]\n"
    "v_mfma_f32_16x16x32_bf16 v[20:23], v[216:219], a[88:91], v[20:23]\n"
    "v_mfma_f32_16x16x32_bf16 v[20:23], v[220:223], a[92:95], v[20:23]\n"
    "ds_read_b64_tr_b16 v[166:167], v246 offset:18432\n"
    "v_mfma_f32_16x16x32_bf16 v[24:27], v[192:195], a[96:99], 0\n"
    "v_mfma_f32_16x16x32_bf16 v[24:27], v[196:199], a[100:103], v[24:27]\n"
    "ds_read_b64_tr_b16 v[168:169], v246 offset:4096\n"
    "v_mfma_f32_16x16x32_bf16 v[24:27], v[200:203], a[104:107], v[24:27]\n"
    "v_mfma_f32_16x16x32_bf16 v[24:27], v[204:207], a[108:111], v[24:27]\n"
    "s_nop 1\n"
    "s_nop 0\n"
    "v_cvt_pk_bf16_f32 v74, v20, v21\n"
    "v_cvt_pk_bf16_f32 v75, v22, v23\n"
    "v_mfma_f32_16x16x32_bf16 v[24:27], v[208:211], a[112:115], v[24:27]\n"
    "ds_read_b64_tr_b16 v[170:171], v246 offset:6144\n"
    "v_mfma_f32_16x16x32_bf16 v[24:27], v[212:215], a[116:119], v[24:27]\n"
    "s_add_u32 s68, s73, 0x102000\n"
    "buffer_store_dwordx4 v[72:75], v248, s[52:55], s68 offen sc1\n"
    "v_mfma_f32_16x16x32_bf16 v[24:27], v[216:219], a[120:123], v[24:27]\n"
    "ds_read_b64_tr_b16 v[172:173], v246 offset:20480\n"
    "v_mfma_f32_16x16x32_bf16 v[24:27], v[220:223], a[124:127], v[24:27]\n"
    "v_mfma_f32_16x16x32_bf16 v[28:31], v[192:195], a[128:131], 0\n"
    "v_mfma_f32_16x16x32_bf16 v[28:31], v[196:199], a[132:135], v[28:31]\n"
    "ds_read_b64_tr_b16 v[174:175], v246 offset:22528\n"
    "v_mfma_f32_16x16x32_bf16 v[28:31], v[200:203], a[136:139], v[28:31]\n"
    "v_mfma_f32_16x16x32_bf16 v[28:31], v[204:207], a[140:143], v[28:31]\n"
    "ds_read_b64_tr_b16 v[176:177], v246 offset:8192\n"
    "s_nop 1\n"
    "s_nop 0\n"
    "v_cvt_pk_bf16_f32 v76, v24, v25\n"
    "v_mfma_f32_16x16x32_bf16 v[28:31], v[208:211], a[144:147], v[28:31]\n"
    "v_cvt_pk_bf16_f32 v77, v26, v27\n"
    "v_mfma_f32_16x16x32_bf16 v[28:31], v[212:215], a[148:151], v[28:31]\n"
    "v_mfma_f32_16x16x32_bf16 v[28:31], v[216:219], a[152:155], v[28:31]\n"
    "ds_read_b64_tr_b16 v[178:179], v246 offset:10240\n"
    "v_mfma_f32_16x16x32_bf16 v[28:31], v[220:223], a[156:159], v[28:31]\n"
    "v_mfma_f32_16x16x32_bf16 v[32:35], v[192:195], a[160:163], 0\n"
    "ds_read_b64_tr_b16 v[180:181], v246 offset:24576\n"
    "v_mfma_f32_16x16x32_bf16 v[32:35], v[196:199], a[164:167], v[32:35]\n"
    "v_mfma_f32_16x16x32_bf16 v[32:35], v[200:203], a[168:171], v[32:35]\n"
    "v_mfma_f32_16x16x32_bf16 v[32:35], v[204:207], a[172:175], v[32:35]\n"
    "s_nop 2\n"
    "s_nop 0\n"
    "v_cvt_pk_bf16_f32 v78, v28, v29\n"
    "v_cvt_pk_bf16_f32 v79, v30, v31\n"
    "v_mfma_f32_16x16x32_bf16 v[32:35], v[208:211], a[176:179], v[32:35]\n"
    "ds_read_b64_tr_b16 v[182:183], v246 offset:26624\n"
    "v_mfma_f32_16x16x32_bf16 v[32:35], v[212:215], a[180:183], v[32:35]\n"
    "s_add_u32 s68, s73, 0x103000\n"
    "buffer_store_dwordx4 v[76:79], v248, s[52:55], s68 offen sc1\n"
    "v_mfma_f32_16x16x32_bf16 v[32:35], v[216:219], a[184:187], v[32:35]\n"
    "ds_read_b64_tr_b16 v[184:185], v246 offset:12288\n"
    "v_mfma_f32_16x16x32_bf16 v[32:35], v[220:223], a[188:191], v[32:35]\n"
    "v_mfma_f32_16x16x32_bf16 v[36:39], v[192:195], a[192:195], 0\n"
    "ds_read_b64_tr_b16 v[186:187], v246 offset:14336\n"
    "v_mfma_f32_16x16x32_bf16 v[36:39], v[196:199], a[196:199], v[36:39]\n"
    "v_mfma_f32_16x16x32_bf16 v[36:39], v[200:203], a[200:203], v[36:39]\n"
    "ds_read_b64_tr_b16 v[188:189], v246 offset:28672\n"
    "v_mfma_f32_16x16x32_bf16 v[36:39], v[204:207], a[204:207], v[36:39]\n"
    "s_nop 1\n"
    "v_cvt_pk_bf16_f32 v80, v32, v33\n"
    "v_mfma_f32_16x16x32_bf16 v[36:39], v[208:211], a[208:211], v[36:39]\n"
    "v_mfma_f32_16x16x32_bf16 v[36:39], v[212:215], a[212:215], v[36:39]\n"
    "s_waitcnt lgkmcnt(4)\n"
    "ds_read_b64_tr_b16 v[190:191], v246 offset:30720\n"
    "v_cvt_pk_bf16_f32 v81, v34, v35\n"
    "v_mfma_f32_16x16x32_bf16 v[36:39], v[216:219], a[216:219], v[36:39]\n"
    "v_mfma_f32_16x16x32_bf16 v[36:39], v[220:223], a[220:223], v[36:39]\n"
    "s_waitcnt vmcnt(4) lgkmcnt(0)\n"
    "s_barrier\n"
    "s_cmp_lg_u32 s60, 0\n"
    "s_cbranch_scc1 L_nopub1\n"
    "s_mov_b64 exec, 0xff\n"
    "buffer_store_dwordx4 v[56:59], v155, s[52:55], s75 offen offset:128 sc1\n"
    "s_mov_b64 exec, -1\n"
    "L_nopub1:\n"
    "s_nop 0\n"
    "v_mfma_f32_16x16x32_bf16 v[0:3], v[160:163], a[64:67], 0\n"
    "v_mfma_f32_16x16x32_bf16 v[0:3], v[164:167], a[68:71], v[0:3]\n"
    "v_mfma_f32_16x16x32_bf16 v[0:3], v[168:171], a[72:75], v[0:3]\n"
    "v_mfma_f32_16x16x32_bf16 v[0:3], v[172:175], a[76:79], v[0:3]\n"
    "v_cvt_pk_bf16_f32 v82, v36, v37\n"
    "v_cvt_pk_bf16_f32 v83, v38, v39\n"
    "v_mfma_f32_16x16x32_bf16 v[0:3], v[176:179], a[80:83], v[0:3]\n"
    "v_mfma_f32_16x16x32_bf16 v[0:3], v[180:183], a[84:87], v[0:3]\n"
    "s_add_u32 s68, s73, 0x104000\n"
    "buffer_store_dwordx4 v[80:83], v248, s[52:55], s68 offen sc1\n"
    "v_mfma_f32_16x16x32_bf16 v[0:3], v[184:187], a[88:91], v[0:3]\n"
    "v_mfma_f32_16x16x32_bf16 v[0:3], v[188:191], a[92:95], v[0:3]\n"
    "v_mfma_f32_16x16x32_bf16 v[4:7], v[160:163], a[96:99], 0\n"
    "v_mfma_f32_16x16x32_bf16 v[4:7], v[164:167], a[100:103], v[4:7]\n"
    "v_mfma_f32_16x16x32_bf16 v[4:7], v[168:171], a[104:107], v[4:7]\n"
    "v_mfma_f32_16x16x32_bf16 v[4:7], v[172:175], a[108:111], v[4:7]\n"
    "s_nop 3\n"
    "s_nop 0\n"
    "v_cvt_pk_bf16_f32 v84, v0, v1\n"
    "v_mfma_f32_16x16x32_bf16 v[4:7], v[176:179], a[112:115], v[4:7]\n"
    "v_cvt_pk_bf16_f32 v85, v2, v3\n"
    "v_mfma_f32_16x16x32_bf16 v[4:7], v[180:183], a[116:119], v[4:7]\n"
    "v_mfma_f32_16x16x32_bf16 v[4:7], v[184:187], a[120:123], v[4:7]\n"
    "v_mfma_f32_16x16x32_bf16 v[4:7], v[188:191], a[124:127], v[4:7]\n"
    "v_mfma_f32_16x16x32_bf16 v[8:11], v[160:163], a[128:131], 0\n"
    "v_mfma_f32_16x16x32_bf16 v[8:11], v[164:167], a[132:135], v[8:11]\n"
    "v_mfma_f32_16x16x32_bf16 v[8:11], v[168:171], a[136:139], v[8:11]\n"
    "v_mfma_f32_16x16x32_bf16 v[8:11], v[172:175], a[140:143], v[8:11]\n"
    "s_nop 3\n"
    "s_nop 0\n"
    "v_cvt_pk_bf16_f32 v86, v4, v5\n"
    "v_cvt_pk_bf16_f32 v87, v6, v7\n"
    "v_mfma_f32_16x16x32_bf16 v[8:11], v[176:179], a[144:147], v[8:11]\n"
    "v_mfma_f32_16x16x32_bf16 v[8:11], v[180:183], a[148:151], v[8:11]\n"
    "s_add_u32 s68, s73, 0x105000\n"
    "buffer_store_dwordx4 v[84:87], v248, s[52:55], s68 offen sc1\n"
    "v_mfma_f32_16x16x32_bf16 v[8:11], v[184:187], a[152:155], v[8:11]\n"
    "v_mfma_f32_16x16x32_bf16 v[8:11], v[188:191], a[156:159], v[8:11]\n"
    "v_mfma_f32_16x16x32_bf16 v[12:15], v[160:163], a[160:163], 0\n"
    "v_mfma_f32_16x16x32_bf16 v[12:15], v[164:167], a[164:167], v[12:15]\n"
    "v_mfma_f32_16x16x32_bf16 v[12:15], v[168:171], a[168:171], v[12:15]\n"
    "v_mfma_f32_16x16x32_bf16 v[12:15], v[172:175], a[172:175], v[12:15]\n"
    "s_nop 3\n"
    "s_nop 0\n"
    "v_cvt_pk_bf16_f32 v88, v8, v9\n"
    "v_cvt_pk_bf16_f32 v89, v10, v11\n"
    "v_mfma_f32_16x16x32_bf16 v[12:15], v[176:179], a[176:179], v[12:15]\n"
    "v_mfma_f32_16x16x32_bf16 v[12:15], v[180:183], a[180:183], v[12:15]\n"
    "v_mfma_f32_16x16x32_bf16 v[12:15], v[184:187], a[184:187], v[12:15]\n"
    "v_mfma_f32_16x16x32_bf16 v[12:15], v[188:191], a[188:191], v[12:15]\n"
    "v_mfma_f32_16x16x32_bf16 v[16:19], v[160:163], a[192:195], 0\n"
    "v_mfma_f32_16x16x32_bf16 v[16:19], v[164:167], a[196:199], v[16:19]\n"
    "v_mfma_f32_16x16x32_bf16 v[16:19], v[168:171], a[200:203], v[16:19]\n"
    "v_mfma_f32_16x16x32_bf16 v[16:19], v[172:175], a[204:207], v[16:19]\n"
    "s_nop 3\n"
    "s_nop 0\n"
    "v_cvt_pk_bf16_f32 v90, v12, v13\n"
    "v_cvt_pk_bf16_f32 v91, v14, v15\n"
    "v_mfma_f32_16x16x32_bf16 v[16:19], v[176:179], a[208:211], v[16:19]\n"
    "v_mfma_f32_16x16x32_bf16 v[16:19], v[180:183], a[212:215], v[16:19]\n"
    "s_add_u32 s68, s73, 0x106000\n"
    "buffer_store_dwordx4 v[88:91], v248, s[52:55], s68 offen sc1\n"
    "v_mfma_f32_16x16x32_bf16 v[16:19], v[184:187], a[216:219], v[16:19]\n"
    "v_mfma_f32_16x16x32_bf16 v[16:19], v[188:191], a[220:223], v[16:19]\n"
    "s_nop 7\n"
    "v_cvt_pk_bf16_f32 v92, v16, v17\n"
    "v_cvt_pk_bf16_f32 v93, v18, v19\n"
    "v_mov_b32_e32 v94, 0\n"
    "v_mov_b32_e64 v95, 0\n"
    "s_add_u32 s68, s73, 0x107000\n"
    "buffer_store_dwordx4 v[92:95], v248, s[52:55], s68 offen sc1\n"
    "s_waitcnt vmcnt(0)\n"
    "s_barrier\n"
    "s_cmp_lg_u32 s60, 0\n"
    "s_cbranch_scc1 L_nopub2\n"
    "s_mov_b64 exec, 0xff\n"
    "buffer_store_dwordx4 v[56:59], v155, s[52:55], s75 offen offset:256 sc1\n"
    "s_mov_b64 exec, -1\n"
    "L_nopub2:\n"
    "s_mov_b32 s67, 0x1800000\n"
    "buffer_load_dwordx2 v[50:51], v250, s[52:55], s67 offen offset:128 sc1\n"
    "s_mov_b32 s71, 0\n"
    "s_waitcnt vmcnt(0)\n"
    "L_poll1:\n"
    "v_xor_b32_e32 v50, s96, v50\n"
    "v_xor_b32_e64 v51, s97, v51\n"
    "v_or_b32_e64 v50, v50, v51\n"
    "v_cmp_ne_u32_e32 vcc, 0, v50\n"
    "s_add_u32 s71, s71, 1\n"
    "s_mov_b32 s67, 0x1800000\n"
    "s_cbranch_vccz L_poll1x\n"
    "s_sleep 4\n"
    "buffer_load_dwordx2 v[50:51], v250, s[52:55], s67 offen offset:128 sc1\n"
    "s_cmp_lt_u32 s71, 0x40000\n"
    "s_waitcnt vmcnt(0)\n"
    "s_cbranch_scc1 L_poll1\n"
    "L_poll1x:\n"
    "s_add_u32 s67, s74, 0\n"
    "s_nop 0\n"
    "buffer_load_dwordx4 a[224:227], v249, s[52:55], s67 offen sc1\n"
    "s_add_u32 s67, s74, 0x10000\n"
    "buffer_load_dwordx4 a[228:231], v249, s[52:55], s67 offen sc1\n"
    "s_add_u32 s67, s74, 0x20000\n"
    "buffer_load_dwordx4 a[232:235], v249, s[52:55], s67 offen sc1\n"
    "s_add_u32 s67, s74, 0x30000\n"
    "buffer_load_dwordx4 a[236:239], v249, s[52:55], s67 offen sc1\n"
    "s_add_u32 s67, s74, 0x40000\n"
    "buffer_load_dwordx4 a[240:243], v249, s[52:55], s67 offen sc1\n"
    "s_add_u32 s67, s74, 0x50000\n"
    "buffer_load_dwordx4 a[244:247], v249, s[52:55], s67 offen sc1\n"
    "s_add_u32 s67, s74, 0x60000\n"
    "buffer_load_dwordx4 a[248:251], v249, s[52:55], s67 offen sc1\n"
    "s_add_u32 s67, s74, 0x70000\n"
    "buffer_load_dwordx4 a[252:255], v249, s[52:55], s67 offen sc1\n"
    "s_add_u32 s67, s74, 0x80000\n"
    "buffer_load_dwordx4 a[0:3], v249, s[52:55], s67 offen sc1\n"
    "s_add_u32 s67, s74, 0x90000\n"
    "buffer_load_dwordx4 a[4:7], v249, s[52:55], s67 offen sc1\n"
    "s_add_u32 s67, s74, 0xa0000\n"
    "buffer_load_dwordx4 a[8:11], v249, s[52:55], s67 offen sc1\n"
    "s_add_u32 s67, s74, 0xb0000\n"
    "buffer_load_dwordx4 a[12:15], v249, s[52:55], s67 offen sc1\n"
    "s_add_u32 s67, s74, 0xc0000\n"
    "buffer_load_dwordx4 a[16:19], v249, s[52:55], s67 offen sc1\n"
    "s_add_u32 s67, s74, 0xd0000\n"
    "buffer_load_dwordx4 a[20:23], v249, s[52:55], s67 offen sc1\n"
    "s_add_u32 s67, s74, 0xe0000\n"
    "buffer_load_dwordx4 a[24:27], v249, s[52:55], s67 offen sc1\n"
    "s_add_u32 s67, s74, 0xf0000\n"
    "buffer_load_dwordx4 a[28:31], v249, s[52:55], s67 offen sc1\n"
    "s_mov_b32 s67, 0x1800000\n"
    "buffer_load_dwordx2 v[52:53], v250, s[52:55], s67 offen offset:256 sc1\n"
    "s_and_b32 s67, %7, 15\n"
    "s_or_b32 s67, s67, s60\n"
    "s_cmp_lg_u32 s67, 0\n"
    "s_cbranch_scc1 L_noep\n"
    "s_add_u32 s67, s75, 0x180\n"
    "s_mov_b64 exec, 0xff\n"
    "buffer_store_dwordx4 v[56:59], v155, s[52:55], s67 offen sc1\n"
    "s_mov_b64 exec, -1\n"
    "L_noep:\n"
    "s_mov_b32 s71, 0\n"
    "s_waitcnt vmcnt(0)\n"
    "L_poll2:\n"
    "v_xor_b32_e32 v52, s96, v52\n"
    "v_xor_b32_e64 v53, s97, v53\n"
    "v_or_b32_e64 v52, v52, v53\n"
    "v_cmp_ne_u32_e32 vcc, 0, v52\n"
    "s_add_u32 s71, s71, 1\n"
    "s_mov_b32 s67, 0x1800000\n"
    "s_cbranch_vccz L_poll2x\n"
    "s_sleep 4\n"
    "buffer_load_dwordx2 v[52:53], v250, s[52:55], s67 offen offset:256 sc1\n"
    "s_cmp_lt_u32 s71, 0x40000\n"
    "s_waitcnt vmcnt(0)\n"
    "s_cbranch_scc1 L_poll2\n"
    "L_poll2x:\n"
    "s_add_u32 s67, s74, 0x100000\n"
    "buffer_load_dwordx4 a[32:35], v249, s[52:55], s67 offen sc1\n"
    "s_add_u32 s67, s74, 0x110000\n"
    "buffer_load_dwordx4 a[36:39], v249, s[52:55], s67 offen sc1\n"
    "s_add_u32 s67, s74, 0x120000\n"
    "buffer_load_dwordx4 a[40:43], v249, s[52:55], s67 offen sc1\n"
    "s_add_u32 s67, s74, 0x130000\n"
    "buffer_load_dwordx4 a[44:47], v249, s[52:55], s67 offen sc1\n"
    "s_add_u32 s67, s74, 0x140000\n"
    "buffer_load_dwordx4 a[48:51], v249, s[52:55], s67 offen sc1\n"
    "s_add_u32 s67, s74, 0x150000\n"
    "buffer_load_dwordx4 a[52:55], v249, s[52:55], s67 offen sc1\n"
    "s_add_u32 s67, s74, 0x160000\n"
    "buffer_load_dwordx4 a[56:59], v249, s[52:55], s67 offen sc1\n"
    "s_add_u32 s67, s74, 0x170000\n"
    "buffer_load_dwordx4 a[60:63], v249, s[52:55], s67 offen sc1\n"
    "v_mfma_f32_16x16x32_bf16 v[228:231], v[224:227], a[224:227], 0\n"
    "v_mfma_f32_16x16x32_bf16 v[228:231], v[224:227], a[228:231], v[228:231]\n"
    "v_mfma_f32_16x16x32_bf16 v[228:231], v[224:227], a[232:235], v[228:231]\n"
    "v_mfma_f32_16x16x32_bf16 v[228:231], v[224:227], a[236:239], v[228:231]\n"
    "v_mfma_f32_16x16x32_bf16 v[228:231], v[224:227], a[240:243], v[228:231]\n"
    "v_mfma_f32_16x16x32_bf16 v[228:231], v[224:227], a[244:247], v[228:231]\n"
    "v_mfma_f32_16x16x32_bf16 v[228:231], v[224:227], a[248:251], v[228:231]\n"
    "v_mfma_f32_16x16x32_bf16 v[228:231], v[224:227], a[252:255], v[228:231]\n"
    "v_mfma_f32_16x16x32_bf16 v[232:235], v[224:227], a[0:3], 0\n"
    "v_mfma_f32_16x16x32_bf16 v[232:235], v[224:227], a[4:7], v[232:235]\n"
    "v_mfma_f32_16x16x32_bf16 v[232:235], v[224:227], a[8:11], v[232:235]\n"
    "v_mfma_f32_16x16x32_bf16 v[232:235], v[224:227], a[12:15], v[232:235]\n"
    "v_mfma_f32_16x16x32_bf16 v[232:235], v[224:227], a[16:19], v[232:235]\n"
    "v_mfma_f32_16x16x32_bf16 v[232:235], v[224:227], a[20:23], v[232:235]\n"
    "v_mfma_f32_16x16x32_bf16 v[232:235], v[224:227], a[24:27], v[232:235]\n"
    "v_mfma_f32_16x16x32_bf16 v[232:235], v[224:227], a[28:31], v[232:235]\n"
    "v_cvt_pk_bf16_f32 v228, v228, v229\n"
    "v_cvt_pk_bf16_f32 v229, v230, v231\n"
    "buffer_store_dwordx2 v[228:229], v251, s[44:47], 0 offen\n"
    "s_nop 4\n"
    "v_cvt_pk_bf16_f32 v232, v232, v233\n"
    "v_cvt_pk_bf16_f32 v233, v234, v235\n"
    "buffer_store_dwordx2 v[232:233], v252, s[44:47], 0 offen\n"
    "s_waitcnt vmcnt(9)\n"
    "v_mfma_f32_16x16x32_bf16 v[236:239], v[224:227], a[32:35], 0\n"
    "s_waitcnt vmcnt(8)\n"
    "v_mfma_f32_16x16x32_bf16 v[236:239], v[224:227], a[36:39], v[236:239]\n"
    "s_waitcnt vmcnt(7)\n"
    "v_mfma_f32_16x16x32_bf16 v[236:239], v[224:227], a[40:43], v[236:239]\n"
    "s_waitcnt vmcnt(6)\n"
    "v_mfma_f32_16x16x32_bf16 v[236:239], v[224:227], a[44:47], v[236:239]\n"
    "s_waitcnt vmcnt(5)\n"
    "v_mfma_f32_16x16x32_bf16 v[236:239], v[224:227], a[48:51], v[236:239]\n"
    "s_waitcnt vmcnt(4)\n"
    "v_mfma_f32_16x16x32_bf16 v[236:239], v[224:227], a[52:55], v[236:239]\n"
    "s_waitcnt vmcnt(3)\n"
    "v_mfma_f32_16x16x32_bf16 v[236:239], v[224:227], a[56:59], v[236:239]\n"
    "s_waitcnt vmcnt(2)\n"
    "v_mfma_f32_16x16x32_bf16 v[236:239], v[224:227], a[60:63], v[236:239]\n"
    "s_nop 7\n"
    "v_cvt_pk_bf16_f32 v236, v236, v237\n"
    "v_cvt_pk_bf16_f32 v237, v238, v239\n"
    "buffer_store_dwordx2 v[236:237], v253, s[44:47], 0 offen\n"
    "s_branch L_end\n"
    "L_hot_end:\n"
    "L_fallback:\n"
    "s_waitcnt vmcnt(0) lgkmcnt(0)\n"
    "s_barrier\n"
    "s_mov_b32 s72, 1\n"
    "s_mov_b32 s71, 0\n"
    "s_mov_b32 s64, s63\n"
    "v_mov_b32_e32 v0, 0xff800000\n"
    "v_mov_b32_e32 v1, 0xff800000\n"
    "v_mov_b32_e32 v2, 0xff800000\n"
    "L_fbloop:\n"
    "s_add_u32 m0, s63, 0\n"
    "s_nop 0\n"
    "buffer_load_dwordx4 v152, s[36:39], s64 offen nt lds\n"
    "buffer_load_dwordx4 v153, s[36:39], s64 offen offset:1024 nt lds\n"
    "buffer_load_dwordx4 v152, s[36:39], s64 offen offset:2048 nt lds\n"
    "buffer_load_dwordx4 v153, s[36:39], s64 offen offset:3072 nt lds\n"
    "s_waitcnt vmcnt(0)\n"
    "s_barrier\n"
    "ds_read_b128 a[64:67], v140\n"
    "ds_read_b128 a[80:83], v140 offset:4096\n"
    "ds_read_b128 a[68:71], v141\n"
    "ds_read_b128 a[84:87], v141 offset:4096\n"
    "ds_read_b128 a[72:75], v140 offset:1024\n"
    "ds_read_b128 a[88:91], v140 offset:5120\n"
    "ds_read_b128 a[76:79], v141 offset:1024\n"
    "ds_read_b128 a[92:95], v141 offset:5120\n"
    "s_waitcnt lgkmcnt(0)\n"
    "s_nop 0\n"
    "ds_read_b128 a[96:99], v142\n"
    "ds_read_b128 a[112:115], v142 offset:4096\n"
    "ds_read_b128 a[100:103], v143\n"
    "ds_read_b128 a[116:119], v143 offset:4096\n"
    "ds_read_b128 a[104:107], v142 offset:1024\n"
    "ds_read_b128 a[120:123], v142 offset:5120\n"
    "ds_read_b128 a[108:111], v143 offset:1024\n"
    "ds_read_b128 a[124:127], v143 offset:5120\n"
    "s_waitcnt lgkmcnt(0)\n"
    "s_barrier\n"
    "v_mfma_f32_16x16x32_bf16 v[96:99], a[64:67], a[0:3], 0\n"
    "v_mfma_f32_16x16x32_bf16 v[100:103], a[80:83], a[0:3], 0\n"
    "v_mfma_f32_16x16x32_bf16 v[96:99], a[68:71], a[4:7], v[96:99]\n"
    "v_mfma_f32_16x16x32_bf16 v[100:103], a[84:87], a[4:7], v[100:103]\n"
    "v_mfma_f32_16x16x32_bf16 v[96:99], a[72:75], a[8:11], v[96:99]\n"
    "v_mfma_f32_16x16x32_bf16 v[100:103], a[88:91], a[8:11], v[100:103]\n"
    "v_mfma_f32_16x16x32_bf16 v[96:99], a[76:79], a[12:15], v[96:99]\n"
    "v_mfma_f32_16x16x32_bf16 v[100:103], a[92:95], a[12:15], v[100:103]\n"
    "s_nop 7\n"
    "s_nop 0\n"
    "v_max3_f32 v0, v0, v96, v97\n"
    "v_max3_f32 v0, v0, v98, v99\n"
    "v_max3_f32 v0, v0, v100, v101\n"
    "v_max3_f32 v0, v0, v102, v103\n"
    "v_mfma_f32_16x16x32_bf16 v[104:107], a[64:67], a[16:19], 0\n"
    "v_mfma_f32_16x16x32_bf16 v[108:111], a[80:83], a[16:19], 0\n"
    "v_mfma_f32_16x16x32_bf16 v[104:107], a[68:71], a[20:23], v[104:107]\n"
    "v_mfma_f32_16x16x32_bf16 v[108:111], a[84:87], a[20:23], v[108:111]\n"
    "v_mfma_f32_16x16x32_bf16 v[104:107], a[72:75], a[24:27], v[104:107]\n"
    "v_mfma_f32_16x16x32_bf16 v[108:111], a[88:91], a[24:27], v[108:111]\n"
    "v_mfma_f32_16x16x32_bf16 v[104:107], a[76:79], a[28:31], v[104:107]\n"
    "v_mfma_f32_16x16x32_bf16 v[108:111], a[92:95], a[28:31], v[108:111]\n"
    "s_nop 7\n"
    "s_nop 0\n"
    "v_max3_f32 v1, v1, v104, v105\n"
    "v_max3_f32 v1, v1, v106, v107\n"
    "v_max3_f32 v1, v1, v108, v109\n"
    "v_max3_f32 v1, v1, v110, v111\n"
    "v_mfma_f32_16x16x32_bf16 v[112:115], a[64:67], a[32:35], 0\n"
    "v_mfma_f32_16x16x32_bf16 v[116:119], a[80:83], a[32:35], 0\n"
    "v_mfma_f32_16x16x32_bf16 v[112:115], a[68:71], a[36:39], v[112:115]\n"
    "v_mfma_f32_16x16x32_bf16 v[116:119], a[84:87], a[36:39], v[116:119]\n"
    "v_mfma_f32_16x16x32_bf16 v[112:115], a[72:75], a[40:43], v[112:115]\n"
    "v_mfma_f32_16x16x32_bf16 v[116:119], a[88:91], a[40:43], v[116:119]\n"
    "v_mfma_f32_16x16x32_bf16 v[112:115], a[76:79], a[44:47], v[112:115]\n"
    "v_mfma_f32_16x16x32_bf16 v[116:119], a[92:95], a[44:47], v[116:119]\n"
    "s_nop 7\n"
    "s_nop 0\n"
    "v_max3_f32 v2, v2, v112, v113\n"
    "v_max3_f32 v2, v2, v114, v115\n"
    "v_max3_f32 v2, v2, v116, v117\n"
    "v_max3_f32 v2, v2, v118, v119\n"
    "v_mfma_f32_16x16x32_bf16 v[120:123], a[96:99], a[0:3], 0\n"
    "v_mfma_f32_16x16x32_bf16 v[124:127], a[112:115], a[0:3], 0\n"
    "v_mfma_f32_16x16x32_bf16 v[120:123], a[100:103], a[4:7], v[120:123]\n"
    "v_mfma_f32_16x16x32_bf16 v[124:127], a[116:119], a[4:7], v[124:127]\n"
    "v_mfma_f32_16x16x32_bf16 v[120:123], a[104:107], a[8:11], v[120:123]\n"
    "v_mfma_f32_16x16x32_bf16 v[124:127], a[120:123], a[8:11], v[124:127]\n"
    "v_mfma_f32_16x16x32_bf16 v[120:123], a[108:111], a[12:15], v[120:123]\n"
    "v_mfma_f32_16x16x32_bf16 v[124:127], a[124:127], a[12:15], v[124:127]\n"
    "s_nop 7\n"
    "s_nop 0\n"
    "v_max3_f32 v0, v0, v120, v121\n"
    "v_max3_f32 v0, v0, v122, v123\n"
    "v_max3_f32 v0, v0, v124, v125\n"
    "v_max3_f32 v0, v0, v126, v127\n"
    "v_mfma_f32_16x16x32_bf16 v[96:99], a[96:99], a[16:19], 0\n"
    "v_mfma_f32_16x16x32_bf16 v[100:103], a[112:115], a[16:19], 0\n"
    "v_mfma_f32_16x16x32_bf16 v[96:99], a[100:103], a[20:23], v[96:99]\n"
    "v_mfma_f32_16x16x32_bf16 v[100:103], a[116:119], a[20:23], v[100:103]\n"
    "v_mfma_f32_16x16x32_bf16 v[96:99], a[104:107], a[24:27], v[96:99]\n"
    "v_mfma_f32_16x16x32_bf16 v[100:103], a[120:123], a[24:27], v[100:103]\n"
    "v_mfma_f32_16x16x32_bf16 v[96:99], a[108:111], a[28:31], v[96:99]\n"
    "v_mfma_f32_16x16x32_bf16 v[100:103], a[124:127], a[28:31], v[100:103]\n"
    "s_nop 7\n"
    "s_nop 0\n"
    "v_max3_f32 v1, v1, v96, v97\n"
    "v_max3_f32 v1, v1, v98, v99\n"
    "v_max3_f32 v1, v1, v100, v101\n"
    "v_max3_f32 v1, v1, v102, v103\n"
    "s_add_u32 s64, s64, 0x4000\n"
    "s_add_u32 s71, s71, 1\n"
    "s_cmp_lt_u32 s71, 16\n"
    "s_cbranch_scc1 L_fbloop\n"
    "v_mov_b32_e32 v3, v0\n"
    "v_mov_b32_e64 v4, v1\n"
    "v_mov_b32_e64 v5, v2\n"
    "v_permlane32_swap_b32 v0, v3\n"
    "v_permlane32_swap_b32 v1, v4\n"
    "v_permlane32_swap_b32 v2, v5\n"
    "v_max_f32_e32 v0, v0, v3\n"
    "v_max_f32_e64 v1, v1, v4\n"
    "v_max_f32_e64 v2, v2, v5\n"
    "v_mov_b32_e64 v3, v0\n"
    "v_mov_b32_e64 v4, v1\n"
    "v_mov_b32_e64 v5, v2\n"
    "v_permlane16_swap_b32 v0, v3\n"
    "v_permlane16_swap_b32 v1, v4\n"
    "v_permlane16_swap_b32 v2, v5\n"
    "v_max_f32_e32 v0, v0, v3\n"
    "v_max_f32_e64 v1, v1, v4\n"
    "v_max_f32_e64 v2, v2, v5\n"
    "v_sub_f32_e64 v128, 0, v0\n"
    "v_sub_f32_e64 v129, 0, v0\n"
    "v_sub_f32_e64 v130, 0, v0\n"
    "v_sub_f32_e64 v131, 0, v0\n"
    "v_sub_f32_e64 v132, 0, v1\n"
    "v_sub_f32_e64 v133, 0, v1\n"
    "v_sub_f32_e64 v134, 0, v1\n"
    "v_sub_f32_e64 v135, 0, v1\n"
    "v_sub_f32_e64 v136, 0, v2\n"
    "v_sub_f32_e64 v137, 0, v2\n"
    "v_sub_f32_e64 v138, 0, v2\n"
    "v_sub_f32_e64 v139, 0, v2\n"
    "v_mov_b32_e64 v0, 0\n"
    "v_mov_b32_e64 v1, 0\n"
    "v_mov_b32_e64 v2, 0\n"
    "v_mov_b32_e64 v3, 0\n"
    "v_mov_b32_e64 v4, 0\n"
    "v_mov_b32_e64 v5, 0\n"
    "v_mov_b32_e64 v6, 0\n"
    "v_mov_b32_e64 v7, 0\n"
    "v_mov_b32_e64 v8, 0\n"
    "v_mov_b32_e64 v9, 0\n"
    "v_mov_b32_e64 v10, 0\n"
    "v_mov_b32_e64 v11, 0\n"
    "v_mov_b32_e64 v12, 0\n"
    "v_mov_b32_e64 v13, 0\n"
    "v_mov_b32_e64 v14, 0\n"
    "v_mov_b32_e64 v15, 0\n"
    "v_mov_b32_e64 v16, 0\n"
    "v_mov_b32_e64 v17, 0\n"
    "v_mov_b32_e64 v18, 0\n"
    "v_mov_b32_e64 v19, 0\n"
    "v_mov_b32_e64 v20, 0\n"
    "v_mov_b32_e64 v21, 0\n"
    "v_mov_b32_e64 v22, 0\n"
    "v_mov_b32_e64 v23, 0\n"
    "v_mov_b32_e64 v24, 0\n"
    "v_mov_b32_e64 v25, 0\n"
    "v_mov_b32_e64 v26, 0\n"
    "v_mov_b32_e64 v27, 0\n"
    "v_mov_b32_e64 v28, 0\n"
    "v_mov_b32_e64 v29, 0\n"
    "v_mov_b32_e64 v30, 0\n"
    "v_mov_b32_e64 v31, 0\n"
    "v_mov_b32_e64 v32, 0\n"
    "v_mov_b32_e64 v33, 0\n"
    "v_mov_b32_e64 v34, 0\n"
    "v_mov_b32_e64 v35, 0\n"
    "v_mov_b32_e64 v36, 0\n"
    "v_mov_b32_e64 v37, 0\n"
    "v_mov_b32_e64 v38, 0\n"
    "v_mov_b32_e64 v39, 0\n"
    "v_mov_b32_e64 v40, 0\n"
    "v_mov_b32_e64 v41, 0\n"
    "v_mov_b32_e64 v42, 0\n"
    "v_mov_b32_e64 v43, 0\n"
    "v_mov_b32_e64 v44, 0\n"
    "v_mov_b32_e64 v45, 0\n"
    "v_mov_b32_e64 v46, 0\n"
    "v_mov_b32_e64 v47, 0\n"
    "v_mov_b32_e64 v48, 0\n"
    "v_mov_b32_e64 v49, 0\n"
    "v_mov_b32_e64 v50, 0\n"
    "v_mov_b32_e64 v51, 0\n"
    "v_mov_b32_e64 v52, 0\n"
    "v_mov_b32_e64 v53, 0\n"
    "v_mov_b32_e64 v54, 0\n"
    "v_mov_b32_e64 v55, 0\n"
    "v_mov_b32_e64 v56, 0\n"
    "v_mov_b32_e64 v57, 0\n"
    "v_mov_b32_e64 v58, 0\n"
    "v_mov_b32_e64 v59, 0\n"
    "v_mov_b32_e64 v60, 0\n"
    "v_mov_b32_e64 v61, 0\n"
    "v_mov_b32_e64 v62, 0\n"
    "v_mov_b32_e64 v63, 0\n"
    "v_mov_b32_e64 v64, 0\n"
    "v_mov_b32_e64 v65, 0\n"
    "v_mov_b32_e64 v66, 0\n"
    "v_mov_b32_e64 v67, 0\n"
    "v_mov_b32_e64 v68, 0\n"
    "v_mov_b32_e64 v69, 0\n"
    "v_mov_b32_e64 v70, 0\n"
    "v_mov_b32_e64 v71, 0\n"
    "v_mov_b32_e64 v72, 0\n"
    "v_mov_b32_e64 v73, 0\n"
    "v_mov_b32_e64 v74, 0\n"
    "v_mov_b32_e64 v75, 0\n"
    "v_mov_b32_e64 v76, 0\n"
    "v_mov_b32_e64 v77, 0\n"
    "v_mov_b32_e64 v78, 0\n"
    "v_mov_b32_e64 v79, 0\n"
    "v_mov_b32_e64 v80, 0\n"
    "v_mov_b32_e64 v81, 0\n"
    "v_mov_b32_e64 v82, 0\n"
    "v_mov_b32_e64 v83, 0\n"
    "v_mov_b32_e64 v84, 0\n"
    "v_mov_b32_e64 v85, 0\n"
    "v_mov_b32_e64 v86, 0\n"
    "v_mov_b32_e64 v87, 0\n"
    "v_mov_b32_e64 v88, 0\n"
    "v_mov_b32_e64 v89, 0\n"
    "v_mov_b32_e64 v90, 0\n"
    "v_mov_b32_e64 v91, 0\n"
    "v_mov_b32_e64 v92, 0\n"
    "v_mov_b32_e64 v93, 0\n"
    "v_mov_b32_e64 v94, 0\n"
    "v_mov_b32_e64 v95, 0\n"
    "v_accvgpr_write_b32 a48, 0\n"
    "v_accvgpr_write_b32 a49, 0\n"
    "v_accvgpr_write_b32 a50, 0\n"
    "v_accvgpr_write_b32 a51, 0\n"
    "v_accvgpr_write_b32 a52, 0\n"
    "v_accvgpr_write_b32 a53, 0\n"
    "v_accvgpr_write_b32 a54, 0\n"
    "v_accvgpr_write_b32 a55, 0\n"
    "v_accvgpr_write_b32 a56, 0\n"
    "v_accvgpr_write_b32 a57, 0\n"
    "v_accvgpr_write_b32 a58, 0\n"
    "v_accvgpr_write_b32 a59, 0\n"
    "s_add_u32 m0, s63, 0\n"
    "s_add_u32 s68, s63, 0\n"
    "buffer_load_dwordx4 v152, s[36:39], s68 offen nt lds\n"
    "buffer_load_dwordx4 v153, s[36:39], s68 offen offset:1024 nt lds\n"
    "buffer_load_dwordx4 v152, s[36:39], s68 offen offset:2048 nt lds\n"
    "buffer_load_dwordx4 v153, s[36:39], s68 offen offset:3072 nt lds\n"
    "s_add_u32 m0, s63, 0x10000\n"
    "s_add_u32 s68, s63, 0\n"
    "s_nop 0\n"
    "buffer_load_dwordx4 v152, s[40:43], s68 offen nt lds\n"
    "buffer_load_dwordx4 v153, s[40:43], s68 offen offset:1024 nt lds\n"
    "buffer_load_dwordx4 v152, s[40:43], s68 offen offset:2048 nt lds\n"
    "buffer_load_dwordx4 v153, s[40:43], s68 offen offset:3072 nt lds\n"
    "s_add_u32 m0, s63, 0x4000\n"
    "s_add_u32 s68, s63, 0x4000\n"
    "buffer_load_dwordx4 v152, s[36:39], s68 offen nt lds\n"
    "buffer_load_dwordx4 v153, s[36:39], s68 offen offset:1024 nt lds\n"
    "buffer_load_dwordx4 v152, s[36:39], s68 offen offset:2048 nt lds\n"
    "buffer_load_dwordx4 v153, s[36:39], s68 offen offset:3072 nt lds\n"
    "s_add_u32 m0, s63, 0x14000\n"
    "s_add_u32 s68, s63, 0x4000\n"
    "buffer_load_dwordx4 v152, s[40:43], s68 offen nt lds\n"
    "buffer_load_dwordx4 v153, s[40:43], s68 offen offset:1024 nt lds\n"
    "buffer_load_dwordx4 v152, s[40:43], s68 offen offset:2048 nt lds\n"
    "buffer_load_dwordx4 v153, s[40:43], s68 offen offset:3072 nt lds\n"
    "s_add_u32 m0, s63, 0x8000\n"
    "s_add_u32 s68, s63, 0x8000\n"
    "buffer_load_dwordx4 v152, s[36:39], s68 offen nt lds\n"
    "buffer_load_dwordx4 v153, s[36:39], s68 offen offset:1024 nt lds\n"
    "buffer_load_dwordx4 v152, s[36:39], s68 offen offset:2048 nt lds\n"
    "buffer_load_dwordx4 v153, s[36:39], s68 offen offset:3072 nt lds\n"
    "s_waitcnt vmcnt(0)\n"
    "s_barrier\n"
    "ds_read_b128 a[64:67], v140\n"
    "ds_read_b128 a[80:83], v140 offset:4096\n"
    "ds_read_b128 a[68:71], v141\n"
    "ds_read_b128 a[84:87], v141 offset:4096\n"
    "ds_read_b128 a[72:75], v140 offset:1024\n"
    "ds_read_b128 a[88:91], v140 offset:5120\n"
    "ds_read_b128 a[76:79], v141 offset:1024\n"
    "ds_read_b128 a[92:95], v141 offset:5120\n"
    "ds_read_b128 a[96:99], v142\n"
    "ds_read_b128 a[112:115], v142 offset:4096\n"
    "ds_read_b128 a[100:103], v143\n"
    "ds_read_b128 a[116:119], v143 offset:4096\n"
    "s_waitcnt lgkmcnt(0)\n"
    "s_branch L_att\n"
    "L_end:\n"
    :
    : "s"(xb), "s"(kb), "s"(vb), "s"(wqb), "s"(ws), "s"(wave), "s"(rot), "s"(ob), "s"(wg), "s"((unsigned)(size_t)lds),
      "s"(wob), "s"(outb), "s"(ep)
    : "v0", "v1", "v2", "v3", "v4", "v5", "v6", "v7", "v8", "v9", "v10", "v11", "v12", "v13", "v14", "v15", "v16", "v17", "v18", "v19", "v20", "v21", "v22", "v23",
      "v24", "v25", "v26", "v27", "v28", "v29", "v30", "v31", "v32", "v33", "v34", "v35", "v36", "v37", "v38", "v39", "v40", "v41", "v42", "v43", "v44", "v45", "v46", "v47",
      "v48", "v49", "v50", "v51", "v52", "v53", "v54", "v55", "v56", "v57", "v58", "v59", "v60", "v61", "v62", "v63", "v64", "v65", "v66", "v67", "v68", "v69", "v70", "v71",
      "v72", "v73", "v74", "v75", "v76", "v77", "v78", "v79", "v80", "v81", "v82", "v83", "v84", "v85", "v86", "v87", "v88", "v89", "v90", "v91", "v92", "v93", "v94", "v95",
      "v96", "v97", "v98", "v99", "v100", "v101", "v102", "v103", "v104", "v105", "v106", "v107", "v108", "v109", "v110", "v111", "v112", "v113", "v114", "v115", "v116", "v117", "v118", "v119",
      "v120", "v121", "v122", "v123", "v124", "v125", "v126", "v127", "v128", "v129", "v130", "v131", "v132", "v133", "v134", "v135", "v136", "v137", "v138", "v139", "v140", "v141", "v142", "v143",
      "v144", "v145", "v146", "v147", "v148", "v149", "v150", "v151", "v152", "v153", "v154", "v155", "v156", "v157", "v158", "v159", "v160", "v161", "v162", "v163", "v164", "v165", "v166", "v167",
      "v168", "v169", "v170", "v171", "v172", "v173", "v174", "v175", "v176", "v177", "v178", "v179", "v180", "v181", "v182", "v183", "v184", "v185", "v186", "v187", "v188", "v189", "v190", "v191",
      "v192", "v193", "v194", "v195", "v196", "v197", "v198", "v199", "v200", "v201", "v202", "v203", "v204", "v205", "v206", "v207", "v208", "v209", "v210", "v211", "v212", "v213", "v214", "v215",
      "v216", "v217", "v218", "v219", "v220", "v221", "v222", "v223", "v224", "v225", "v226", "v227", "v228", "v229", "v230", "v231", "v232", "v233", "v234", "v235", "v236", "v237", "v238", "v239",
      "v240", "v241", "v242", "v243", "v244", "v245", "v246", "v247", "v248", "v249", "v250", "v251", "v252", "v253", "v254", "v255", "a0", "a1", "a2", "a3", "a4", "a5", "a6", "a7",
      "a8", "a9", "a10", "a11", "a12", "a13", "a14", "a15", "a16", "a17", "a18", "a19", "a20", "a21", "a22", "a23", "a24", "a25", "a26", "a27", "a28", "a29", "a30", "a31",
      "a32", "a33", "a34", "a35", "a36", "a37", "a38", "a39", "a40", "a41", "a42", "a43", "a44", "a45", "a46", "a47", "a48", "a49", "a50", "a51", "a52", "a53", "a54", "a55",
      "a56", "a57", "a58", "a59", "a60", "a61", "a62", "a63", "a64", "a65", "a66", "a67", "a68", "a69", "a70", "a71", "a72", "a73", "a74", "a75", "a76", "a77", "a78", "a79",
      "a80", "a81", "a82", "a83", "a84", "a85", "a86", "a87", "a88", "a89", "a90", "a91", "a92", "a93", "a94", "a95", "a96", "a97", "a98", "a99", "a100", "a101", "a102", "a103",
      "a104", "a105", "a106", "a107", "a108", "a109", "a110", "a111", "a112", "a113", "a114", "a115", "a116", "a117", "a118", "a119", "a120", "a121", "a122", "a123", "a124", "a125", "a126", "a127",
      "a128", "a129", "a130", "a131", "a132", "a133", "a134", "a135", "a136", "a137", "a138", "a139", "a140", "a141", "a142", "a143", "a144", "a145", "a146", "a147", "a148", "a149", "a150", "a151",
      "a152", "a153", "a154", "a155", "a156", "a157", "a158", "a159", "a160", "a161", "a162", "a163", "a164", "a165", "a166", "a167", "a168", "a169", "a170", "a171", "a172", "a173", "a174", "a175",
      "a176", "a177", "a178", "a179", "a180", "a181", "a182", "a183", "a184", "a185", "a186", "a187", "a188", "a189", "a190", "a191", "a192", "a193", "a194", "a195", "a196", "a197", "a198", "a199",
      "a200", "a201", "a202", "a203", "a204", "a205", "a206", "a207", "a208", "a209", "a210", "a211", "a212", "a213", "a214", "a215", "a216", "a217", "a218", "a219", "a220", "a221", "a222", "a223",
      "a224", "a225", "a226", "a227", "a228", "a229", "a230", "a231", "a232", "a233", "a234", "a235", "a236", "a237", "a238", "a239", "a240", "a241", "a242", "a243", "a244", "a245", "a246", "a247",
      "a248", "a249", "a250", "a251", "a252", "a253", "a254", "a255", "s36", "s37", "s38", "s39", "s40", "s41", "s42", "s43", "s44", "s45", "s46", "s47", "s48", "s49", "s50", "s51",
      "s52", "s53", "s54", "s55", "s56", "s57", "s58", "s59", "s60", "s61", "s62", "s63", "s64", "s65", "s66", "s67", "s68", "s69", "s70", "s71", "s72", "s73", "s74", "s75",
      "s76", "s77", "s78", "s79", "s80", "s81", "s82", "s83", "s84", "s85", "s86", "s87", "s88", "s89", "s90", "s91", "s92", "s93", "s94", "s95", "s96", "s97", "s98", "s99",
      "vcc", "scc", "memory");
}

extern "C" void kernel_launch(void* const* d_in, const int* in_sizes, int n_in, void* d_out, int out_size, void* d_ws, size_t ws_size, hipStream_t stream) {
  gqa_attn<<<256, 256, 0, stream>>>((const char*)d_in[0], (const char*)d_in[1], (const char*)d_in[2], (const char*)d_in[3], (const char*)d_in[4], (char*)d_out, (char*)d_ws);
}
```
